```python
import torch

def _softmax(x, axis=-1):
    return torch.softmax(x, dim=axis)

def _gelu(x):
    return torch.nn.functional.gelu(x, approximate='tanh')

def _silu(x):
    return torch.nn.functional.silu(x)

def _sigmoid(x):
    return torch.sigmoid(x)

def _layernorm(x, axis=-1, eps=1e-05):
    m = x.mean(dim=axis, keepdim=True)
    v = x.var(dim=axis, keepdim=True, unbiased=False)
    return (x - m) / torch.sqrt(v + eps)

def _scatter_add(tgt, idx, src, axis=0):
    return tgt.clone().index_add_(axis, idx, src)

def _segment_sum(src, seg, n):
    out = torch.zeros((n,) + src.shape[1:], dtype=src.dtype)
    return out.index_add_(0, seg, src)

def _pad(x, pw):
    flat = tuple((v for pair in reversed(pw) for v in pair))
    return torch.nn.functional.pad(x, flat)
kernel_name = 'sequential_preLN__mha_block__mixer_gmlp__prompt'

def setup_inputs():

    _w = lambda a: (lambda t: t.to(torch.bfloat16) if t.is_floating_point() else t)(torch.as_tensor(a))

    def _A_setup():
        B, S, D, H = (1, 2048, 576, 32)
        return {'x': _w((lambda *s: torch.randn(*s, dtype=torch.bfloat16) * 0.05)(B, S, D)), 'wqkv': _w((lambda *s: torch.randn(*s, dtype=torch.bfloat16) * 0.05)(D, 3 * D)), 'wo': _w((lambda *s: torch.randn(*s, dtype=torch.bfloat16) * 0.05)(D, D))}
    _A_in = _A_setup()

    def _B_setup():
        B, S, D, F = (1, 2048, 576, 4608)
        Fh = F // 2
        return {'x': _w((lambda *s: torch.randn(*s, dtype=torch.bfloat16) * 0.05)(B, S, D)), 'w_in': _w((lambda *s: torch.randn(*s, dtype=torch.bfloat16) * 0.8)(D, 2 * Fh)), 'w_sp': _w(0.01 * (lambda *s: torch.randn(*s, dtype=torch.bfloat16) * 0.8)(S, S)), 'b_sp': _w(1.0 + 0.01 * (lambda *s: torch.randn(*s, dtype=torch.bfloat16) * 0.05)(S)), 'w_out': _w((lambda *s: torch.randn(*s, dtype=torch.bfloat16) * 0.05)(Fh, D))}
    _B_in = _B_setup()
    return {'A_x': _A_in['x'], 'A_wqkv': _A_in['wqkv'], 'A_wo': _A_in['wo'], 'B_x': _B_in['x'], 'B_w_in': _B_in['w_in'], 'B_w_sp': _B_in['w_sp'], 'B_b_sp': _B_in['b_sp'], 'B_w_out': _B_in['w_out']}

def reference(A_x, A_wqkv, A_wo, B_x, B_w_in, B_w_sp, B_b_sp, B_w_out):

    def _A_ref(x, wqkv, wo):
        B, S, D = x.shape
        H = 32
        Dh = D // H
        qkv = (lambda x, *d: x.permute(*d))((x @ wqkv).reshape(B, S, 3, H, Dh), 2, 0, 3, 1, 4)
        q, k, v = (qkv[0], qkv[1], qkv[2])
        scores = torch.einsum('bhqd,bhkd->bhqk', q, k) * Dh ** (-0.5)
        p = _softmax(scores, axis=-1)
        o = torch.einsum('bhqk,bhkd->bhqd', p, v)
        return (lambda x, *d: x.permute(*d))(o, 0, 2, 1, 3).reshape(B, S, D) @ wo

    def _B_ref(x, w_in, w_sp, b_sp, w_out):
        B, S, D = x.shape
        F = w_in.shape[-1]
        Fh = F // 2
        z = _gelu(torch.einsum('bsd,df->bsf', x, w_in))
        z1 = z[..., :Fh].clone()
        z2 = _layernorm(z[..., Fh:], axis=-1)
        g = torch.einsum('ts,bsf->btf', w_sp, z2) + b_sp[None, :, None]
        return torch.einsum('bsf,fd->bsd', z1 * g, w_out)
    x = A_x
    h = x + _A_ref(_layernorm(x, axis=-1), A_wqkv, A_wo)
    h2 = h + _B_ref(_layernorm(h, axis=-1), B_w_in, B_w_sp, B_b_sp, B_w_out)
    return h2
```

```cpp
#include <hip/hip_runtime.h>
#include <hip/hip_bf16.h>
#include <stdint.h>
typedef unsigned short u16;
using bf16x8 = __attribute__((ext_vector_type(8))) short;
using f32x4 = __attribute__((ext_vector_type(4))) float;
constexpr int S = 2048, D = 576, H = 32, DH = 18, F = 4608, FH = 2304;
__device__ __forceinline__ float bf2f(u16 x) { return __uint_as_float((unsigned)x << 16); }
__device__ __forceinline__ u16 f2bf(float f) { unsigned u = __float_as_uint(f); u += 0x7fffu + ((u >> 16) & 1u); return (u16)(u >> 16); }

__global__ void ln_kernel(const u16* __restrict__ x, int ldi, u16* __restrict__ y, int ldo, int N) {
  int row = blockIdx.x; const u16* xr = x + (size_t)row * ldi; u16* yr = y + (size_t)row * ldo;
  __shared__ float red[8];
  float s = 0; for (int i = threadIdx.x; i < N; i += 256) s += bf2f(xr[i]);
  for (int o = 32; o; o >>= 1) s += __shfl_xor(s, o);
  if ((threadIdx.x & 63) == 0) red[threadIdx.x >> 6] = s; __syncthreads();
  float m = (red[0] + red[1] + red[2] + red[3]) / N; __syncthreads();
  float v = 0; for (int i = threadIdx.x; i < N; i += 256) { float t = bf2f(xr[i]) - m; v += t * t; }
  for (int o = 32; o; o >>= 1) v += __shfl_xor(v, o);
  if ((threadIdx.x & 63) == 0) red[threadIdx.x >> 6] = v; __syncthreads();
  float r = rsqrtf((red[0] + red[1] + red[2] + red[3]) / N + 1e-5f);
  for (int i = threadIdx.x; i < N; i += 256) yr[i] = f2bf((bf2f(xr[i]) - m) * r);
}
template <int EPI>
__global__ void gemm(const u16* __restrict__ A, int lda, const u16* __restrict__ B, int ldb, u16* __restrict__ C, int ldc, int K,
                     const u16* __restrict__ aux, int ldaux, const u16* __restrict__ bias) {
  __shared__ __attribute__((aligned(16))) u16 As[64][40];
  __shared__ __attribute__((aligned(16))) u16 Bs[64][40];
  int tid = threadIdx.x, lane = tid & 63, w = tid >> 6, wr = w >> 1, wc = w & 1;
  int m0 = blockIdx.y * 64, n0 = blockIdx.x * 64;
  f32x4 acc[2][2] = {};
  for (int k0 = 0; k0 < K; k0 += 32) {
    { int r = tid >> 2, c = (tid & 3) * 8; *(bf16x8*)&As[r][c] = *(const bf16x8*)(A + (size_t)(m0 + r) * lda + k0 + c); }
    { int r = tid >> 3, c = (tid & 7) * 8; bf16x8 v = *(const bf16x8*)(B + (size_t)(k0 + r) * ldb + n0 + c);
      for (int j = 0; j < 8; ++j) Bs[c + j][r] = v[j]; }
    __syncthreads();
    bf16x8 a[2], b[2];
    for (int i = 0; i < 2; ++i) a[i] = *(bf16x8*)&As[wr * 32 + i * 16 + (lane & 15)][(lane >> 4) * 8];
    for (int i = 0; i < 2; ++i) b[i] = *(bf16x8*)&Bs[wc * 32 + i * 16 + (lane & 15)][(lane >> 4) * 8];
    for (int i = 0; i < 2; ++i) for (int j = 0; j < 2; ++j) acc[i][j] = __builtin_amdgcn_mfma_f32_16x16x32_bf16(a[i], b[j], acc[i][j], 0, 0, 0);
    __syncthreads();
  }
  for (int i = 0; i < 2; ++i) for (int j = 0; j < 2; ++j) for (int r = 0; r < 4; ++r) {
    int row = m0 + wr * 32 + i * 16 + (lane >> 4) * 4 + r, col = n0 + wc * 32 + j * 16 + (lane & 15);
    float v = acc[i][j][r];
    if (EPI == 1) v = bf2f(f2bf(v)) + bf2f(aux[(size_t)row * ldaux + col]);
    if (EPI == 2) { v = bf2f(f2bf(v)); float t = 0.7978845608f * (v + 0.044715f * v * v * v); v = 0.5f * v * (1.f + tanhf(t)); }
    if (EPI == 3) { v = (v + bf2f(bias[row])) * bf2f(aux[(size_t)row * ldaux + col]); }
    C[(size_t)row * ldc + col] = f2bf(v);
  }
}
__global__ void attn_naive(const u16* __restrict__ qkv, u16* __restrict__ o) {
  int h = blockIdx.y, q = blockIdx.x * 256 + threadIdx.x;
  __shared__ float Ks[256][DH], Vs[256][DH];
  float qr[DH], acc[DH]; for (int d = 0; d < DH; ++d) { qr[d] = bf2f(qkv[(size_t)q * 1728 + h * DH + d]) * 0.23570226f; acc[d] = 0; }
  float m = -1e30f, l = 0;
  for (int k0 = 0; k0 < S; k0 += 256) {
    __syncthreads();
    for (int d = 0; d < DH; ++d) { Ks[threadIdx.x][d] = bf2f(qkv[(size_t)(k0 + threadIdx.x) * 1728 + D + h * DH + d]); Vs[threadIdx.x][d] = bf2f(qkv[(size_t)(k0 + threadIdx.x) * 1728 + 2 * D + h * DH + d]); }
    __syncthreads();
    for (int k = 0; k < 256; ++k) {
      float s = 0; for (int d = 0; d < DH; ++d) s += qr[d] * Ks[k][d];
      float mn = fmaxf(m, s), a = __expf(m - mn), p = __expf(s - mn);
      l = l * a + p; for (int d = 0; d < DH; ++d) acc[d] = acc[d] * a + p * Vs[k][d]; m = mn;
    }
  }
  for (int d = 0; d < DH; ++d) o[(size_t)q * D + h * DH + d] = f2bf(acc[d] / l);
}
extern "C" void kernel_launch(void* const* d_in, const int* in_sizes, int n_in, void* d_out, int out_size, void* d_ws, size_t ws_size, hipStream_t stream) {
  const u16* x = (const u16*)d_in[0]; const u16* wqkv = (const u16*)d_in[1]; const u16* wo = (const u16*)d_in[2];
  const u16* w_in = (const u16*)d_in[4]; const u16* w_sp = (const u16*)d_in[5]; const u16* b_sp = (const u16*)d_in[6]; const u16* w_out = (const u16*)d_in[7];
  u16* out = (u16*)d_out; char* ws = (char*)d_ws;
  u16* ln1 = (u16*)(ws); u16* qkv = (u16*)(ws + (4 << 20)); u16* o = (u16*)(ws + (12 << 20)); u16* h = (u16*)(ws + (16 << 20)); u16* ln2 = (u16*)(ws + (20 << 20));
  u16* z = (u16*)(ws + (24 << 20)); u16* z2 = (u16*)(ws + (48 << 20)); u16* u = (u16*)(ws + (60 << 20));
  ln_kernel<<<S, 256, 0, stream>>>(x, D, ln1, D, D);
  gemm<0><<<dim3(1728 / 64, S / 64), 256, 0, stream>>>(ln1, D, wqkv, 1728, qkv, 1728, D, nullptr, 0, nullptr);
  attn_naive<<<dim3(S / 256, H), 256, 0, stream>>>(qkv, o);
  gemm<1><<<dim3(D / 64, S / 64), 256, 0, stream>>>(o, D, wo, D, h, D, D, x, D, nullptr);
  ln_kernel<<<S, 256, 0, stream>>>(h, D, ln2, D, D);
  gemm<2><<<dim3(F / 64, S / 64), 256, 0, stream>>>(ln2, D, w_in, F, z, F, D, nullptr, 0, nullptr);
  ln_kernel<<<S, 256, 0, stream>>>(z + FH, F, z2, FH, FH);
  gemm<3><<<dim3(FH / 64, S / 64), 256, 0, stream>>>(w_sp, S, z2, FH, u, FH, S, z, F, b_sp);
  gemm<1><<<dim3(D / 64, S / 64), 256, 0, stream>>>(u, FH, w_out, D, out, D, FH, h, D, nullptr);
}
```

```cpp
#include <hip/hip_runtime.h>
#include <hip/hip_bf16.h>
#include <stdint.h>
typedef unsigned short u16;
using bf16x8 = __attribute__((ext_vector_type(8))) short;
using s16x4 = __attribute__((ext_vector_type(4))) short;
using f32x4 = __attribute__((ext_vector_type(4))) float;
using f32x16 = __attribute__((ext_vector_type(16))) float;
using u32x4 = __attribute__((ext_vector_type(4))) unsigned;
using u32x2 = __attribute__((ext_vector_type(2))) unsigned;
using u32x32 = __attribute__((ext_vector_type(32))) unsigned;
typedef __bf16 bf16x2_t __attribute__((ext_vector_type(2)));
typedef float f32x2_t __attribute__((ext_vector_type(2)));
constexpr int S = 2048, D = 576, H = 32, DH = 18, F = 4608, FH = 2304;
#define DI __device__ __forceinline__
#define LAS __attribute__((address_space(3)))
#define GAS __attribute__((address_space(1)))
DI float bf2f(u16 x) { return __uint_as_float((unsigned)x << 16); }
DI float bflo(unsigned x) { return __uint_as_float(x << 16); }
DI float bfhi(unsigned x) { return __uint_as_float(x & 0xffff0000u); }
DI unsigned pk2(float lo, float hi) { f32x2_t v = {lo, hi}; return __builtin_bit_cast(unsigned, __builtin_convertvector(v, bf16x2_t)); }
#define MFMA32(a, b, c) __builtin_amdgcn_mfma_f32_32x32x16_bf16((a), (b), (c), 0, 0, 0)
#define MFMA16(a, b, c) __builtin_amdgcn_mfma_f32_16x16x32_bf16((a), (b), (c), 0, 0, 0)
#define SB() __builtin_amdgcn_sched_barrier(0)
#define SGB(mask, n) __builtin_amdgcn_sched_group_barrier(mask, n, 0)
#define PIN(x) asm volatile("" : "+v"(x))
#define BAR() __builtin_amdgcn_s_barrier()
#define WAITV(n) asm volatile("s_waitcnt vmcnt(%0)" :: "i"(n) : "memory")
#define WAITL0() asm volatile("s_waitcnt lgkmcnt(0)" ::: "memory")
DI void glds16(const void* g, void* l) { __builtin_amdgcn_global_load_lds((const GAS unsigned*)g, (LAS unsigned*)l, 16, 0, 0); }
typedef LAS const char* lds_cptr;
typedef LAS char* lds_ptr;
#define LDS_RD128(p) (*(const LAS bf16x8*)(p))
#define LDS_RD32(p) (*(const LAS unsigned*)(p))
typedef short v4i16_t __attribute__((ext_vector_type(4)));
DI s16x4 lds_tr(lds_cptr p) { return __builtin_bit_cast(s16x4, __builtin_amdgcn_ds_read_tr16_b64_v4i16((LAS v4i16_t*)p)); }
DI bf16x8 lds_tr2(lds_cptr p0, lds_cptr p1) { const s16x4 a = lds_tr(p0), b = lds_tr(p1); return __builtin_shufflevector(a, b, 0, 1, 2, 3, 4, 5, 6, 7); }

#define TIMING 0
#define TS(k, i)
#define TSTAMP(v)
#define RSTAMP(v)

constexpr int KV_TILE = 2400, KV_K1 = 1024, KV_V = 1152, KV_VS = 624, KV_Z = KV_V + 608, KV_HEAD = 64 * KV_TILE;
constexpr int Q_TILE = 1152, Q_HEAD = 64 * Q_TILE;
constexpr size_t WS_KV = 0;
constexpr size_t WS_Q = 5u << 20;
constexpr size_t WS_O = 8u << 20;
constexpr size_t WS_H = 11u << 20;
constexpr size_t WS_ST2 = 14u << 20;
constexpr size_t WS_CS = 15u << 20;
constexpr size_t WS_XS = 16u << 20;
constexpr size_t WS_Z2T = 17u << 20;
constexpr size_t WS_Z1 = 27u << 20;
constexpr size_t WS_U = 37u << 20;
constexpr size_t WS_WIN = 47u << 20;
constexpr size_t WS_WO = 53u << 20;
constexpr size_t WS_WOUT = 54u << 20;
constexpr size_t WS_DBG = 64u << 20;

template <int NP, int NW, class SRC>
DI void first_tile(char* lds, int w, SRC src) {
#pragma unroll
  for (int j = 0; j < (NP + NW - 1) / NW; ++j) { const int p = w + NW * j; if (p < NP) glds16(src(p), lds + p * 1024); }
}
template <int NT, int NS, int STG, int NP, int NPX, int NLD, class SRC, class OFF, class POST>
DI void loader_loop(char* lds, int lw, SRC src, OFF off, POST post) {
  constexpr int PPW = (NP + NLD - 1) / NLD, PMIN = NP / NLD;
  static_assert((NS - 1) * PPW + 4 <= 63, "vmcnt");
  static_assert(NPX % NLD == 0, "X pieces");
  const char* base[PPW];
#pragma unroll
  for (int j = 0; j < PPW; ++j) { const int p = lw + NLD * j; base[j] = src(p < NP ? p : NP - 1); }
  auto issue = [&](int kt, int stage) {
    long ox, ow; off(kt, ox, ow);
#pragma unroll
    for (int j = 0; j < PPW; ++j) if (j < PMIN || lw + NLD * j < NP) glds16(base[j] + (j < NPX / NLD ? ox : ow), lds + stage * STG + (lw + NLD * j) * 1024);
  };
  issue(1, 1);
  WAITV(PMIN);
  BAR();
#pragma unroll
  for (int t = 2; t < NS - 1; ++t) issue(t, t);
  post(0);
  int stg = NS - 1, pst = 1;
#pragma unroll 1
  for (int kt = 0; kt < NT - (NS - 1); ++kt) {
    WAITV((NS - 3) * PMIN);
    BAR();
    issue(kt + NS - 1, stg); stg = stg + 1 == NS ? 0 : stg + 1;
    post(pst); pst = pst + 1 == NS ? 0 : pst + 1;
  }
#pragma unroll
  for (int r = NS - 2; r >= 0; --r) {
    WAITV((r - 1 > 0 ? r - 1 : 0) * PMIN); BAR();
    if (r > 0) { post(pst); pst = pst + 1 == NS ? 0 : pst + 1; }
  }
}
struct NoPost { DI void operator()(int) const {} };
DI unsigned tn_off(int row, int lane, int ld_bytes) { return (unsigned)row * ld_bytes + ((((lane & 7) ^ ((row >> 1) & 7))) << 4); }
template <int BN> DI unsigned kn_off(int p, int lane, int ld_bytes) {
  constexpr int CPR = BN / 8;
  const int c = 64 * p + lane, kp = c / CPR, cc = c - kp * CPR, odd = (kp >> 3) & 1, k = kp ^ (odd << 2);
  int cs = cc;
  if (BN == 96) { int blk = (cc >> 1) - odd; if (blk < 0) blk += 6; cs = blk * 2 + (cc & 1); }
  return (unsigned)k * ld_bytes + (unsigned)cs * 16;
}

DI float gelu_t(float x) {
  const float c1 = -2.f * 0.7978845608028654f * 1.4426950408889634f, c2 = c1 * 0.044715f;
  const float e = __builtin_amdgcn_exp2f(x * __builtin_fmaf(x * x, c2, c1));
  return x * __builtin_amdgcn_rcpf(1.f + e);
}

constexpr int C8_STG = 272 * 128, C8_W = 16384, C8_NS = 4, C8_LDS = C8_NS * C8_STG;
struct C8Addr { lds_cptr x0, x1, w0, w1; };
template <bool BKN> DI C8Addr c8_addr(char* lds, int lane, int wr, int nb0) {
  lds_cptr L = (lds_cptr)lds; const int fr = lane & 15, fq = lane >> 4, sw = (fr >> 1) & 7; C8Addr a;
  a.x0 = L + (32 * wr + fr) * 128 + ((fq ^ sw) << 4); a.x1 = L + (32 * wr + fr) * 128 + (((4 + fq) ^ sw) << 4);
  if (BKN) { const int q = fr >> 2, p4 = lane & 3, b = C8_W + (8 * fq + q) * 288 + nb0 * 32 + p4 * 8;
             a.w0 = L + b + (fq & 1) * 1152; a.w1 = L + b + ((fq & 1) ^ 1) * 1152; }
  else { a.w0 = L + C8_W + (16 * nb0 + fr) * 128 + ((fq ^ sw) << 4); a.w1 = L + C8_W + (16 * nb0 + fr) * 128 + (((4 + fq) ^ sw) << 4); }
  return a;
}
template <int NTL> struct C8Frag { bf16x8 x[2], w[NTL]; };
template <int NTL, bool BKN> DI void c8_read(C8Frag<NTL>& f, const C8Addr& a, int st, int ks) {
  f.x[0] = LDS_RD128((ks ? a.x1 : a.x0) + st); f.x[1] = LDS_RD128((ks ? a.x1 : a.x0) + st + 2048);
#pragma unroll
  for (int n = 0; n < NTL; ++n) {
    if (BKN) f.w[n] = lds_tr2(a.w0 + st + ks * 9216 + n * 32, a.w1 + st + ks * 9216 + n * 32);
    else f.w[n] = LDS_RD128((ks ? a.w1 : a.w0) + st + n * 2048);
  }
}
template <int NTL, bool WX> DI void c8_mma(f32x4 (&acc)[NTL][2], const C8Frag<NTL>& f) {
#pragma unroll
  for (int n = 0; n < NTL; ++n) {
#pragma unroll
    for (int m = 0; m < 2; ++m) acc[n][m] = WX ? MFMA16(f.w[n], f.x[m], acc[n][m]) : MFMA16(f.x[m], f.w[n], acc[n][m]);
  }
}
template <int NTL, bool BKN> DI void c8_sched() {
  constexpr int NR = 2 + (BKN ? 2 * NTL : NTL), NM = 2 * NTL;
  if (NR <= NM) {
#pragma unroll
    for (int i = 0; i < NR; ++i) { SGB(0x8, 1); SGB(0x100, 1); }
    SGB(0x8, NM - NR);
  } else {
#pragma unroll
    for (int i = 0; i < NM; ++i) { SGB(0x8, 1); SGB(0x100, 1); if (i < NR - NM) SGB(0x100, 1); }
  }
}

constexpr int K1_THREADS = 1024;
constexpr int EP_PITCH = 304;
#define K1SCHED() do { _Pragma("unroll") for (int i_ = 0; i_ < 7; ++i_) { SGB(0x8, 1); SGB(0x100, 2); } SGB(0x8, 4); } while (0)
template <int NTL, bool WX>
DI void k1_compute(char* lds, int lane, int w, f32x4 (&acc)[NTL][2], f32x4& csa, f32x4& csb, char* ws) {
  const int wr = w & 3, nb0 = NTL == 5 ? 0 : 5;
  const C8Addr a = c8_addr<true>(lds, lane, wr, nb0);
  const C8Addr ac = c8_addr<true>(lds, lane, 0, nb0 + wr), ad = c8_addr<true>(lds, lane, 0, 4);
  const u32x4 onesv = {0x3f803f80u, 0x3f803f80u, 0x3f803f80u, 0x3f803f80u}; const bf16x8 ones = __builtin_bit_cast(bf16x8, onesv);
  C8Frag<NTL> f0, f1; bf16x8 c0, c1, d0, d1;
  WAITV(0); BAR();
  TS(1, 1);
  c8_read<NTL, true>(f0, a, 0, 0); c0 = lds_tr2(ac.w0, ac.w1); if (NTL == 4) d0 = lds_tr2(ad.w0, ad.w1);
  int st = 0;
#pragma unroll 1
  for (int kt = 0; kt < 9; ++kt) {
    const int stn = st + C8_STG == C8_LDS ? 0 : st + C8_STG;
    c8_read<NTL, true>(f1, a, st, 1); c1 = lds_tr2(ac.w0 + st + 9216, ac.w1 + st + 9216); if (NTL == 4) d1 = lds_tr2(ad.w0 + st + 9216, ad.w1 + st + 9216);
    c8_mma<NTL, WX>(acc, f0); csa = MFMA16(ones, c0, csa); if (NTL == 4) csb = MFMA16(ones, d0, csb);
    K1SCHED();
    SB(); BAR(); SB();
    c8_read<NTL, true>(f0, a, stn, 0); c0 = lds_tr2(ac.w0 + stn, ac.w1 + stn); if (NTL == 4) d0 = lds_tr2(ad.w0 + stn, ad.w1 + stn);
    c8_mma<NTL, WX>(acc, f1); csa = MFMA16(ones, c1, csa); if (NTL == 4) csb = MFMA16(ones, d1, csb);
    K1SCHED();
    SB();
    st = stn;
  }
}
template <int NTL>
DI void k1_epilogue(char* lds, int lane, int w, int type, int rt, int hg, const float* tabs, f32x4 (&acc)[NTL][2], char* kvimg) {
  asm volatile("" : "+v"(lane));
  const int wr = w & 3, nb0 = NTL == 5 ? 0 : 5, fr = lane & 15, fq = lane >> 4;
  const float* rs = tabs; const float* mr = tabs + 128; const float* cs = tabs + 256;
  if (type == 2) {
#pragma unroll
    for (int m = 0; m < 2; ++m) {
      const int row = 32 * wr + 16 * m + 4 * fq;
      const f32x4 r4 = *(const f32x4*)(rs + row), m4 = *(const f32x4*)(mr + row);
      char* tb = kvimg + (size_t)(rt * 4 + wr) * KV_TILE + KV_V + m * KV_VS + (fq & 1) * 304 + (fq >> 1) * 8;
#pragma unroll
      for (int n = 0; n < NTL; ++n) {
        const int col = 16 * (nb0 + n) + fr, hh = col / 18, d = col - hh * 18; const float c = cs[col];
        u32x2 o; o[0] = pk2(acc[n][m][0] * r4[0] - m4[0] * c, acc[n][m][1] * r4[1] - m4[1] * c); o[1] = pk2(acc[n][m][2] * r4[2] - m4[2] * c, acc[n][m][3] * r4[3] - m4[3] * c);
        *(u32x2*)(tb + (size_t)(hg * 8 + hh) * KV_HEAD + d * 16) = o;
      }
    }
  } else {
    const float qs = type == 0 ? 0.23570226039551584f * 1.4426950408889634f : 1.f;
#pragma unroll
    for (int m = 0; m < 2; ++m) {
      const int row = 32 * wr + 16 * m + fr; const float r = rs[row] * qs, mm = mr[row] * qs;
#pragma unroll
      for (int n = 0; n < NTL; ++n) {
        const int col = 16 * (nb0 + n) + 4 * fq; const f32x4 c4 = *(const f32x4*)(cs + col);
        u32x2 o; o[0] = pk2(acc[n][m][0] * r - mm * c4[0], acc[n][m][1] * r - mm * c4[1]); o[1] = pk2(acc[n][m][2] * r - mm * c4[2], acc[n][m][3] * r - mm * c4[3]);
        *(u32x2*)(lds + row * EP_PITCH + col * 2) = o;
      }
    }
  }
}
__global__ __launch_bounds__(K1_THREADS, 4) void k1_kernel(const u16* __restrict__ x, const u16* __restrict__ wqkv, char* __restrict__ ws) {
  extern __shared__ __attribute__((aligned(16))) char lds[];
  const int tid = threadIdx.x, lane = tid & 63; const int w = __builtin_amdgcn_readfirstlane(tid >> 6);
  const int b = blockIdx.x;
  TS(1, 0); TS(1, 6);
  if (b >= 192) {
    if (tid < 512) ((u32x4*)(ws + WS_XS))[(b - 192) * 512 + tid] = u32x4{0, 0, 0, 0};
    return;
  }
  const int xc = b & 7, idx = b >> 3;
  const int rt = xc * 2 + (idx & 1), ct = idx >> 1;
  const int type = ct >> 2, hg = ct & 3;
  float* tabs = (float*)(lds + C8_LDS);
  char* kvimg = ws + WS_KV; char* qimg = ws + WS_Q;
  const char* Xb = (const char*)(x + (size_t)rt * 128 * D); const char* Wb = (const char*)(wqkv + ct * 144);
  auto src = [&](int p) { return p < 16 ? Xb + tn_off(p * 8 + (lane >> 3), lane, D * 2) : Wb + kn_off<144>(p - 16, lane, 1728 * 2); };
  first_tile<34, 16>(lds, w, src);
  if (w >= 8) {
    const int lw = w - 8;
    __builtin_amdgcn_s_setprio(2);
    float sum = 0.f, ssq = 0.f;
    const int row = 16 * lw + (lane >> 2), sw = (row >> 1) & 7;
    lds_cptr xr = (lds_cptr)lds + row * 128;
    loader_loop<9, C8_NS, C8_STG, 34, 16, 8>(lds, lw, src, [&](int kt, long& ox, long& ow) { ox = (long)kt * 128; ow = (long)kt * 64 * 1728 * 2; },
      [&](int stage) {
        const int st = stage * C8_STG;
#pragma unroll
        for (int c = 0; c < 2; ++c) {
          const u32x4 v = __builtin_bit_cast(u32x4, LDS_RD128(xr + st + (((2 * (lane & 3) + c) ^ sw) << 4)));
#pragma unroll
          for (int e = 0; e < 4; ++e) { const float lo = bflo(v[e]), hi = bfhi(v[e]); sum += lo + hi; ssq = __builtin_fmaf(lo, lo, ssq); ssq = __builtin_fmaf(hi, hi, ssq); }
        }
      });
    sum += __shfl_xor(sum, 1); ssq += __shfl_xor(ssq, 1); sum += __shfl_xor(sum, 2); ssq += __shfl_xor(ssq, 2);
    const float mean = sum * (1.f / D), var = fmaxf(ssq * (1.f / D) - mean * mean, 0.f), rstd = rsqrtf(var + 1e-5f);
    if ((lane & 3) == 0) { tabs[row] = rstd; tabs[128 + row] = mean * rstd; }
  } else {
#define K1_PATH(NTL, WX) do { f32x4 csa = {0, 0, 0, 0}, csb = {0, 0, 0, 0}; f32x4 acc[NTL][2] = {}; \
      k1_compute<NTL, WX>(lds, lane, w, acc, csa, csb, ws); TS(1, 2); \
      if (lane < 16) { tabs[256 + 16 * ((NTL == 5 ? 0 : 5) + (w & 3)) + lane] = csa[0]; if (NTL == 4) tabs[256 + 64 + lane] = csb[0]; } \
      __syncthreads(); \
      k1_epilogue<NTL>(lds, lane, w, WX ? (type & 1) : 2, rt, hg, tabs, acc, kvimg); } while (0)
    if (type == 2) { if (w < 4) K1_PATH(5, false); else K1_PATH(4, false); }
    else { if (w < 4) K1_PATH(5, true); else K1_PATH(4, true); }
  }
  if (w >= 8) __syncthreads();
  if (type == 2) {
    if (tid < 192) {
      const int tl = tid / 48, r = tid - tl * 48, hh = r / 6, c = r - hh * 6;
      char* tb = kvimg + (size_t)(hg * 8 + hh) * KV_HEAD + (size_t)(rt * 4 + tl) * KV_TILE;
      if (c < 4) *(u32x4*)(tb + KV_V + (c >> 1) * KV_VS + (c & 1) * 304 + 18 * 16) = u32x4{0x3f803f80u, 0x3f803f80u, 0x3f803f80u, 0x3f803f80u};
      else *(u32x4*)(tb + KV_Z + (c - 4) * KV_VS) = u32x4{0, 0, 0, 0};
    }
    TS(1, 3); TS(1, 7);
    return;
  }
  __syncthreads();
  for (int it = tid; it < 1024; it += K1_THREADS) {
    const int tok = it & 127, hh = it >> 7;
    const unsigned* sp = (const unsigned*)(lds + tok * EP_PITCH + hh * 36);
    u32x4 c0 = {sp[0], sp[1], sp[2], sp[3]}, c1 = {sp[4], sp[5], sp[6], sp[7]}; const unsigned c2 = sp[8];
    char* tb = type == 0 ? qimg + (size_t)(hg * 8 + hh) * Q_HEAD + (size_t)(rt * 4 + (tok >> 5)) * Q_TILE
                         : kvimg + (size_t)(hg * 8 + hh) * KV_HEAD + (size_t)(rt * 4 + (tok >> 5)) * KV_TILE;
    const int key = tok & 31;
    *(u32x4*)(tb + key * 16) = c0; *(u32x4*)(tb + 512 + key * 16) = c1; *(unsigned*)(tb + 1024 + key * 4) = c2;
  }
  TS(1, 3); TS(1, 7);
}
static_assert(128 * EP_PITCH <= C8_LDS, "");
constexpr int K1_LDS = C8_LDS + 400 * 4;

constexpr int ATT_LDS = KV_HEAD + 8 * Q_TILE;
#define SWITCH 50
__global__ __launch_bounds__(768, 3) void k2_kernel(const u16* __restrict__ wo, const u16* __restrict__ w_in, const u16* __restrict__ w_out, char* __restrict__ ws) {
  extern __shared__ __attribute__((aligned(16))) char lds[];
  TS(2, 0); TS(2, 6);
  const char* kvimg = ws + WS_KV; const char* qimg = ws + WS_Q; u16* o = (u16*)(ws + WS_O);
  const int tid = threadIdx.x, lane = tid & 63; const int w = __builtin_amdgcn_readfirstlane(tid >> 6);
  const int b = blockIdx.x, xcd = b & 7, idx = b >> 3, h = xcd * 4 + (idx >> 3), qb = idx & 7;
  const char* kvg = kvimg + (size_t)h * KV_HEAD + lane * 16;
  if (w < 8) {
    const char* qg = qimg + (size_t)h * Q_HEAD + (qb * 8 + w) * Q_TILE + lane * 16;
    char* qtile = lds + KV_HEAD + w * Q_TILE;
    glds16(qg, qtile);
    if (lane < 8) glds16(qg + 1024, qtile + 1024);
  }
  glds16(kvg + w * 1024, lds + w * 1024); glds16(kvg + (w + 12) * 1024, lds + (w + 12) * 1024);
  WAITV(0);
  BAR();
  if (w >= 8) {
    const int lw = w - 8;
    __builtin_amdgcn_s_setprio(3);
#pragma unroll 1
    for (int p = 24 + lw; p < 150; p += 4) glds16(kvg + p * 1024, lds + p * 1024);
    WAITV(0);
    BAR();
    __builtin_amdgcn_s_setprio(0);
    const int gw = b * 4 + lw, c = lane >> 3, nb = lane & 7;
#pragma unroll 1
    for (int blk = gw; blk < 648 + 81 + 324; blk += 1024) {
      const u16* sp; int ld, kt, n0, kind;
      if (blk < 648) { kt = blk / 72; n0 = (blk - kt * 72) * 64; sp = w_in; ld = F; kind = 0; }
      else if (blk < 729) { const int q = blk - 648; kt = q / 9; n0 = (q - kt * 9) * 64; sp = wo; ld = D; kind = 1; }
      else { const int q = blk - 729; kt = q / 9; n0 = (q - kt * 9) * 64; sp = w_out; ld = D; kind = 2; }
      sp += (size_t)(kt * 64 + c * 8) * ld + n0 + nb * 8;
      u32x4 r[8];
#pragma unroll
      for (int j = 0; j < 8; ++j) r[j] = *(const u32x4*)(sp + (size_t)j * ld);
#pragma unroll
      for (int e = 0; e < 4; ++e) {
#pragma unroll
        for (int hi = 0; hi < 2; ++hi) {
          const unsigned sel = hi ? 0x07060302u : 0x05040100u;
          u32x4 o;
#pragma unroll
          for (int q = 0; q < 4; ++q) o[q] = __builtin_amdgcn_perm(r[2 * q + 1][e], r[2 * q][e], sel);
          const int n = n0 + nb * 8 + 2 * e + hi;
          char* dp;
          if (kind == 0) { const int z1 = n < FH, nn = z1 ? n : n - FH, t = nn / 144, row = nn - t * 144;
            dp = ws + WS_WIN + ((size_t)((z1 ? 16 : 0) + t) * 9 + kt) * 18432 + row * 128 + ((c ^ ((row >> 1) & 7)) << 4); }
          else { const int t = n / 96, row = n - t * 96;
            dp = ws + (kind == 1 ? WS_WO + ((size_t)t * 9 + kt) * 12288 : WS_WOUT + ((size_t)t * 36 + kt) * 12288) + row * 128 + ((c ^ ((row >> 1) & 7)) << 4); }
          *(u32x4*)dp = o;
        }
      }
    }
    return;
  }
  TS(2, 1);
  if (w >= 4) __builtin_amdgcn_s_setprio(1);
  const int r32 = lane & 31, hh = lane >> 5;
  lds_cptr L = (lds_cptr)lds;
  const bf16x8 qf0 = LDS_RD128(L + KV_HEAD + w * Q_TILE + lane * 16);
  u32x4 q1v = {0, 0, 0, 0}; { unsigned t = LDS_RD32(L + KV_HEAD + w * Q_TILE + 1024 + r32 * 4); q1v[0] = hh ? 0u : t; }
  const bf16x8 qf1 = __builtin_bit_cast(bf16x8, q1v);
  lds_cptr kp0 = L + lane * 16, kp1 = L + (hh ? KV_Z : KV_K1 + r32 * 4), vp = L + (r32 <= 18 ? KV_V + hh * 304 + r32 * 16 : KV_Z);
  f32x16 negm;
  {
    bf16x8 k0 = LDS_RD128(kp0); u32x4 k1 = {LDS_RD32(kp1), 0, 0, 0};
    f32x16 s = {}; s = MFMA32(k0, qf0, s); s = MFMA32(__builtin_bit_cast(bf16x8, k1), qf1, s);
    float m = s[0];
#pragma unroll
    for (int i = 1; i < 16; ++i) m = fmaxf(m, s[i]);
    auto rr = __builtin_amdgcn_permlane32_swap(__float_as_uint(m), __float_as_uint(m), false, false);
    m = fmaxf(__uint_as_float(rr[0]), __uint_as_float(rr[1]));
#pragma unroll
    for (int i = 0; i < 16; ++i) negm[i] = -m;
  }
  f32x16 O = {};
#define EX2(Sx, i) pk2(__builtin_amdgcn_exp2f(Sx[i]), __builtin_amdgcn_exp2f(Sx[(i) + 1]))
  bf16x8 k0, v0, v1; u32x4 k1 = {0, 0, 0, 0};
  f32x16 SA, SBc; u32x4 pA0, pA1, pB0, pB1;
#define STEP(Sc, Sn, Pc0, Pc1, Pp0, Pp1, koff, voff) do { \
    SB(); Sn = MFMA32(k0, qf0, negm); SB(); \
    Pc0[0] = EX2(Sc, 0); Pc0[1] = EX2(Sc, 2); PIN(Pc0); SB(); \
    Sn = MFMA32(__builtin_bit_cast(bf16x8, k1), qf1, Sn); SB(); \
    k0 = LDS_RD128(kp0 + (koff)); k1[0] = LDS_RD32(kp1 + (koff)); SB(); \
    Pc0[2] = EX2(Sc, 4); Pc0[3] = EX2(Sc, 6); PIN(Pc0); SB(); \
    O = MFMA32(v0, __builtin_bit_cast(bf16x8, Pp0), O); SB(); \
    Pc1[0] = EX2(Sc, 8); Pc1[1] = EX2(Sc, 10); PIN(Pc1); SB(); \
    O = MFMA32(v1, __builtin_bit_cast(bf16x8, Pp1), O); SB(); \
    v0 = LDS_RD128(vp + (voff)); v1 = LDS_RD128(vp + (voff) + KV_VS); SB(); \
    Pc1[2] = EX2(Sc, 12); Pc1[3] = EX2(Sc, 14); PIN(Pc1); SB(); \
  } while (0)
  k0 = LDS_RD128(kp0); k1[0] = LDS_RD32(kp1);
  SA = MFMA32(k0, qf0, negm); SA = MFMA32(__builtin_bit_cast(bf16x8, k1), qf1, SA);
  k0 = LDS_RD128(kp0 + KV_TILE); k1[0] = LDS_RD32(kp1 + KV_TILE);
  v0 = LDS_RD128(vp); v1 = LDS_RD128(vp + KV_VS);
  pB0 = u32x4{0, 0, 0, 0}; pB1 = u32x4{0, 0, 0, 0};
  int off = 0;
  for (int t = 0; t < 8; t += 2) {
    STEP(SA, SBc, pA0, pA1, pB0, pB1, off + 2 * KV_TILE, off);
    STEP(SBc, SA, pB0, pB1, pA0, pA1, off + 3 * KV_TILE, off + KV_TILE);
    off += 2 * KV_TILE;
  }
  BAR();
  for (int t = 8; t < 62; t += 2) {
    if (t == SWITCH) __builtin_amdgcn_s_setprio(0);
    STEP(SA, SBc, pA0, pA1, pB0, pB1, off + 2 * KV_TILE, off);
    STEP(SBc, SA, pB0, pB1, pA0, pA1, off + 3 * KV_TILE, off + KV_TILE);
    off += 2 * KV_TILE;
  }
  STEP(SA, SBc, pA0, pA1, pB0, pB1, off + KV_TILE, off);
  STEP(SBc, SA, pB0, pB1, pA0, pA1, off + KV_TILE, off + KV_TILE);
  O = MFMA32(v0, __builtin_bit_cast(bf16x8, pB0), O); O = MFMA32(v1, __builtin_bit_cast(bf16x8, pB1), O);
  TS(2, 2);
  {
    float mx = 0.f;
#pragma unroll
    for (int i = 0; i < 11; ++i) mx = fmaxf(mx, fabsf(O[i]));
    bool bad = !(mx < 1.2676506e30f);
#pragma unroll
    for (int i = 0; i < 11; ++i) bad |= (O[i] != O[i]);
    if (__any(bad)) {
      float m = -3.0e38f; O = f32x16{};
#pragma unroll 1
      for (int t = 0; t < 64; ++t) {
        const int of = t * KV_TILE;
        const bf16x8 a0 = LDS_RD128(kp0 + of); const u32x4 a1 = {LDS_RD32(kp1 + of), 0, 0, 0};
        f32x16 s = {}; s = MFMA32(a0, qf0, s); s = MFMA32(__builtin_bit_cast(bf16x8, a1), qf1, s);
        float tm = s[0];
#pragma unroll
        for (int i = 1; i < 16; ++i) tm = fmaxf(tm, s[i]);
        auto rr = __builtin_amdgcn_permlane32_swap(__float_as_uint(tm), __float_as_uint(tm), false, false);
        tm = fmaxf(__uint_as_float(rr[0]), __uint_as_float(rr[1]));
        const float mn = fmaxf(m, tm), al = __builtin_amdgcn_exp2f(m - mn); m = mn;
#pragma unroll
        for (int i = 0; i < 16; ++i) { O[i] *= al; s[i] -= mn; }
        u32x4 p0, p1;
#pragma unroll
        for (int i = 0; i < 4; ++i) { p0[i] = EX2(s, 2 * i); p1[i] = EX2(s, 8 + 2 * i); }
        O = MFMA32(LDS_RD128(vp + of), __builtin_bit_cast(bf16x8, p0), O); O = MFMA32(LDS_RD128(vp + of + KV_VS), __builtin_bit_cast(bf16x8, p1), O);
      }
    }
  }
  {
    float l = O[10];
    { auto rr = __builtin_amdgcn_permlane32_swap(__float_as_uint(l), __float_as_uint(l), false, false); l = __uint_as_float(rr[0]); }
    const float inv = 1.f / l;
    u16* orow = o + (size_t)(qb * 256 + w * 32 + r32) * D + h * DH;
    const unsigned a0 = pk2(O[0] * inv, O[1] * inv), a1_ = pk2(O[2] * inv, O[3] * inv), a2_ = pk2(O[4] * inv, O[5] * inv), a3 = pk2(O[6] * inv, O[7] * inv), a4 = pk2(O[8] * inv, O[9] * inv);
    unsigned* op = (unsigned*)(orow + hh * 4);
    op[0] = a0; op[1] = a1_; op[4] = a2_; op[5] = a3;
    if (hh == 0) op[8] = a4;
  }
  TS(2, 3); TS(2, 7);
}

constexpr int G_STG = 160 * 128, G_W = 8192, G_NS = 6, G_LDS = G_NS * G_STG;
template <int K, bool STATS, int KID>
DI void g_body(char* ws, char* lds, const u16* __restrict__ A, const char* __restrict__ W, const u16* __restrict__ res, u16* __restrict__ out, float* __restrict__ st2, int b) {
  constexpr int NT = K / 64;
  const int tid = threadIdx.x, lane = tid & 63; const int w = __builtin_amdgcn_readfirstlane(tid >> 6);
  const int xc = b & 7, idx = b >> 3, rt = xc * 4 + (idx & 3), ct = idx >> 2;
  const int m0 = rt * 64, n0 = ct * 96;
  TS(KID, 0); TS(KID, 6);
  const char* Xb = (const char*)(A + (size_t)m0 * K); const char* Wb = W + (size_t)ct * NT * 12288;
  auto src = [&](int p) { return p < 8 ? Xb + tn_off(p * 8 + (lane >> 3), lane, K * 2) : Wb + (p - 8) * 1024 + lane * 16; };
  first_tile<20, 12>(lds, w, src);
  if (w >= 4) {
    __builtin_amdgcn_s_setprio(2);
    loader_loop<NT, G_NS, G_STG, 20, 8, 8>(lds, w - 4, src, [&](int kt, long& ox, long& ow) { ox = (long)kt * 128; ow = (long)kt * 12288; }, NoPost());
    return;
  }
  const int wr = w & 1, wc = w >> 1, fr = lane & 15, fq = lane >> 4, sw = (fr >> 1) & 7;
  lds_cptr L = (lds_cptr)lds;
  lds_cptr x0 = L + (32 * wr + fr) * 128 + ((fq ^ sw) << 4), x1 = L + (32 * wr + fr) * 128 + (((4 + fq) ^ sw) << 4);
  lds_cptr w0 = L + G_W + (48 * wc + fr) * 128 + ((fq ^ sw) << 4), w1 = L + G_W + (48 * wc + fr) * 128 + (((4 + fq) ^ sw) << 4);
  f32x4 acc[3][2] = {};
  bf16x8 xa[2], wa[3], xb[2], wb_[3];
#define GRD(X, Wf, st, ks) do { X[0] = LDS_RD128(((ks) ? x1 : x0) + (st)); X[1] = LDS_RD128(((ks) ? x1 : x0) + (st) + 2048); \
    _Pragma("unroll") for (int n = 0; n < 3; ++n) Wf[n] = LDS_RD128(((ks) ? w1 : w0) + (st) + n * 2048); } while (0)
#define GSCHED() do { _Pragma("unroll") for (int i_ = 0; i_ < 5; ++i_) { SGB(0x8, 1); SGB(0x100, 1); } SGB(0x8, 1); } while (0)
#define GMM(X, Wf) do { _Pragma("unroll") for (int n = 0; n < 3; ++n) { acc[n][0] = MFMA16(Wf[n], X[0], acc[n][0]); acc[n][1] = MFMA16(Wf[n], X[1], acc[n][1]); } } while (0)
  WAITV(0); BAR();
  TS(KID, 1);
  GRD(xa, wa, 0, 0);
  int st = 0;
#pragma unroll 1
  for (int kt = 0; kt < NT; ++kt) {
    const int stn = st + G_STG == G_LDS ? 0 : st + G_STG;
    GRD(xb, wb_, st, 1);
    GMM(xa, wa);
    GSCHED();
    SB(); BAR(); SB();
    GRD(xa, wa, stn, 0);
    GMM(xb, wb_);
    GSCHED();
    SB();
    st = stn;
  }
  TS(KID, 2);
#pragma unroll
  for (int m = 0; m < 2; ++m) {
    const int row = m0 + 32 * wr + 16 * m + fr;
    float sum = 0.f, ssq = 0.f;
#pragma unroll
    for (int n = 0; n < 3; ++n) {
      const int col = n0 + 48 * wc + 16 * n + 4 * fq;
      const u32x2 r = *(const u32x2*)(res + (size_t)row * D + col);
      u32x2 o; o[0] = pk2(bflo(r[0]) + acc[n][m][0], bfhi(r[0]) + acc[n][m][1]); o[1] = pk2(bflo(r[1]) + acc[n][m][2], bfhi(r[1]) + acc[n][m][3]);
      *(u32x2*)(out + (size_t)row * D + col) = o;
      if (STATS) { const float a = bflo(o[0]), bq = bfhi(o[0]), c = bflo(o[1]), d = bfhi(o[1]); sum += (a + bq) + (c + d); ssq += (a * a + bq * bq) + (c * c + d * d); }
    }
    if (STATS) {
      sum += __shfl_xor(sum, 16); sum += __shfl_xor(sum, 32); ssq += __shfl_xor(ssq, 16); ssq += __shfl_xor(ssq, 32);
      if (fq == 0) *(f32x2_t*)(st2 + ((size_t)row * 12 + ct * 2 + wc) * 2) = f32x2_t{sum, ssq};
    }
  }
  TS(KID, 3); TS(KID, 7);
}
__global__ __launch_bounds__(768, 3) void k3_kernel(const u16* __restrict__ x, const u16* __restrict__ wo, const u16* __restrict__ w_in, char* __restrict__ ws) {
  extern __shared__ __attribute__((aligned(16))) char lds[];
  const int b = blockIdx.x;
  if (b >= 192) {
    const int tid = threadIdx.x, c = tid % 9, rg = tid / 9;
    float* red = (float*)lds;
    if (rg < 48) {
      const char* p = (const char*)(w_in + (b - 192) * 72 + c * 8) + (size_t)rg * (F * 2);
      u32x4 v[12];
#pragma unroll
      for (int i = 0; i < 12; ++i) v[i] = *(const u32x4*)(p + (size_t)i * 48 * F * 2);
      float a[8] = {};
#pragma unroll
      for (int i = 0; i < 12; ++i)
#pragma unroll
        for (int e = 0; e < 4; ++e) { a[2 * e] += bflo(v[i][e]); a[2 * e + 1] += bfhi(v[i][e]); }
#pragma unroll
      for (int e = 0; e < 8; ++e) red[rg * 72 + c * 8 + e] = a[e];
    }
    __syncthreads();
    if (tid < 72) { float t = 0.f; for (int g = 0; g < 48; ++g) t += red[g * 72 + tid]; ((float*)(ws + WS_CS))[(b - 192) * 72 + tid] = t; }
    return;
  }
  g_body<D, true, 3>(ws, lds, (const u16*)(ws + WS_O), ws + WS_WO, x, (u16*)(ws + WS_H), (float*)(ws + WS_ST2), b);
}
__global__ __launch_bounds__(768, 3) void k6_kernel(const u16* __restrict__ w_out, u16* __restrict__ out, char* __restrict__ ws) {
  extern __shared__ __attribute__((aligned(16))) char lds[];
  g_body<FH, false, 0>(ws, lds, (const u16*)(ws + WS_U), ws + WS_WOUT, (const u16*)(ws + WS_H), out, nullptr, blockIdx.x);
}

constexpr unsigned XTAG = 0x9E3779B9u;
DI float dpp_sum16(float v) {
  v += __builtin_bit_cast(float, __builtin_amdgcn_mov_dpp(__builtin_bit_cast(int, v), 0xB1, 0xf, 0xf, true));
  v += __builtin_bit_cast(float, __builtin_amdgcn_mov_dpp(__builtin_bit_cast(int, v), 0x4E, 0xf, 0xf, true));
  v += __builtin_bit_cast(float, __builtin_amdgcn_mov_dpp(__builtin_bit_cast(int, v), 0x141, 0xf, 0xf, true));
  v += __builtin_bit_cast(float, __builtin_amdgcn_mov_dpp(__builtin_bit_cast(int, v), 0x140, 0xf, 0xf, true));
  return v;
}
constexpr int K4_TAB = C8_LDS;
constexpr int K4_LDS = C8_LDS + (128 + 128 + 288 + 512 + 256) * 4;
template <int NTL>
DI void k4_compute(char* lds, int lane, int w, int pi, int ft, char* ws) {
  const int wr = w & 3, nb0 = NTL == 5 ? 0 : 5, wc = NTL == 5 ? 0 : 1, fr = lane & 15, fq = lane >> 4;
  float* tab = (float*)(lds + K4_TAB); const float* rs = tab; const float* mr = tab + 128; const float* cs = tab + 256; float* part = tab + 544; const float* st2 = tab + 1056;
  const C8Addr a = c8_addr<false>(lds, lane, wr, nb0);
  C8Frag<NTL> f0, f1;
  u32x32 z2v;
  WAITV(0); WAITL0(); BAR();
  TS(4, 1);
  c8_read<NTL, false>(f0, a, 0, 0);
  int st = 0;
#define C8_ITER(ACC, WXF, SIDE0, SIDE1) do { const int stn = st + C8_STG == C8_LDS ? 0 : st + C8_STG; \
    c8_read<NTL, false>(f1, a, st, 1); c8_mma<NTL, WXF>(ACC, f0); c8_sched<NTL, false>(); SB(); SIDE0; SB(); BAR(); SB(); \
    c8_read<NTL, false>(f0, a, stn, 0); c8_mma<NTL, WXF>(ACC, f1); c8_sched<NTL, false>(); SB(); SIDE1; SB(); st = stn; } while (0)
  {
    f32x4 acc2[NTL][2] = {};
#pragma unroll 1
    for (int kt = 0; kt < 9; ++kt) C8_ITER(acc2, false, (void)0, (void)0);
    TS(4, 4);
#pragma unroll
    for (int m = 0; m < 2; ++m) {
      const f32x4 r4 = *(const f32x4*)(rs + 32 * wr + 16 * m + 4 * fq), m4 = *(const f32x4*)(mr + 32 * wr + 16 * m + 4 * fq);
#pragma unroll
      for (int n = 0; n < NTL; ++n) {
        const float c = cs[16 * (nb0 + n) + fr];
        z2v[4 * n + 2 * m] = pk2(acc2[n][m][0] * r4[0] - m4[0] * c, acc2[n][m][1] * r4[1] - m4[1] * c);
        z2v[4 * n + 2 * m + 1] = pk2(acc2[n][m][2] * r4[2] - m4[2] * c, acc2[n][m][3] * r4[3] - m4[3] * c);
      }
    }
  }
  f32x4 acc1[NTL][2] = {};
  f32x4 rsum[2] = {}, rssq[2] = {};
  auto gl = [&](int n, int m) {
    const unsigned p0 = z2v[4 * n + 2 * m], p1 = z2v[4 * n + 2 * m + 1];
    const unsigned o0 = pk2(gelu_t(bflo(p0)), gelu_t(bfhi(p0))), o1 = pk2(gelu_t(bflo(p1)), gelu_t(bfhi(p1)));
    z2v[4 * n + 2 * m] = o0; z2v[4 * n + 2 * m + 1] = o1;
    const float q0 = bflo(o0), q1 = bfhi(o0), q2 = bflo(o1), q3 = bfhi(o1);
    rsum[m][0] += q0; rsum[m][1] += q1; rsum[m][2] += q2; rsum[m][3] += q3;
    rssq[m][0] += q0 * q0; rssq[m][1] += q1 * q1; rssq[m][2] += q2 * q2; rssq[m][3] += q3 * q3;
  };
#pragma unroll 1
  for (int i = 0; i < NTL; ++i) C8_ITER(acc1, true, gl(i, 0), gl(i, 1));
  if (NTL == 4) C8_ITER(acc1, true, (void)0, (void)0);
  auto red = [&]() {
#pragma unroll
    for (int m = 0; m < 2; ++m)
#pragma unroll
      for (int r = 0; r < 4; ++r) {
        const float su = dpp_sum16(rsum[m][r]), sq = dpp_sum16(rssq[m][r]);
        if (fr == 0) *(f32x2_t*)(part + (wc * 128 + 32 * wr + 16 * m + 4 * fq + r) * 2) = f32x2_t{su, sq};
      }
    WAITL0();
  };
  auto pub = [&]() {
    if (wc == 0 && lane < 32) {
      const int row = 32 * wr + lane; const f32x2_t p0 = *(const f32x2_t*)(part + row * 2), p1 = *(const f32x2_t*)(part + (128 + row) * 2);
      const __amdgpu_buffer_rsrc_t rs_ = __builtin_amdgcn_make_buffer_rsrc((void*)(ws + WS_XS), 0, 2048 * 16 * 16, 0x00020000);
      const u32x4 g = {__float_as_uint(p0[0] + p1[0]), XTAG, __float_as_uint(p0[1] + p1[1]), XTAG};
      __builtin_amdgcn_raw_buffer_store_b128(g, rs_, (unsigned)(((pi * 16 + ft) * 128 + row) * 16), 0, 16);
    }
  };
  C8_ITER(acc1, true, red(), pub());
#pragma unroll 1
  for (int i = 6; i < 9; ++i) C8_ITER(acc1, true, (void)0, (void)0);
  TS(4, 2);
  int lz = lane; asm volatile("" : "+v"(lz));
  {
    const int lane = lz, fr = lz & 15, fq = lz >> 4;
    char* zb = ws + WS_Z1 + ((size_t)(pi * 16 + ft) * 4 + wr) * (18 * 512) + lane * 8;
#pragma unroll
    for (int m = 0; m < 2; ++m) {
      const float r = rs[32 * wr + 16 * m + fr], mm = mr[32 * wr + 16 * m + fr];
#pragma unroll
      for (int n = 0; n < NTL; ++n) {
        const f32x4 c4 = *(const f32x4*)(cs + 144 + 16 * (nb0 + n) + 4 * fq);
        u32x2 o; o[0] = pk2(acc1[n][m][0] * r - mm * c4[0], acc1[n][m][1] * r - mm * c4[1]); o[1] = pk2(acc1[n][m][2] * r - mm * c4[2], acc1[n][m][3] * r - mm * c4[3]);
        *(u32x2*)(zb + ((nb0 + n) * 2 + m) * 512) = o;
      }
    }
  }
  TS(4, 5);
  __syncthreads();
  asm volatile("" : "+v"(lz));
#pragma unroll
  for (int m = 0; m < 2; ++m) {
    const int fr = lz & 15, fq = lz >> 4;
    const int sl = 32 * wr + 16 * m + 4 * fq;
    const f32x4 sa = *(const f32x4*)(st2 + sl * 2), sb = *(const f32x4*)(st2 + sl * 2 + 4);
    const float mu[4] = {sa[0], sa[2], sb[0], sb[2]}, rr[4] = {sa[1], sa[3], sb[1], sb[3]};
#pragma unroll
    for (int n = 0; n < NTL; ++n) {
      const int f = 16 * (nb0 + n) + fr; const u32x2 p = {z2v[4 * n + 2 * m], z2v[4 * n + 2 * m + 1]};
      u32x2 o; o[0] = pk2(bflo(p[0]) * rr[0] - mu[0], bfhi(p[0]) * rr[1] - mu[1]); o[1] = pk2(bflo(p[1]) * rr[2] - mu[2], bfhi(p[1]) * rr[3] - mu[3]);
      *(u32x2*)(lds + (sl >> 6) * 18432 + f * 128 + ((((sl & 63) >> 3) ^ ((f >> 1) & 7)) << 4) + (sl & 4) * 2) = o;
    }
  }
}
__global__ __launch_bounds__(768, 3) void k4_kernel(char* __restrict__ ws) {
  extern __shared__ __attribute__((aligned(16))) char lds[];
  const int tid = threadIdx.x, lane = tid & 63; const int w = __builtin_amdgcn_readfirstlane(tid >> 6);
  const int b = blockIdx.x, xc = b & 7, idx = b >> 3;
  const int pi = (xc & 1) * 8 + (idx & 7), ft = (xc >> 1) * 4 + (idx >> 3);
  float* tab = (float*)(lds + K4_TAB);
  TS(4, 0); TS(4, 6);
  const char* Xb = ws + WS_H + (size_t)pi * 128 * D * 2; const char* Wb = ws + WS_WIN + (size_t)ft * 9 * 18432;
  auto src = [&](int p) { return p < 16 ? Xb + tn_off(p * 8 + (lane >> 3), lane, D * 2) : Wb + (p - 16) * 1024 + lane * 16; };
  first_tile<34, 12>(lds, w, src);
  if (w >= 8) {
    const int lw = w - 8;
    __builtin_amdgcn_s_setprio(2);
    loader_loop<18, C8_NS, C8_STG, 34, 16, 4>(lds, lw, src, [&](int kt, long& ox, long& ow) {
      const int k = kt < 9 ? kt : kt - 9; ox = (long)k * 128; ow = (long)k * 18432 + (kt < 9 ? 0 : 16 * 9 * 18432); }, NoPost());
    const int row = 32 * lw + (lane >> 1);
    const __amdgpu_buffer_rsrc_t rs_ = __builtin_amdgcn_make_buffer_rsrc((void*)(ws + WS_XS), 0, 2048 * 16 * 16, 0x00020000);
    const unsigned vo = (unsigned)(((pi * 16 + (lane & 1) * 8) * 128 + row) * 16);
    float sum = 0.f, ssq = 0.f;
    for (int spin = 0; spin < (1 << 20); ++spin) {
      bool ok = true; sum = 0.f; ssq = 0.f;
      u32x4 g[8];
#pragma unroll
      for (int t = 0; t < 8; ++t) g[t] = __builtin_amdgcn_raw_buffer_load_b128(rs_, vo + t * 2048, 0, 16);
#pragma unroll
      for (int t = 0; t < 8; ++t) { ok &= g[t][1] == XTAG && g[t][3] == XTAG; sum += __uint_as_float(g[t][0]); ssq += __uint_as_float(g[t][2]); }
      if (__all(ok)) break;
      __builtin_amdgcn_s_sleep(8);
    }
    sum += __shfl_xor(sum, 1); ssq += __shfl_xor(ssq, 1);
    const float mean = sum * (1.f / FH), var = fmaxf(ssq * (1.f / FH) - mean * mean, 0.f), rstd = rsqrtf(var + 1e-5f);
    if ((lane & 1) == 0) *(f32x2_t*)(tab + 1056 + row * 2) = f32x2_t{mean * rstd, rstd};
    __syncthreads();
  } else {
    if (tid < 128) {
      const f32x4* p = (const f32x4*)(ws + WS_ST2) + (size_t)(pi * 128 + tid) * 6;
      float sum = 0.f, ssq = 0.f;
#pragma unroll
      for (int t = 0; t < 6; ++t) { const f32x4 v = p[t]; sum += v[0] + v[2]; ssq += v[1] + v[3]; }
      const float mean = sum * (1.f / D), var = fmaxf(ssq * (1.f / D) - mean * mean, 0.f), rstd = rsqrtf(var + 1e-5f);
      tab[tid] = rstd; tab[128 + tid] = mean * rstd;
    } else if (tid < 416) {
      const int c = tid - 128;
      tab[256 + c] = ((const float*)(ws + WS_CS))[c < 144 ? FH + ft * 144 + c : ft * 144 + c - 144];
    }
    if (w < 4) k4_compute<5>(lds, lane, w, pi, ft, ws); else k4_compute<4>(lds, lane, w, pi, ft, ws);
  }
  __syncthreads();
  {
    u32x4* dst = (u32x4*)(ws + WS_Z2T + ((size_t)ft * 32 + pi * 2) * 18432);
    for (int i = tid; i < 2304; i += 768) dst[i] = *(const u32x4*)(lds + i * 16);
  }
  TS(4, 3); TS(4, 7);
}

template <int NTL>
DI void k5_compute(char* lds, int lane, int w, int tt, int ft, const u16* __restrict__ bsp, char* ws) {
  const int wr = w & 3, nb0 = NTL == 5 ? 0 : 5, fr = lane & 15, fq = lane >> 4;
  const C8Addr a = c8_addr<false>(lds, lane, wr, nb0);
  f32x4 acc[NTL][2] = {};
  C8Frag<NTL> f0, f1;
  WAITV(0); BAR();
  TS(5, 1);
  c8_read<NTL, false>(f0, a, 0, 0);
  u32x32 z1v;
  { const char* zb = ws + WS_Z1 + ((size_t)(tt * 16 + ft) * 4 + wr) * (18 * 512) + lane * 8;
#pragma unroll
    for (int n = 0; n < NTL; ++n)
#pragma unroll
      for (int m = 0; m < 2; ++m) { const u32x2 v = *(const u32x2*)(zb + ((nb0 + n) * 2 + m) * 512); z1v[4 * n + 2 * m] = v[0]; z1v[4 * n + 2 * m + 1] = v[1]; } }
  float bb[2];
#pragma unroll
  for (int m = 0; m < 2; ++m) bb[m] = bf2f(bsp[tt * 128 + 32 * wr + 16 * m + fr]);
  int st = 0;
#define C5_ITER(SIDE0, SIDE1) do { const int stn = st + C8_STG == C8_LDS ? 0 : st + C8_STG; \
    c8_read<NTL, false>(f1, a, st, 1); c8_mma<NTL, true>(acc, f0); c8_sched<NTL, false>(); SB(); SIDE0; SB(); BAR(); SB(); \
    c8_read<NTL, false>(f0, a, stn, 0); c8_mma<NTL, true>(acc, f1); c8_sched<NTL, false>(); SB(); SIDE1; SB(); st = stn; } while (0)
  auto gl = [&](int n, int m) {
    const unsigned p0 = z1v[4 * n + 2 * m], p1 = z1v[4 * n + 2 * m + 1];
    z1v[4 * n + 2 * m] = pk2(gelu_t(bflo(p0)), gelu_t(bfhi(p0))); z1v[4 * n + 2 * m + 1] = pk2(gelu_t(bflo(p1)), gelu_t(bfhi(p1)));
  };
#pragma unroll 1
  for (int kt = 0; kt < 4; ++kt) C5_ITER((void)0, (void)0);
#pragma unroll 1
  for (int i = 0; i < NTL; ++i) C5_ITER(gl(i, 0), gl(i, 1));
#pragma unroll 1
  for (int kt = 4 + NTL; kt < 32; ++kt) C5_ITER((void)0, (void)0);
  TS(5, 2);
  u16* u = (u16*)(ws + WS_U);
#pragma unroll
  for (int m = 0; m < 2; ++m) {
    const int t = tt * 128 + 32 * wr + 16 * m + fr;
#pragma unroll
    for (int n = 0; n < NTL; ++n) {
      const int f = ft * 144 + 16 * (nb0 + n) + 4 * fq; const unsigned z0 = z1v[4 * n + 2 * m], z1 = z1v[4 * n + 2 * m + 1];
      u32x2 o; o[0] = pk2(bflo(z0) * (acc[n][m][0] + bb[m]), bfhi(z0) * (acc[n][m][1] + bb[m]));
      o[1] = pk2(bflo(z1) * (acc[n][m][2] + bb[m]), bfhi(z1) * (acc[n][m][3] + bb[m]));
      *(u32x2*)(u + (size_t)t * FH + f) = o;
    }
  }
}
__global__ __launch_bounds__(768, 3) void k5_kernel(const u16* __restrict__ wsp, const u16* __restrict__ bsp, char* __restrict__ ws) {
  extern __shared__ __attribute__((aligned(16))) char lds[];
  TS(5, 0); TS(5, 6);
  const int tid = threadIdx.x, lane = tid & 63; const int w = __builtin_amdgcn_readfirstlane(tid >> 6);
  const int b = blockIdx.x, xc = b & 7, idx = b >> 3;
  const int tt = (xc & 1) * 8 + (idx & 7), ft = (xc >> 1) * 4 + (idx >> 3);
  const char* Xb = (const char*)(wsp + (size_t)tt * 128 * S); const char* Wb = ws + WS_Z2T + (size_t)ft * 32 * 18432;
  auto src = [&](int p) { return p < 16 ? Xb + tn_off(p * 8 + (lane >> 3), lane, S * 2) : Wb + (p - 16) * 1024 + lane * 16; };
  first_tile<34, 12>(lds, w, src);
  if (w >= 8) {
    __builtin_amdgcn_s_setprio(2);
    loader_loop<32, C8_NS, C8_STG, 34, 16, 4>(lds, w - 8, src, [&](int kt, long& ox, long& ow) { ox = (long)kt * 128; ow = (long)kt * 18432; }, NoPost());
    return;
  }
  if (w < 4) k5_compute<5>(lds, lane, w, tt, ft, bsp, ws); else k5_compute<4>(lds, lane, w, tt, ft, bsp, ws);
  TS(5, 3); TS(5, 7);
}

extern "C" void kernel_launch(void* const* d_in, const int* in_sizes, int n_in, void* d_out, int out_size, void* d_ws, size_t ws_size, hipStream_t stream) {
  const u16* x = (const u16*)d_in[0]; const u16* wqkv = (const u16*)d_in[1]; const u16* wo = (const u16*)d_in[2];
  const u16* w_in = (const u16*)d_in[4]; const u16* w_sp = (const u16*)d_in[5]; const u16* b_sp = (const u16*)d_in[6]; const u16* w_out = (const u16*)d_in[7];
  u16* out = (u16*)d_out; char* ws = (char*)d_ws;
  static bool attr = false;
  if (!attr) {
    (void)hipFuncSetAttribute((const void*)k1_kernel, hipFuncAttributeMaxDynamicSharedMemorySize, K1_LDS);
    (void)hipFuncSetAttribute((const void*)k2_kernel, hipFuncAttributeMaxDynamicSharedMemorySize, ATT_LDS);
    (void)hipFuncSetAttribute((const void*)k3_kernel, hipFuncAttributeMaxDynamicSharedMemorySize, G_LDS);
    (void)hipFuncSetAttribute((const void*)k4_kernel, hipFuncAttributeMaxDynamicSharedMemorySize, K4_LDS);
    (void)hipFuncSetAttribute((const void*)k5_kernel, hipFuncAttributeMaxDynamicSharedMemorySize, C8_LDS);
    (void)hipFuncSetAttribute((const void*)k6_kernel, hipFuncAttributeMaxDynamicSharedMemorySize, G_LDS);
    attr = true;
  }
  k1_kernel<<<256, K1_THREADS, K1_LDS, stream>>>(x, wqkv, ws);
  k2_kernel<<<256, 768, ATT_LDS, stream>>>(wo, w_in, w_out, ws);
  k3_kernel<<<256, 768, G_LDS, stream>>>(x, wo, w_in, ws);
  k4_kernel<<<256, 768, K4_LDS, stream>>>(ws);
  k5_kernel<<<256, 768, C8_LDS, stream>>>(w_sp, b_sp, ws);
  k6_kernel<<<192, 768, G_LDS, stream>>>(w_out, out, ws);
}
```

```cpp
#include <hip/hip_runtime.h>
#include <hip/hip_bf16.h>
#include <stdint.h>
typedef unsigned short u16;
using bf16x8 = __attribute__((ext_vector_type(8))) short;
using s16x4 = __attribute__((ext_vector_type(4))) short;
using f32x4 = __attribute__((ext_vector_type(4))) float;
using f32x16 = __attribute__((ext_vector_type(16))) float;
using u32x4 = __attribute__((ext_vector_type(4))) unsigned;
using u32x2 = __attribute__((ext_vector_type(2))) unsigned;
using u32x32 = __attribute__((ext_vector_type(32))) unsigned;
typedef __bf16 bf16x2_t __attribute__((ext_vector_type(2)));
typedef float f32x2_t __attribute__((ext_vector_type(2)));
constexpr int S = 2048, D = 576, H = 32, DH = 18, F = 4608, FH = 2304;
#define DI __device__ __forceinline__
#define LAS __attribute__((address_space(3)))
#define GAS __attribute__((address_space(1)))
DI int lane_id() { int l = __builtin_amdgcn_mbcnt_hi(~0u, __builtin_amdgcn_mbcnt_lo(~0u, 0u)); asm volatile("" : "+v"(l)); return l; }
DI float bf2f(u16 x) { return __uint_as_float((unsigned)x << 16); }
DI float bflo(unsigned x) { return __uint_as_float(x << 16); }
DI float bfhi(unsigned x) { return __uint_as_float(x & 0xffff0000u); }
DI unsigned pk2(float lo, float hi) { f32x2_t v = {lo, hi}; return __builtin_bit_cast(unsigned, __builtin_convertvector(v, bf16x2_t)); }
#define MFMA32(a, b, c) __builtin_amdgcn_mfma_f32_32x32x16_bf16((a), (b), (c), 0, 0, 0)
#define MFMA16(a, b, c) __builtin_amdgcn_mfma_f32_16x16x32_bf16((a), (b), (c), 0, 0, 0)
#define SB() __builtin_amdgcn_sched_barrier(0)
#define SGB(mask, n) __builtin_amdgcn_sched_group_barrier(mask, n, 0)
#define PIN(x) asm volatile("" : "+v"(x))
#define BAR() __builtin_amdgcn_s_barrier()
#define WAITV(n) asm volatile("s_waitcnt vmcnt(%0)" :: "i"(n) : "memory")
#define WAITL0() asm volatile("s_waitcnt lgkmcnt(0)" ::: "memory")
DI void glds16(const void* g, void* l) { __builtin_amdgcn_global_load_lds((const GAS unsigned*)g, (LAS unsigned*)l, 16, 0, 0); }
DI void st16_wt(char* ws, size_t off, u32x4 v) {
  const __amdgpu_buffer_rsrc_t r = __builtin_amdgcn_make_buffer_rsrc((void*)ws, 0, 128 << 20, 0x00020000);
  __builtin_amdgcn_raw_buffer_store_b128(v, r, (unsigned)off, 0, 16);
}
typedef LAS const char* lds_cptr;
typedef LAS char* lds_ptr;
#define LDS_RD128(p) (*(const LAS bf16x8*)(p))
#define LDS_RD32(p) (*(const LAS unsigned*)(p))
typedef short v4i16_t __attribute__((ext_vector_type(4)));
DI s16x4 lds_tr(lds_cptr p) { return __builtin_bit_cast(s16x4, __builtin_amdgcn_ds_read_tr16_b64_v4i16((LAS v4i16_t*)p)); }
DI bf16x8 lds_tr2(lds_cptr p0, lds_cptr p1) { const s16x4 a = lds_tr(p0), b = lds_tr(p1); return __builtin_shufflevector(a, b, 0, 1, 2, 3, 4, 5, 6, 7); }

#define TIMING 0
#define TS(k, i)
#define TSTAMP(v)
#define RSTAMP(v)

constexpr int KV_TILE = 2400, KV_K1 = 1024, KV_V = 1152, KV_VS = 624, KV_Z = KV_V + 608, KV_HEAD = 64 * KV_TILE;
constexpr int Q_TILE = 1152, Q_HEAD = 64 * Q_TILE;
constexpr size_t WS_KV = 0;
constexpr size_t WS_Q = 5u << 20;
constexpr size_t WS_O = 8u << 20;
constexpr size_t WS_H = 11u << 20;
constexpr size_t WS_ST2 = 14u << 20;
constexpr size_t WS_CS = 15u << 20;
constexpr size_t WS_XS = 16u << 20;
constexpr size_t WS_Z2T = 17u << 20;
constexpr size_t WS_Z1 = 27u << 20;
constexpr size_t WS_U = 37u << 20;
constexpr size_t WS_WIN = 47u << 20;
constexpr size_t WS_WO = 53u << 20;
constexpr size_t WS_WOUT = 54u << 20;
constexpr size_t WS_DBG = 64u << 20;

template <int NP, int NW, class SRC>
DI void first_tile(char* lds, int w, SRC src) {
#pragma unroll
  for (int j = 0; j < (NP + NW - 1) / NW; ++j) { const int p = w + NW * j; if (p < NP) glds16(src(p), lds + p * 1024); }
}
template <int NT, int NS, int STG, int NP, int NPX, int NLD, class SRC, class OFF, class POST>
DI void loader_loop(char* lds, int lw, SRC src, OFF off, POST post) {
  constexpr int PPW = (NP + NLD - 1) / NLD, PMIN = NP / NLD;
  static_assert((NS - 1) * PPW + 4 <= 63, "vmcnt");
  static_assert(NPX % NLD == 0, "X pieces");
  const char* base[PPW];
#pragma unroll
  for (int j = 0; j < PPW; ++j) { const int p = lw + NLD * j; base[j] = src(p < NP ? p : NP - 1); }
  auto issue = [&](int kt, int stage) {
    long ox, ow; off(kt, ox, ow);
#pragma unroll
    for (int j = 0; j < PPW; ++j) if (j < PMIN || lw + NLD * j < NP) glds16(base[j] + (j < NPX / NLD ? ox : ow), lds + stage * STG + (lw + NLD * j) * 1024);
  };
  issue(1, 1);
  WAITV(PMIN);
  BAR();
#pragma unroll
  for (int t = 2; t < NS - 1; ++t) issue(t, t);
  post(0);
  int stg = NS - 1, pst = 1;
#pragma unroll 1
  for (int kt = 0; kt < NT - (NS - 1); ++kt) {
    WAITV((NS - 3) * PMIN);
    BAR();
    issue(kt + NS - 1, stg); stg = stg + 1 == NS ? 0 : stg + 1;
    post(pst); pst = pst + 1 == NS ? 0 : pst + 1;
  }
#pragma unroll
  for (int r = NS - 2; r >= 0; --r) {
    WAITV((r - 1 > 0 ? r - 1 : 0) * PMIN); BAR();
    if (r > 0) { post(pst); pst = pst + 1 == NS ? 0 : pst + 1; }
  }
}
struct NoPost { DI void operator()(int) const {} };
DI unsigned tn_off(int row, int lane, int ld_bytes) { return (unsigned)row * ld_bytes + ((((lane & 7) ^ ((row >> 1) & 7))) << 4); }
template <int BN> DI unsigned kn_off(int p, int lane, int ld_bytes) {
  constexpr int CPR = BN / 8;
  const int c = 64 * p + lane, kp = c / CPR, cc = c - kp * CPR, odd = (kp >> 3) & 1, k = kp ^ (odd << 2);
  int cs = cc;
  if (BN == 96) { int blk = (cc >> 1) - odd; if (blk < 0) blk += 6; cs = blk * 2 + (cc & 1); }
  return (unsigned)k * ld_bytes + (unsigned)cs * 16;
}

DI float gelu_t(float x) {
  const float c1 = -2.f * 0.7978845608028654f * 1.4426950408889634f, c2 = c1 * 0.044715f;
  const float e = __builtin_amdgcn_exp2f(x * __builtin_fmaf(x * x, c2, c1));
  return x * __builtin_amdgcn_rcpf(1.f + e);
}

constexpr int C8_STG = 272 * 128, C8_W = 16384, C8_NS = 4, C8_LDS = C8_NS * C8_STG;
struct C8Addr { lds_cptr x0, x1, w0, w1; };
template <bool BKN> DI C8Addr c8_addr(char* lds, int lane, int wr, int nb0) {
  lds_cptr L = (lds_cptr)lds; const int fr = lane & 15, fq = lane >> 4, sw = (fr >> 1) & 7; C8Addr a;
  a.x0 = L + (32 * wr + fr) * 128 + ((fq ^ sw) << 4); a.x1 = L + (32 * wr + fr) * 128 + (((4 + fq) ^ sw) << 4);
  if (BKN) { const int q = fr >> 2, p4 = lane & 3, b = C8_W + (8 * fq + q) * 288 + nb0 * 32 + p4 * 8;
             a.w0 = L + b + (fq & 1) * 1152; a.w1 = L + b + ((fq & 1) ^ 1) * 1152; }
  else { a.w0 = L + C8_W + (16 * nb0 + fr) * 128 + ((fq ^ sw) << 4); a.w1 = L + C8_W + (16 * nb0 + fr) * 128 + (((4 + fq) ^ sw) << 4); }
  return a;
}
template <int NTL> struct C8Frag { bf16x8 x[2], w[NTL]; };
template <int NTL, bool BKN> DI void c8_read(C8Frag<NTL>& f, const C8Addr& a, int st, int ks) {
  f.x[0] = LDS_RD128((ks ? a.x1 : a.x0) + st); f.x[1] = LDS_RD128((ks ? a.x1 : a.x0) + st + 2048);
#pragma unroll
  for (int n = 0; n < NTL; ++n) {
    if (BKN) f.w[n] = lds_tr2(a.w0 + st + ks * 9216 + n * 32, a.w1 + st + ks * 9216 + n * 32);
    else f.w[n] = LDS_RD128((ks ? a.w1 : a.w0) + st + n * 2048);
  }
}
template <int NTL, bool WX> DI void c8_mma(f32x4 (&acc)[NTL][2], const C8Frag<NTL>& f) {
#pragma unroll
  for (int n = 0; n < NTL; ++n) {
#pragma unroll
    for (int m = 0; m < 2; ++m) acc[n][m] = WX ? MFMA16(f.w[n], f.x[m], acc[n][m]) : MFMA16(f.x[m], f.w[n], acc[n][m]);
  }
}
template <int NTL, bool BKN> DI void c8_sched() {
  constexpr int NR = 2 + (BKN ? 2 * NTL : NTL), NM = 2 * NTL;
  if (NR <= NM) {
#pragma unroll
    for (int i = 0; i < NR; ++i) { SGB(0x8, 1); SGB(0x100, 1); }
    SGB(0x8, NM - NR);
  } else {
#pragma unroll
    for (int i = 0; i < NM; ++i) { SGB(0x8, 1); SGB(0x100, 1); if (i < NR - NM) SGB(0x100, 1); }
  }
}

constexpr int K1_THREADS = 768;
constexpr int EP_PITCH = 304;
template <int NTL, bool WX>
DI void k1_compute(char* lds, int lane, int w, f32x4 (&acc)[NTL][4], f32x4 (&cs)[NTL], char* ws) {
  const int wr = w & 1, wc = w >> 1, nb0 = wc == 0 ? 0 : 2 * wc + 1, fr = lane & 15, fq = lane >> 4, sw = (fr >> 1) & 7;
  lds_cptr L = (lds_cptr)lds;
  lds_cptr x0 = L + (64 * wr + fr) * 128 + ((fq ^ sw) << 4), x1 = L + (64 * wr + fr) * 128 + (((4 + fq) ^ sw) << 4);
  const int wb = C8_W + (8 * fq + (fr >> 2)) * 288 + nb0 * 32 + (lane & 3) * 8;
  lds_cptr w0 = L + wb + (fq & 1) * 1152, w1 = L + wb + ((fq & 1) ^ 1) * 1152;
  const u32x4 onesv = {0x3f803f80u, 0x3f803f80u, 0x3f803f80u, 0x3f803f80u}; const bf16x8 ones = __builtin_bit_cast(bf16x8, onesv);
  bf16x8 xa[4], wa[NTL], xb[4], wb_[NTL];
#define K1RD(X, Wf, st, ks) do { _Pragma("unroll") for (int n = 0; n < NTL; ++n) Wf[n] = lds_tr2(w0 + (st) + (ks) * 9216 + n * 32, w1 + (st) + (ks) * 9216 + n * 32); \
    _Pragma("unroll") for (int m = 0; m < 4; ++m) X[m] = LDS_RD128(((ks) ? x1 : x0) + (st) + m * 2048); } while (0)
#define K1MM(X, Wf) do { _Pragma("unroll") for (int n = 0; n < NTL; ++n) { _Pragma("unroll") for (int m = 0; m < 4; ++m) acc[n][m] = WX ? MFMA16(Wf[n], X[m], acc[n][m]) : MFMA16(X[m], Wf[n], acc[n][m]); \
    cs[n] = MFMA16(ones, Wf[n], cs[n]); } } while (0)
#define K1SCHED() do { _Pragma("unroll") for (int i_ = 0; i_ < 2 * NTL + 4; ++i_) { SGB(0x8, 1); SGB(0x100, 1); } SGB(0x8, 3 * NTL - 4); } while (0)
  WAITV(0); BAR();
  TS(1, 1);
  K1RD(xa, wa, 0, 0);
  int st = 0;
#pragma unroll 1
  for (int kt = 0; kt < 9; ++kt) {
    const int stn = st + C8_STG == C8_LDS ? 0 : st + C8_STG;
    K1RD(xb, wb_, st, 1); K1MM(xa, wa); K1SCHED();
    SB(); BAR(); SB();
    K1RD(xa, wa, stn, 0); K1MM(xb, wb_); K1SCHED();
    SB();
    st = stn;
  }
}
template <int NTL>
DI void k1_epilogue(char* lds, int lane, int w, int type, int rt, int hg, const float* tabs, f32x4 (&acc)[NTL][4], char* kvimg) {
  asm volatile("" : "+v"(lane));
  const int wr = w & 1, wc = w >> 1, nb0 = wc == 0 ? 0 : 2 * wc + 1, fr = lane & 15, fq = lane >> 4;
  const float* rs = tabs; const float* mr = tabs + 128; const float* cs = tabs + 256;
  if (type == 2) {
#pragma unroll
    for (int n = 0; n < NTL; ++n) {
      const int col = 16 * (nb0 + n) + fr, hh = col / 18, d = col - hh * 18; const float c = cs[col];
      char* hb = kvimg + (size_t)(hg * 8 + hh) * KV_HEAD + (size_t)(rt * 4 + 2 * wr) * KV_TILE + KV_V + (fq & 1) * 304 + (fq >> 1) * 8 + d * 16;
#pragma unroll
      for (int m = 0; m < 4; ++m) {
        const int row = 64 * wr + 16 * m + 4 * fq;
        const f32x4 r4 = *(const f32x4*)(rs + row), m4 = *(const f32x4*)(mr + row);
        u32x2 o; o[0] = pk2(acc[n][m][0] * r4[0] - m4[0] * c, acc[n][m][1] * r4[1] - m4[1] * c); o[1] = pk2(acc[n][m][2] * r4[2] - m4[2] * c, acc[n][m][3] * r4[3] - m4[3] * c);
        *(u32x2*)(hb + (m >> 1) * KV_TILE + (m & 1) * KV_VS) = o;
      }
    }
  } else {
    const float qs = type == 0 ? 0.23570226039551584f * 1.4426950408889634f : 1.f;
#pragma unroll
    for (int m = 0; m < 4; ++m) {
      const int row = 64 * wr + 16 * m + fr; const float r = rs[row] * qs, mm = mr[row] * qs;
#pragma unroll
      for (int n = 0; n < NTL; ++n) {
        const int col = 16 * (nb0 + n) + 4 * fq; const f32x4 c4 = *(const f32x4*)(cs + col);
        u32x2 o; o[0] = pk2(acc[n][m][0] * r - mm * c4[0], acc[n][m][1] * r - mm * c4[1]); o[1] = pk2(acc[n][m][2] * r - mm * c4[2], acc[n][m][3] * r - mm * c4[3]);
        *(u32x2*)(lds + row * EP_PITCH + col * 2) = o;
      }
    }
  }
}
__global__ __launch_bounds__(K1_THREADS, 3) void k1_kernel(const u16* __restrict__ x, const u16* __restrict__ wqkv, char* __restrict__ ws) {
  extern __shared__ __attribute__((aligned(16))) char lds[];
  const int tid = threadIdx.x, lane = tid & 63; const int w = __builtin_amdgcn_readfirstlane(tid >> 6);
  const int b = blockIdx.x;
  TS(1, 0); TS(1, 6);
  if (b >= 192) {
    if (tid < 512) ((u32x4*)(ws + WS_XS))[(b - 192) * 512 + tid] = u32x4{0, 0, 0, 0};
    return;
  }
  const int xc = b & 7, idx = b >> 3;
  const int rt = xc * 2 + (idx & 1), ct = idx >> 1;
  const int type = ct >> 2, hg = ct & 3;
  float* tabs = (float*)(lds + C8_LDS);
  char* kvimg = ws + WS_KV; char* qimg = ws + WS_Q;
  const char* Xb = (const char*)(x + (size_t)rt * 128 * D); const char* Wb = (const char*)(wqkv + ct * 144);
  auto src = [&](int p) { return p < 16 ? Xb + tn_off(p * 8 + (lane >> 3), lane, D * 2) : Wb + kn_off<144>(p - 16, lane, 1728 * 2); };
  first_tile<34, 12>(lds, w, src);
  if (w >= 8) {
    const int lw = w - 8;
    __builtin_amdgcn_s_setprio(2);
    float sum = 0.f, ssq = 0.f;
    const int row = 32 * lw + (lane >> 1), sw = (row >> 1) & 7;
    lds_cptr xr = (lds_cptr)lds + row * 128;
    loader_loop<9, C8_NS, C8_STG, 34, 16, 4>(lds, lw, src, [&](int kt, long& ox, long& ow) { ox = (long)kt * 128; ow = (long)kt * 64 * 1728 * 2; },
      [&](int stage) {
        const int st = stage * C8_STG;
#pragma unroll
        for (int c = 0; c < 4; ++c) {
          const u32x4 v = __builtin_bit_cast(u32x4, LDS_RD128(xr + st + (((4 * (lane & 1) + c) ^ sw) << 4)));
#pragma unroll
          for (int e = 0; e < 4; ++e) { const float lo = bflo(v[e]), hi = bfhi(v[e]); sum += lo + hi; ssq = __builtin_fmaf(lo, lo, ssq); ssq = __builtin_fmaf(hi, hi, ssq); }
        }
      });
    sum += __shfl_xor(sum, 1); ssq += __shfl_xor(ssq, 1);
    const float mean = sum * (1.f / D), var = fmaxf(ssq * (1.f / D) - mean * mean, 0.f), rstd = rsqrtf(var + 1e-5f);
    if ((lane & 1) == 0) { tabs[row] = rstd; tabs[128 + row] = mean * rstd; }
  } else {
#define K1_PATH(NTL, WX) do { f32x4 cs[NTL] = {}; f32x4 acc[NTL][4] = {}; \
      k1_compute<NTL, WX>(lds, lane, w, acc, cs, ws); TS(1, 2); \
      const int l2 = lane_id(); \
      if (l2 < 16 && (w & 1) == 0) { _Pragma("unroll") for (int n = 0; n < NTL; ++n) tabs[256 + 16 * (((w >> 1) == 0 ? 0 : 2 * (w >> 1) + 1) + n) + l2] = cs[n][0]; } \
      __syncthreads(); \
      k1_epilogue<NTL>(lds, l2, w, WX ? (type & 1) : 2, rt, hg, tabs, acc, kvimg); } while (0)
    if (type == 2) { if (w < 2) K1_PATH(3, false); else K1_PATH(2, false); }
    else { if (w < 2) K1_PATH(3, true); else K1_PATH(2, true); }
  }
  if (w >= 8) __syncthreads();
  const int tid2 = w * 64 + lane_id();
  if (type == 2) {
    if (tid2 < 192) {
      const int tl = tid2 / 48, r = tid2 - tl * 48, hh = r / 6, c = r - hh * 6;
      char* tb = kvimg + (size_t)(hg * 8 + hh) * KV_HEAD + (size_t)(rt * 4 + tl) * KV_TILE;
      if (c < 4) *(u32x4*)(tb + KV_V + (c >> 1) * KV_VS + (c & 1) * 304 + 18 * 16) = u32x4{0x3f803f80u, 0x3f803f80u, 0x3f803f80u, 0x3f803f80u};
      else *(u32x4*)(tb + KV_Z + (c - 4) * KV_VS) = u32x4{0, 0, 0, 0};
    }
    TS(1, 3); TS(1, 7);
    return;
  }
  __syncthreads();
  for (int it = tid2; it < 1024; it += K1_THREADS) {
    const int tok = it & 127, hh = it >> 7;
    const unsigned* sp = (const unsigned*)(lds + tok * EP_PITCH + hh * 36);
    u32x4 c0 = {sp[0], sp[1], sp[2], sp[3]}, c1 = {sp[4], sp[5], sp[6], sp[7]}; const unsigned c2 = sp[8];
    char* tb = type == 0 ? qimg + (size_t)(hg * 8 + hh) * Q_HEAD + (size_t)(rt * 4 + (tok >> 5)) * Q_TILE
                         : kvimg + (size_t)(hg * 8 + hh) * KV_HEAD + (size_t)(rt * 4 + (tok >> 5)) * KV_TILE;
    const int key = tok & 31;
    *(u32x4*)(tb + key * 16) = c0; *(u32x4*)(tb + 512 + key * 16) = c1; *(unsigned*)(tb + 1024 + key * 4) = c2;
  }
  TS(1, 3); TS(1, 7);
}
static_assert(128 * EP_PITCH <= C8_LDS, "");
constexpr int K1_LDS = C8_LDS + 400 * 4;

constexpr int ATT_LDS = KV_HEAD + 8 * Q_TILE;
#define SWITCH 50
__global__ __launch_bounds__(768, 3) void k2_kernel(const u16* __restrict__ wo, const u16* __restrict__ w_in, const u16* __restrict__ w_out, const u16* __restrict__ w_sp, char* __restrict__ ws) {
  extern __shared__ __attribute__((aligned(16))) char lds[];
  TS(2, 0); TS(2, 6);
  const char* kvimg = ws + WS_KV; const char* qimg = ws + WS_Q; u16* o = (u16*)(ws + WS_O);
  const int tid = threadIdx.x, lane = tid & 63; const int w = __builtin_amdgcn_readfirstlane(tid >> 6);
  const int b = blockIdx.x, xcd = b & 7, idx = b >> 3, h = xcd * 4 + (idx >> 3), qb = idx & 7;
  const char* kvg = kvimg + (size_t)h * KV_HEAD + lane * 16;
  if (w < 8) {
    const char* qg = qimg + (size_t)h * Q_HEAD + (qb * 8 + w) * Q_TILE + lane * 16;
    char* qtile = lds + KV_HEAD + w * Q_TILE;
    glds16(qg, qtile);
    if (lane < 8) glds16(qg + 1024, qtile + 1024);
  }
  glds16(kvg + w * 1024, lds + w * 1024); glds16(kvg + (w + 12) * 1024, lds + (w + 12) * 1024);
  WAITV(0);
  BAR();
  if (w >= 8) {
    const int lw = w - 8;
    __builtin_amdgcn_s_setprio(3);
#pragma unroll 1
    for (int p = 24 + lw; p < 150; p += 4) glds16(kvg + p * 1024, lds + p * 1024);
    WAITV(0);
    BAR();
    {
      const u32x4* wp = (const u32x4*)w_sp + (size_t)(b * 4 + lw) * 512 + lane;
      u32x4 acc = wp[0];
#pragma unroll
      for (int i = 1; i < 8; ++i) acc ^= wp[i * 64];
      if (acc[0] == 0x12345678u && acc[1] == 0x9abcdef0u && acc[2] == 0x0fedcba9u && acc[3] == 0x87654321u) ((unsigned*)(ws + WS_DBG))[0] = 1;
    }
    const int gw = b * 4 + lw, c = lane >> 3, nb = lane & 7;
#pragma unroll 1
    for (int blk = gw; blk < 648 + 81 + 324; blk += 1024) {
      const u16* sp; int ld, kt, n0, kind;
      if (blk < 648) { kt = blk / 72; n0 = (blk - kt * 72) * 64; sp = w_in; ld = F; kind = 0; }
      else if (blk < 729) { const int q = blk - 648; kt = q / 9; n0 = (q - kt * 9) * 64; sp = wo; ld = D; kind = 1; }
      else { const int q = blk - 729; kt = q / 9; n0 = (q - kt * 9) * 64; sp = w_out; ld = D; kind = 2; }
      sp += (size_t)(kt * 64 + c * 8) * ld + n0 + nb * 8;
      u32x4 r[8];
#pragma unroll
      for (int j = 0; j < 8; ++j) r[j] = *(const u32x4*)(sp + (size_t)j * ld);
#pragma unroll
      for (int e = 0; e < 4; ++e) {
#pragma unroll
        for (int hi = 0; hi < 2; ++hi) {
          const unsigned sel = hi ? 0x07060302u : 0x05040100u;
          u32x4 o;
#pragma unroll
          for (int q = 0; q < 4; ++q) o[q] = __builtin_amdgcn_perm(r[2 * q + 1][e], r[2 * q][e], sel);
          const int n = n0 + nb * 8 + 2 * e + hi;
          char* dp;
          if (kind == 0) { const int z1 = n < FH, nn = z1 ? n : n - FH, t = nn / 144, row = nn - t * 144;
            dp = ws + WS_WIN + ((size_t)((z1 ? 16 : 0) + t) * 9 + kt) * 18432 + row * 128 + ((c ^ ((row >> 1) & 7)) << 4); }
          else { const int t = n / 96, row = n - t * 96;
            dp = ws + (kind == 1 ? WS_WO + ((size_t)t * 9 + kt) * 12288 : WS_WOUT + ((size_t)t * 36 + kt) * 12288) + row * 128 + ((c ^ ((row >> 1) & 7)) << 4); }
          st16_wt(ws, (size_t)(dp - ws), o);
        }
      }
    }
    return;
  }
  TS(2, 1);
  if (w >= 4) __builtin_amdgcn_s_setprio(1);
  const int r32 = lane & 31, hh = lane >> 5;
  lds_cptr L = (lds_cptr)lds;
  const bf16x8 qf0 = LDS_RD128(L + KV_HEAD + w * Q_TILE + lane * 16);
  u32x4 q1v = {0, 0, 0, 0}; { unsigned t = LDS_RD32(L + KV_HEAD + w * Q_TILE + 1024 + r32 * 4); q1v[0] = hh ? 0u : t; }
  const bf16x8 qf1 = __builtin_bit_cast(bf16x8, q1v);
  lds_cptr kp0 = L + lane * 16, kp1 = L + (hh ? KV_Z : KV_K1 + r32 * 4), vp = L + (r32 <= 18 ? KV_V + hh * 304 + r32 * 16 : KV_Z);
  f32x16 negm;
  {
    bf16x8 k0 = LDS_RD128(kp0); u32x4 k1 = {LDS_RD32(kp1), 0, 0, 0};
    f32x16 s = {}; s = MFMA32(k0, qf0, s); s = MFMA32(__builtin_bit_cast(bf16x8, k1), qf1, s);
    float m = s[0];
#pragma unroll
    for (int i = 1; i < 16; ++i) m = fmaxf(m, s[i]);
    auto rr = __builtin_amdgcn_permlane32_swap(__float_as_uint(m), __float_as_uint(m), false, false);
    m = fmaxf(__uint_as_float(rr[0]), __uint_as_float(rr[1]));
#pragma unroll
    for (int i = 0; i < 16; ++i) negm[i] = -m;
  }
  f32x16 O = {};
#define EX2(Sx, i) pk2(__builtin_amdgcn_exp2f(Sx[i]), __builtin_amdgcn_exp2f(Sx[(i) + 1]))
  bf16x8 k0, v0, v1; u32x4 k1 = {0, 0, 0, 0};
  f32x16 SA, SBc; u32x4 pA0, pA1, pB0, pB1;
#define STEP(Sc, Sn, Pc0, Pc1, Pp0, Pp1, koff, voff) do { \
    SB(); Sn = MFMA32(k0, qf0, negm); SB(); \
    Pc0[0] = EX2(Sc, 0); Pc0[1] = EX2(Sc, 2); PIN(Pc0); SB(); \
    Sn = MFMA32(__builtin_bit_cast(bf16x8, k1), qf1, Sn); SB(); \
    k0 = LDS_RD128(kp0 + (koff)); k1[0] = LDS_RD32(kp1 + (koff)); SB(); \
    Pc0[2] = EX2(Sc, 4); Pc0[3] = EX2(Sc, 6); PIN(Pc0); SB(); \
    O = MFMA32(v0, __builtin_bit_cast(bf16x8, Pp0), O); SB(); \
    Pc1[0] = EX2(Sc, 8); Pc1[1] = EX2(Sc, 10); PIN(Pc1); SB(); \
    O = MFMA32(v1, __builtin_bit_cast(bf16x8, Pp1), O); SB(); \
    v0 = LDS_RD128(vp + (voff)); v1 = LDS_RD128(vp + (voff) + KV_VS); SB(); \
    Pc1[2] = EX2(Sc, 12); Pc1[3] = EX2(Sc, 14); PIN(Pc1); SB(); \
  } while (0)
  k0 = LDS_RD128(kp0); k1[0] = LDS_RD32(kp1);
  SA = MFMA32(k0, qf0, negm); SA = MFMA32(__builtin_bit_cast(bf16x8, k1), qf1, SA);
  k0 = LDS_RD128(kp0 + KV_TILE); k1[0] = LDS_RD32(kp1 + KV_TILE);
  v0 = LDS_RD128(vp); v1 = LDS_RD128(vp + KV_VS);
  pB0 = u32x4{0, 0, 0, 0}; pB1 = u32x4{0, 0, 0, 0};
  int off = 0;
  for (int t = 0; t < 8; t += 2) {
    STEP(SA, SBc, pA0, pA1, pB0, pB1, off + 2 * KV_TILE, off);
    STEP(SBc, SA, pB0, pB1, pA0, pA1, off + 3 * KV_TILE, off + KV_TILE);
    off += 2 * KV_TILE;
  }
  BAR();
  for (int t = 8; t < 62; t += 2) {
    if (t == SWITCH) __builtin_amdgcn_s_setprio(0);
    STEP(SA, SBc, pA0, pA1, pB0, pB1, off + 2 * KV_TILE, off);
    STEP(SBc, SA, pB0, pB1, pA0, pA1, off + 3 * KV_TILE, off + KV_TILE);
    off += 2 * KV_TILE;
  }
  STEP(SA, SBc, pA0, pA1, pB0, pB1, off + KV_TILE, off);
  STEP(SBc, SA, pB0, pB1, pA0, pA1, off + KV_TILE, off + KV_TILE);
  O = MFMA32(v0, __builtin_bit_cast(bf16x8, pB0), O); O = MFMA32(v1, __builtin_bit_cast(bf16x8, pB1), O);
  TS(2, 2);
  {
    float mx = 0.f;
#pragma unroll
    for (int i = 0; i < 11; ++i) mx = fmaxf(mx, fabsf(O[i]));
    bool bad = !(mx < 1.2676506e30f);
#pragma unroll
    for (int i = 0; i < 11; ++i) bad |= (O[i] != O[i]);
    if (__any(bad)) {
      float m = -3.0e38f; O = f32x16{};
#pragma unroll 1
      for (int t = 0; t < 64; ++t) {
        const int of = t * KV_TILE;
        const bf16x8 a0 = LDS_RD128(kp0 + of); const u32x4 a1 = {LDS_RD32(kp1 + of), 0, 0, 0};
        f32x16 s = {}; s = MFMA32(a0, qf0, s); s = MFMA32(__builtin_bit_cast(bf16x8, a1), qf1, s);
        float tm = s[0];
#pragma unroll
        for (int i = 1; i < 16; ++i) tm = fmaxf(tm, s[i]);
        auto rr = __builtin_amdgcn_permlane32_swap(__float_as_uint(tm), __float_as_uint(tm), false, false);
        tm = fmaxf(__uint_as_float(rr[0]), __uint_as_float(rr[1]));
        const float mn = fmaxf(m, tm), al = __builtin_amdgcn_exp2f(m - mn); m = mn;
#pragma unroll
        for (int i = 0; i < 16; ++i) { O[i] *= al; s[i] -= mn; }
        u32x4 p0, p1;
#pragma unroll
        for (int i = 0; i < 4; ++i) { p0[i] = EX2(s, 2 * i); p1[i] = EX2(s, 8 + 2 * i); }
        O = MFMA32(LDS_RD128(vp + of), __builtin_bit_cast(bf16x8, p0), O); O = MFMA32(LDS_RD128(vp + of + KV_VS), __builtin_bit_cast(bf16x8, p1), O);
      }
    }
  }
  {
    float l = O[10];
    { auto rr = __builtin_amdgcn_permlane32_swap(__float_as_uint(l), __float_as_uint(l), false, false); l = __uint_as_float(rr[0]); }
    const float inv = 1.f / l;
    u16* orow = o + (size_t)(qb * 256 + w * 32 + r32) * D + h * DH;
    const unsigned a0 = pk2(O[0] * inv, O[1] * inv), a1_ = pk2(O[2] * inv, O[3] * inv), a2_ = pk2(O[4] * inv, O[5] * inv), a3 = pk2(O[6] * inv, O[7] * inv), a4 = pk2(O[8] * inv, O[9] * inv);
    unsigned* op = (unsigned*)(orow + hh * 4);
    op[0] = a0; op[1] = a1_; op[4] = a2_; op[5] = a3;
    if (hh == 0) op[8] = a4;
  }
  TS(2, 3); TS(2, 7);
}

constexpr int G_STG = 160 * 128, G_W = 8192, G_NS = 6, G_LDS = G_NS * G_STG;
template <int K, bool STATS, int KID>
DI void g_body(char* ws, char* lds, const u16* __restrict__ A, const char* __restrict__ W, const u16* __restrict__ res, u16* __restrict__ out, float* __restrict__ st2, int b) {
  constexpr int NT = K / 64;
  const int tid = threadIdx.x, lane = tid & 63; const int w = __builtin_amdgcn_readfirstlane(tid >> 6);
  const int xc = b & 7, idx = b >> 3, rt = xc * 4 + (idx & 3), ct = idx >> 2;
  const int m0 = rt * 64, n0 = ct * 96;
  TS(KID, 0); TS(KID, 6);
  const char* Xb = (const char*)(A + (size_t)m0 * K); const char* Wb = W + (size_t)ct * NT * 12288;
  auto src = [&](int p) { return p < 8 ? Xb + tn_off(p * 8 + (lane >> 3), lane, K * 2) : Wb + (p - 8) * 1024 + lane * 16; };
  first_tile<20, 12>(lds, w, src);
  if (w >= 4) {
    __builtin_amdgcn_s_setprio(2);
    loader_loop<NT, G_NS, G_STG, 20, 8, 8>(lds, w - 4, src, [&](int kt, long& ox, long& ow) { ox = (long)kt * 128; ow = (long)kt * 12288; }, NoPost());
    return;
  }
  const int wr = w & 1, wc = w >> 1, fr = lane & 15, fq = lane >> 4, sw = (fr >> 1) & 7;
  lds_cptr L = (lds_cptr)lds;
  lds_cptr x0 = L + (32 * wr + fr) * 128 + ((fq ^ sw) << 4), x1 = L + (32 * wr + fr) * 128 + (((4 + fq) ^ sw) << 4);
  lds_cptr w0 = L + G_W + (48 * wc + fr) * 128 + ((fq ^ sw) << 4), w1 = L + G_W + (48 * wc + fr) * 128 + (((4 + fq) ^ sw) << 4);
  f32x4 acc[3][2] = {};
  bf16x8 xa[2], wa[3], xb[2], wb_[3];
#define GRD(X, Wf, st, ks) do { X[0] = LDS_RD128(((ks) ? x1 : x0) + (st)); X[1] = LDS_RD128(((ks) ? x1 : x0) + (st) + 2048); \
    _Pragma("unroll") for (int n = 0; n < 3; ++n) Wf[n] = LDS_RD128(((ks) ? w1 : w0) + (st) + n * 2048); } while (0)
#define GSCHED() do { _Pragma("unroll") for (int i_ = 0; i_ < 5; ++i_) { SGB(0x8, 1); SGB(0x100, 1); } SGB(0x8, 1); } while (0)
#define GMM(X, Wf) do { _Pragma("unroll") for (int n = 0; n < 3; ++n) { acc[n][0] = MFMA16(Wf[n], X[0], acc[n][0]); acc[n][1] = MFMA16(Wf[n], X[1], acc[n][1]); } } while (0)
  WAITV(0); BAR();
  TS(KID, 1);
  GRD(xa, wa, 0, 0);
  u32x2 rv[2][3];
#pragma unroll
  for (int m = 0; m < 2; ++m)
#pragma unroll
    for (int n = 0; n < 3; ++n) rv[m][n] = *(const u32x2*)(res + (size_t)(m0 + 32 * wr + 16 * m + fr) * D + n0 + 48 * wc + 16 * n + 4 * fq);
  int st = 0;
#pragma unroll 1
  for (int kt = 0; kt < NT; ++kt) {
    const int stn = st + G_STG == G_LDS ? 0 : st + G_STG;
    GRD(xb, wb_, st, 1);
    GMM(xa, wa);
    GSCHED();
    SB(); BAR(); SB();
    GRD(xa, wa, stn, 0);
    GMM(xb, wb_);
    GSCHED();
    SB();
    st = stn;
  }
  TS(KID, 2);
#pragma unroll
  for (int m = 0; m < 2; ++m) {
    const int row = m0 + 32 * wr + 16 * m + fr;
    float sum = 0.f, ssq = 0.f;
#pragma unroll
    for (int n = 0; n < 3; ++n) {
      const int col = n0 + 48 * wc + 16 * n + 4 * fq;
      const u32x2 r = rv[m][n];
      u32x2 o; o[0] = pk2(bflo(r[0]) + acc[n][m][0], bfhi(r[0]) + acc[n][m][1]); o[1] = pk2(bflo(r[1]) + acc[n][m][2], bfhi(r[1]) + acc[n][m][3]);
      *(u32x2*)(out + (size_t)row * D + col) = o;
      if (STATS) { const float a = bflo(o[0]), bq = bfhi(o[0]), c = bflo(o[1]), d = bfhi(o[1]); sum += (a + bq) + (c + d); ssq += (a * a + bq * bq) + (c * c + d * d); }
    }
    if (STATS) {
      sum += __shfl_xor(sum, 16); sum += __shfl_xor(sum, 32); ssq += __shfl_xor(ssq, 16); ssq += __shfl_xor(ssq, 32);
      if (fq == 0) *(f32x2_t*)(st2 + ((size_t)row * 12 + ct * 2 + wc) * 2) = f32x2_t{sum, ssq};
    }
  }
  TS(KID, 3); TS(KID, 7);
}
__global__ __launch_bounds__(768, 3) void k3_kernel(const u16* __restrict__ x, const u16* __restrict__ wo, const u16* __restrict__ w_in, char* __restrict__ ws) {
  extern __shared__ __attribute__((aligned(16))) char lds[];
  const int b = blockIdx.x;
  if (b >= 192) {
    const int tid = threadIdx.x, c = tid % 9, rg = tid / 9;
    float* red = (float*)lds;
    if (rg < 48) {
      const char* p = (const char*)(w_in + (b - 192) * 72 + c * 8) + (size_t)rg * (F * 2);
      u32x4 v[12];
#pragma unroll
      for (int i = 0; i < 12; ++i) v[i] = *(const u32x4*)(p + (size_t)i * 48 * F * 2);
      float a[8] = {};
#pragma unroll
      for (int i = 0; i < 12; ++i)
#pragma unroll
        for (int e = 0; e < 4; ++e) { a[2 * e] += bflo(v[i][e]); a[2 * e + 1] += bfhi(v[i][e]); }
#pragma unroll
      for (int e = 0; e < 8; ++e) red[rg * 72 + c * 8 + e] = a[e];
    }
    __syncthreads();
    if (tid < 72) { float t = 0.f; for (int g = 0; g < 48; ++g) t += red[g * 72 + tid]; ((float*)(ws + WS_CS))[(b - 192) * 72 + tid] = t; }
    return;
  }
  g_body<D, true, 3>(ws, lds, (const u16*)(ws + WS_O), ws + WS_WO, x, (u16*)(ws + WS_H), (float*)(ws + WS_ST2), b);
}
__global__ __launch_bounds__(768, 3) void k6_kernel(const u16* __restrict__ w_out, u16* __restrict__ out, char* __restrict__ ws) {
  extern __shared__ __attribute__((aligned(16))) char lds[];
  g_body<FH, false, 0>(ws, lds, (const u16*)(ws + WS_U), ws + WS_WOUT, (const u16*)(ws + WS_H), out, nullptr, blockIdx.x);
}

constexpr unsigned XTAG = 0x9E3779B9u;
DI float dpp_sum16(float v) {
  v += __builtin_bit_cast(float, __builtin_amdgcn_mov_dpp(__builtin_bit_cast(int, v), 0xB1, 0xf, 0xf, true));
  v += __builtin_bit_cast(float, __builtin_amdgcn_mov_dpp(__builtin_bit_cast(int, v), 0x4E, 0xf, 0xf, true));
  v += __builtin_bit_cast(float, __builtin_amdgcn_mov_dpp(__builtin_bit_cast(int, v), 0x141, 0xf, 0xf, true));
  v += __builtin_bit_cast(float, __builtin_amdgcn_mov_dpp(__builtin_bit_cast(int, v), 0x140, 0xf, 0xf, true));
  return v;
}
constexpr int K4_STG = (128 + 288) * 128, K4_W2 = 16384, K4_W1 = 16384 + 18432, K4_NS = 3;
constexpr int K4_TAB = K4_NS * K4_STG;
constexpr int K4_PART = 40960, K4_ST2 = 45056;
constexpr int K4_LDS = K4_TAB + 544 * 4;
template <int NTL>
DI void k4_compute(char* lds, int lane, int w, int pi, int ft, char* ws) {
  const int wr = w & 3, nb0 = NTL == 5 ? 0 : 5, wc = NTL == 5 ? 0 : 1, fr = lane & 15, fq = lane >> 4, sw = (fr >> 1) & 7;
  lds_cptr L = (lds_cptr)lds;
  lds_cptr x0 = L + (32 * wr + fr) * 128 + ((fq ^ sw) << 4), x1 = L + (32 * wr + fr) * 128 + (((4 + fq) ^ sw) << 4);
  lds_cptr w0 = L + K4_W2 + (16 * nb0 + fr) * 128 + ((fq ^ sw) << 4), w1 = L + K4_W2 + (16 * nb0 + fr) * 128 + (((4 + fq) ^ sw) << 4);
  f32x4 acc2[NTL][2] = {}, acc1[NTL][2] = {};
  bf16x8 xa[2], xb[2], wa[NTL], wb[NTL];
#define RDX(X, P, st) do { X[0] = LDS_RD128(P + (st)); X[1] = LDS_RD128(P + (st) + 2048); } while (0)
#define RDW(Wf, P, st) do { _Pragma("unroll") for (int n = 0; n < NTL; ++n) Wf[n] = LDS_RD128(P + (st) + n * 2048); } while (0)
#define MM2(X, Wf) do { _Pragma("unroll") for (int n = 0; n < NTL; ++n) { acc2[n][0] = MFMA16(X[0], Wf[n], acc2[n][0]); acc2[n][1] = MFMA16(X[1], Wf[n], acc2[n][1]); } } while (0)
#define MM1(X, Wf) do { _Pragma("unroll") for (int n = 0; n < NTL; ++n) { acc1[n][0] = MFMA16(Wf[n], X[0], acc1[n][0]); acc1[n][1] = MFMA16(Wf[n], X[1], acc1[n][1]); } } while (0)
#define SCH(NR) do { _Pragma("unroll") for (int i_ = 0; i_ < (NR); ++i_) { SGB(0x8, 1); SGB(0x100, 1); } SGB(0x8, 2 * NTL - (NR)); SB(); } while (0)
  WAITV(0); WAITL0();
  if (NTL == 4) BAR();
  int st = 0;
#pragma unroll 1
  for (int kt = 0; kt < 9; ++kt) {
    BAR(); SB();
    if (kt == 0) TS(4, 1);
    RDX(xa, x0, st); RDW(wa, w0, st); SB();
    RDW(wb, w0, st + 18432); MM2(xa, wa); SCH(NTL);
    RDX(xb, x1, st); RDW(wa, w1, st); MM1(xa, wb); SCH(NTL + 2);
    BAR(); SB();
    RDW(wb, w1, st + 18432); MM2(xb, wa); SCH(NTL);
    MM1(xb, wb); SB();
    st = st + K4_STG == K4_TAB ? 0 : st + K4_STG;
  }
  if (NTL == 5) BAR();
  TS(4, 4);
  int lz = lane_id();
  const float* tab = (const float*)(lds + K4_TAB); const float* rs = tab; const float* mr = tab + 128; const float* cs = tab + 256;
  float* part = (float*)(lds + K4_PART); const float* st2 = (const float*)(lds + K4_ST2);
  u32x2 z2p[NTL][2];
  {
    const int fr = lz & 15, fq = lz >> 4;
#pragma unroll
    for (int m = 0; m < 2; ++m) {
      const f32x4 r4 = *(const f32x4*)(rs + 32 * wr + 16 * m + 4 * fq), m4 = *(const f32x4*)(mr + 32 * wr + 16 * m + 4 * fq);
      f32x4 su = {0, 0, 0, 0}, sq = {0, 0, 0, 0};
#pragma unroll
      for (int n = 0; n < NTL; ++n) {
        const float c = cs[16 * (nb0 + n) + fr];
        f32x4 g;
#pragma unroll
        for (int r = 0; r < 4; ++r) { g[r] = gelu_t(acc2[n][m][r] * r4[r] - m4[r] * c); su[r] += g[r]; sq[r] = __builtin_fmaf(g[r], g[r], sq[r]); }
        z2p[n][m][0] = pk2(g[0], g[1]); z2p[n][m][1] = pk2(g[2], g[3]);
      }
#pragma unroll
      for (int r = 0; r < 4; ++r) { su[r] = dpp_sum16(su[r]); sq[r] = dpp_sum16(sq[r]); }
      if (fr == 0) {
#pragma unroll
        for (int r = 0; r < 4; ++r) *(f32x2_t*)(part + (wc * 128 + 32 * wr + 16 * m + 4 * fq + r) * 2) = f32x2_t{su[r], sq[r]};
      }
    }
  }
  WAITL0(); BAR();
  if (wc == 0 && lz < 32) {
    const int row = 32 * wr + lz; const f32x2_t p0 = *(const f32x2_t*)(part + row * 2), p1 = *(const f32x2_t*)(part + (128 + row) * 2);
    const u32x4 g = {__float_as_uint(p0[0] + p1[0]), XTAG, __float_as_uint(p0[1] + p1[1]), XTAG};
    st16_wt(ws, WS_XS + (size_t)(((pi * 16 + ft) * 128 + row) * 16), g);
  }
  TS(4, 2);
  {
    const int fr = lz & 15, fq = lz >> 4;
    const size_t zb = WS_Z1 + ((size_t)(pi * 16 + ft) * 4 + wr) * (9 * 1024) + lz * 16;
    const float r0 = rs[32 * wr + fr], mm0 = mr[32 * wr + fr], r1 = rs[32 * wr + 16 + fr], mm1 = mr[32 * wr + 16 + fr];
#pragma unroll
    for (int n = 0; n < NTL; ++n) {
      const f32x4 c4 = *(const f32x4*)(cs + 144 + 16 * (nb0 + n) + 4 * fq);
      float g0[4], g1[4];
#pragma unroll
      for (int r = 0; r < 4; ++r) { g0[r] = gelu_t(acc1[n][0][r] * r0 - mm0 * c4[r]); g1[r] = gelu_t(acc1[n][1][r] * r1 - mm1 * c4[r]); }
      const u32x4 o = {pk2(g0[0], g0[1]), pk2(g0[2], g0[3]), pk2(g1[0], g1[1]), pk2(g1[2], g1[3])};
      st16_wt(ws, zb + (nb0 + n) * 1024, o);
    }
  }
  TS(4, 5);
  __syncthreads();
  asm volatile("" : "+v"(lz));
#pragma unroll
  for (int m = 0; m < 2; ++m) {
    const int fr = lz & 15, fq = lz >> 4;
    const int sl = 32 * wr + 16 * m + 4 * fq;
    const f32x4 sa = *(const f32x4*)(st2 + sl * 2), sb = *(const f32x4*)(st2 + sl * 2 + 4);
    const float mu[4] = {sa[0], sa[2], sb[0], sb[2]}, rr[4] = {sa[1], sa[3], sb[1], sb[3]};
#pragma unroll
    for (int n = 0; n < NTL; ++n) {
      const int f = 16 * (nb0 + n) + fr; const u32x2 p = z2p[n][m];
      u32x2 o; o[0] = pk2(bflo(p[0]) * rr[0] - mu[0], bfhi(p[0]) * rr[1] - mu[1]); o[1] = pk2(bflo(p[1]) * rr[2] - mu[2], bfhi(p[1]) * rr[3] - mu[3]);
      *(u32x2*)(lds + (sl >> 6) * 18432 + f * 128 + ((((sl & 63) >> 3) ^ ((f >> 1) & 7)) << 4) + (sl & 4) * 2) = o;
    }
  }
}
__global__ __launch_bounds__(768, 3) void k4_kernel(char* __restrict__ ws) {
  extern __shared__ __attribute__((aligned(16))) char lds[];
  const int tid = threadIdx.x, lane = tid & 63; const int w = __builtin_amdgcn_readfirstlane(tid >> 6);
  const int b = blockIdx.x, xc = b & 7, idx = b >> 3;
  const int pi = (xc & 1) * 8 + (idx & 7), ft = (xc >> 1) * 4 + (idx >> 3);
  float* tab = (float*)(lds + K4_TAB);
  TS(4, 0); TS(4, 6);
  const char* Xb = ws + WS_H + (size_t)pi * 128 * D * 2; const char* W2b = ws + WS_WIN + (size_t)ft * 9 * 18432; const char* W1b = W2b + (size_t)16 * 9 * 18432;
  auto src = [&](int p) { return p < 16 ? Xb + tn_off(p * 8 + (lane >> 3), lane, D * 2) : (p < 34 ? W2b + (p - 16) * 1024 : W1b + (p - 34) * 1024) + lane * 16; };
  first_tile<52, 12>(lds, w, src);
  if (w >= 8) {
    const int lw = w - 8;
    __builtin_amdgcn_s_setprio(2);
    const char* base[13];
#pragma unroll
    for (int j = 0; j < 13; ++j) base[j] = src(lw + 4 * j);
    auto issue = [&](int kt, int stage) {
#pragma unroll
      for (int j = 0; j < 13; ++j) glds16(base[j] + (j < 4 ? (long)kt * 128 : (long)kt * 18432), lds + stage * K4_STG + (lw + 4 * j) * 1024);
    };
    issue(1, 1);
    int stg = 2;
#pragma unroll 1
    for (int kt = 0; kt < 7; ++kt) { WAITV(13); BAR(); BAR(); issue(kt + 2, stg); stg = stg == 2 ? 0 : stg + 1; }
    WAITV(13); BAR(); BAR();
    WAITV(0); BAR(); BAR();
    BAR();
    BAR();
    const int row = 32 * lw + (lane >> 1);
    const __amdgpu_buffer_rsrc_t rs_ = __builtin_amdgcn_make_buffer_rsrc((void*)(ws + WS_XS), 0, 2048 * 16 * 16, 0x00020000);
    const unsigned vo = (unsigned)(((pi * 16 + (lane & 1) * 8) * 128 + row) * 16);
    float sum = 0.f, ssq = 0.f;
    for (int spin = 0; spin < (1 << 20); ++spin) {
      bool ok = true; sum = 0.f; ssq = 0.f;
      u32x4 g[8];
#pragma unroll
      for (int t = 0; t < 8; ++t) g[t] = __builtin_amdgcn_raw_buffer_load_b128(rs_, vo + t * 2048, 0, 16);
#pragma unroll
      for (int t = 0; t < 8; ++t) { ok &= g[t][1] == XTAG && g[t][3] == XTAG; sum += __uint_as_float(g[t][0]); ssq += __uint_as_float(g[t][2]); }
      if (__all(ok)) break;
      __builtin_amdgcn_s_sleep(8);
    }
    sum += __shfl_xor(sum, 1); ssq += __shfl_xor(ssq, 1);
    const float mean = sum * (1.f / FH), var = fmaxf(ssq * (1.f / FH) - mean * mean, 0.f), rstd = rsqrtf(var + 1e-5f);
    if ((lane & 1) == 0) *(f32x2_t*)(lds + K4_ST2 + row * 8) = f32x2_t{mean * rstd, rstd};
    __syncthreads();
  } else {
    if (tid < 128) {
      const f32x4* p = (const f32x4*)(ws + WS_ST2) + (size_t)(pi * 128 + tid) * 6;
      float sum = 0.f, ssq = 0.f;
#pragma unroll
      for (int t = 0; t < 6; ++t) { const f32x4 v = p[t]; sum += v[0] + v[2]; ssq += v[1] + v[3]; }
      const float mean = sum * (1.f / D), var = fmaxf(ssq * (1.f / D) - mean * mean, 0.f), rstd = rsqrtf(var + 1e-5f);
      tab[tid] = rstd; tab[128 + tid] = mean * rstd;
    } else if (tid < 416) {
      const int c = tid - 128;
      tab[256 + c] = ((const float*)(ws + WS_CS))[c < 144 ? FH + ft * 144 + c : ft * 144 + c - 144];
    }
    if (w < 4) k4_compute<5>(lds, lane, w, pi, ft, ws); else k4_compute<4>(lds, lane, w, pi, ft, ws);
  }
  __syncthreads();
  {
    const size_t dst = WS_Z2T + ((size_t)ft * 32 + pi * 2) * 18432;
    for (int i = w * 64 + lane_id(); i < 2304; i += 768) st16_wt(ws, dst + i * 16, *(const u32x4*)(lds + i * 16));
  }
  TS(4, 3); TS(4, 7);
}

template <int NTL>
DI void k5_compute(char* lds, int lane, int w, int tt, int ft, const u16* __restrict__ bsp, char* ws) {
  const int wr = w & 3, nb0 = NTL == 5 ? 0 : 5, fr = lane & 15, fq = lane >> 4;
  const C8Addr a = c8_addr<false>(lds, lane, wr, nb0);
  f32x4 acc[NTL][2] = {};
  C8Frag<NTL> f0, f1;
  WAITV(0); BAR();
  TS(5, 1);
  c8_read<NTL, false>(f0, a, 0, 0);
  unsigned z1v[4 * NTL];
  { const char* zb = ws + WS_Z1 + ((size_t)(tt * 16 + ft) * 4 + wr) * (9 * 1024) + lane * 16;
#pragma unroll
    for (int n = 0; n < NTL; ++n) { const u32x4 v = *(const u32x4*)(zb + (nb0 + n) * 1024); z1v[4 * n] = v[0]; z1v[4 * n + 1] = v[1]; z1v[4 * n + 2] = v[2]; z1v[4 * n + 3] = v[3]; } }
  float bb[2];
#pragma unroll
  for (int m = 0; m < 2; ++m) bb[m] = bf2f(bsp[tt * 128 + 32 * wr + 16 * m + fr]);
  int st = 0;
#define C5_ITER(SIDE0, SIDE1) do { const int stn = st + C8_STG == C8_LDS ? 0 : st + C8_STG; \
    c8_read<NTL, false>(f1, a, st, 1); c8_mma<NTL, true>(acc, f0); c8_sched<NTL, false>(); SB(); SIDE0; SB(); BAR(); SB(); \
    c8_read<NTL, false>(f0, a, stn, 0); c8_mma<NTL, true>(acc, f1); c8_sched<NTL, false>(); SB(); SIDE1; SB(); st = stn; } while (0)
#pragma unroll 1
  for (int kt = 0; kt < 32; ++kt) C5_ITER((void)0, (void)0);
  TS(5, 2);
  __syncthreads();
#pragma unroll
  for (int m = 0; m < 2; ++m) {
    const int t = 32 * wr + 16 * m + fr;
#pragma unroll
    for (int n = 0; n < NTL; ++n) {
      const int f = 16 * (nb0 + n) + 4 * fq; const unsigned z0 = z1v[4 * n + 2 * m], z1 = z1v[4 * n + 2 * m + 1];
      u32x2 o; o[0] = pk2(bflo(z0) * (acc[n][m][0] + bb[m]), bfhi(z0) * (acc[n][m][1] + bb[m]));
      o[1] = pk2(bflo(z1) * (acc[n][m][2] + bb[m]), bfhi(z1) * (acc[n][m][3] + bb[m]));
      *(u32x2*)(lds + t * EP_PITCH + f * 2) = o;
    }
  }
  __syncthreads();
  {
    const size_t ub = WS_U + ((size_t)tt * 128 * FH + ft * 144) * 2;
    for (int i = w * 64 + lane; i < 128 * 18; i += 512) { const int t = i / 18, c = i - t * 18; st16_wt(ws, ub + (size_t)t * FH * 2 + c * 16, *(const u32x4*)(lds + t * EP_PITCH + c * 16)); }
  }
}
__global__ __launch_bounds__(768, 3) void k5_kernel(const u16* __restrict__ wsp, const u16* __restrict__ bsp, char* __restrict__ ws) {
  extern __shared__ __attribute__((aligned(16))) char lds[];
  TS(5, 0); TS(5, 6);
  const int tid = threadIdx.x, lane = tid & 63; const int w = __builtin_amdgcn_readfirstlane(tid >> 6);
  const int b = blockIdx.x, xc = b & 7, idx = b >> 3;
  const int tt = (xc & 1) * 8 + (idx & 7), ft = (xc >> 1) * 4 + (idx >> 3);
  const char* Xb = (const char*)(wsp + (size_t)tt * 128 * S); const char* Wb = ws + WS_Z2T + (size_t)ft * 32 * 18432;
  auto src = [&](int p) { return p < 16 ? Xb + tn_off(p * 8 + (lane >> 3), lane, S * 2) : Wb + (p - 16) * 1024 + lane * 16; };
  first_tile<34, 12>(lds, w, src);
  if (w >= 8) {
    __builtin_amdgcn_s_setprio(2);
    loader_loop<32, C8_NS, C8_STG, 34, 16, 4>(lds, w - 8, src, [&](int kt, long& ox, long& ow) { ox = (long)kt * 128; ow = (long)kt * 18432; }, NoPost());
    return;
  }
  if (w < 4) k5_compute<5>(lds, lane, w, tt, ft, bsp, ws); else k5_compute<4>(lds, lane, w, tt, ft, bsp, ws);
  TS(5, 3); TS(5, 7);
}

extern "C" void kernel_launch(void* const* d_in, const int* in_sizes, int n_in, void* d_out, int out_size, void* d_ws, size_t ws_size, hipStream_t stream) {
  const u16* x = (const u16*)d_in[0]; const u16* wqkv = (const u16*)d_in[1]; const u16* wo = (const u16*)d_in[2];
  const u16* w_in = (const u16*)d_in[4]; const u16* w_sp = (const u16*)d_in[5]; const u16* b_sp = (const u16*)d_in[6]; const u16* w_out = (const u16*)d_in[7];
  u16* out = (u16*)d_out; char* ws = (char*)d_ws;
  static bool attr = false;
  if (!attr) {
    (void)hipFuncSetAttribute((const void*)k1_kernel, hipFuncAttributeMaxDynamicSharedMemorySize, K1_LDS);
    (void)hipFuncSetAttribute((const void*)k2_kernel, hipFuncAttributeMaxDynamicSharedMemorySize, ATT_LDS);
    (void)hipFuncSetAttribute((const void*)k3_kernel, hipFuncAttributeMaxDynamicSharedMemorySize, G_LDS);
    (void)hipFuncSetAttribute((const void*)k4_kernel, hipFuncAttributeMaxDynamicSharedMemorySize, K4_LDS);
    (void)hipFuncSetAttribute((const void*)k5_kernel, hipFuncAttributeMaxDynamicSharedMemorySize, C8_LDS);
    (void)hipFuncSetAttribute((const void*)k6_kernel, hipFuncAttributeMaxDynamicSharedMemorySize, G_LDS);
    attr = true;
  }
  k1_kernel<<<256, K1_THREADS, K1_LDS, stream>>>(x, wqkv, ws);
  k2_kernel<<<256, 768, ATT_LDS, stream>>>(wo, w_in, w_out, w_sp, ws);
  k3_kernel<<<256, 768, G_LDS, stream>>>(x, wo, w_in, ws);
  k4_kernel<<<256, 768, K4_LDS, stream>>>(ws);
  k5_kernel<<<256, 768, C8_LDS, stream>>>(w_sp, b_sp, ws);
  k6_kernel<<<192, 768, G_LDS, stream>>>(w_out, out, ws);
}
```

```cpp
#include <hip/hip_runtime.h>
#include <hip/hip_bf16.h>
#include <stdint.h>
typedef unsigned short u16;
using bf16x8 = __attribute__((ext_vector_type(8))) short;
using s16x4 = __attribute__((ext_vector_type(4))) short;
using f32x4 = __attribute__((ext_vector_type(4))) float;
using f32x16 = __attribute__((ext_vector_type(16))) float;
using u32x4 = __attribute__((ext_vector_type(4))) unsigned;
using u32x2 = __attribute__((ext_vector_type(2))) unsigned;
using u32x32 = __attribute__((ext_vector_type(32))) unsigned;
typedef __bf16 bf16x2_t __attribute__((ext_vector_type(2)));
typedef float f32x2_t __attribute__((ext_vector_type(2)));
constexpr int S = 2048, D = 576, H = 32, DH = 18, F = 4608, FH = 2304;
#define DI __device__ __forceinline__
#define LAS __attribute__((address_space(3)))
#define GAS __attribute__((address_space(1)))
DI int lane_id() { int l = __builtin_amdgcn_mbcnt_hi(~0u, __builtin_amdgcn_mbcnt_lo(~0u, 0u)); asm volatile("" : "+v"(l)); return l; }
DI float bf2f(u16 x) { return __uint_as_float((unsigned)x << 16); }
DI float bflo(unsigned x) { return __uint_as_float(x << 16); }
DI float bfhi(unsigned x) { return __uint_as_float(x & 0xffff0000u); }
DI unsigned pk2(float lo, float hi) { f32x2_t v = {lo, hi}; return __builtin_bit_cast(unsigned, __builtin_convertvector(v, bf16x2_t)); }
#define MFMA32(a, b, c) __builtin_amdgcn_mfma_f32_32x32x16_bf16((a), (b), (c), 0, 0, 0)
#define MFMA16(a, b, c) __builtin_amdgcn_mfma_f32_16x16x32_bf16((a), (b), (c), 0, 0, 0)
#define SB() __builtin_amdgcn_sched_barrier(0)
#define SGB(mask, n) __builtin_amdgcn_sched_group_barrier(mask, n, 0)
#define PIN(x) asm volatile("" : "+v"(x))
#define BAR() __builtin_amdgcn_s_barrier()
#define WAITV(n) asm volatile("s_waitcnt vmcnt(%0)" :: "i"(n) : "memory")
#define WAITL0() asm volatile("s_waitcnt lgkmcnt(0)" ::: "memory")
DI void glds16(const void* g, void* l) { __builtin_amdgcn_global_load_lds((const GAS unsigned*)g, (LAS unsigned*)l, 16, 0, 0); }
DI void st16_wt(char* ws, size_t off, u32x4 v) {
  const __amdgpu_buffer_rsrc_t r = __builtin_amdgcn_make_buffer_rsrc((void*)ws, 0, 128 << 20, 0x00020000);
  __builtin_amdgcn_raw_buffer_store_b128(v, r, (unsigned)off, 0, 16);
}
typedef LAS const char* lds_cptr;
typedef LAS char* lds_ptr;
#define LDS_RD128(p) (*(const LAS bf16x8*)(p))
#define LDS_RD32(p) (*(const LAS unsigned*)(p))
typedef short v4i16_t __attribute__((ext_vector_type(4)));
DI s16x4 lds_tr(lds_cptr p) { return __builtin_bit_cast(s16x4, __builtin_amdgcn_ds_read_tr16_b64_v4i16((LAS v4i16_t*)p)); }
DI bf16x8 lds_tr2(lds_cptr p0, lds_cptr p1) { const s16x4 a = lds_tr(p0), b = lds_tr(p1); return __builtin_shufflevector(a, b, 0, 1, 2, 3, 4, 5, 6, 7); }

#define TIMING 0
#define TS(k, i)
#define TSTAMP(v)
#define RSTAMP(v)

constexpr int KV_TILE = 2400, KV_K1 = 1024, KV_V = 1152, KV_VS = 624, KV_Z = KV_V + 608, KV_HEAD = 64 * KV_TILE;
constexpr int Q_TILE = 1152, Q_HEAD = 64 * Q_TILE;
constexpr size_t WS_KV = 0;
constexpr size_t WS_Q = 5u << 20;
constexpr size_t WS_O = 8u << 20;
constexpr size_t WS_H = 11u << 20;
constexpr size_t WS_ST2 = 14u << 20;
constexpr size_t WS_CS = 15u << 20;
constexpr size_t WS_XS = 16u << 20;
constexpr size_t WS_Z2T = 17u << 20;
constexpr size_t WS_Z1 = 27u << 20;
constexpr size_t WS_U = 37u << 20;
constexpr size_t WS_WIN = 47u << 20;
constexpr size_t WS_WO = 53u << 20;
constexpr size_t WS_WOUT = 54u << 20;
constexpr size_t WS_DBG = 64u << 20;

template <int NP, int NW, class SRC>
DI void first_tile(char* lds, int w, SRC src) {
#pragma unroll
  for (int j = 0; j < (NP + NW - 1) / NW; ++j) { const int p = w + NW * j; if (p < NP) glds16(src(p), lds + p * 1024); }
}
template <int NT, int NS, int STG, int NP, int NPX, int NLD, class SRC, class OFF, class POST>
DI void loader_loop(char* lds, int lw, SRC src, OFF off, POST post) {
  constexpr int PPW = (NP + NLD - 1) / NLD, PMIN = NP / NLD;
  static_assert((NS - 1) * PPW + 4 <= 63, "vmcnt");
  static_assert(NPX % NLD == 0, "X pieces");
  const char* base[PPW];
#pragma unroll
  for (int j = 0; j < PPW; ++j) { const int p = lw + NLD * j; base[j] = src(p < NP ? p : NP - 1); }
  auto issue = [&](int kt, int stage) {
    long ox, ow; off(kt, ox, ow);
#pragma unroll
    for (int j = 0; j < PPW; ++j) if (j < PMIN || lw + NLD * j < NP) glds16(base[j] + (j < NPX / NLD ? ox : ow), lds + stage * STG + (lw + NLD * j) * 1024);
  };
  issue(1, 1);
  WAITV(PMIN);
  BAR();
#pragma unroll
  for (int t = 2; t < NS - 1; ++t) issue(t, t);
  post(0);
  int stg = NS - 1, pst = 1;
#pragma unroll 1
  for (int kt = 0; kt < NT - (NS - 1); ++kt) {
    WAITV((NS - 3) * PMIN);
    BAR();
    issue(kt + NS - 1, stg); stg = stg + 1 == NS ? 0 : stg + 1;
    post(pst); pst = pst + 1 == NS ? 0 : pst + 1;
  }
#pragma unroll
  for (int r = NS - 2; r >= 0; --r) {
    WAITV((r - 1 > 0 ? r - 1 : 0) * PMIN); BAR();
    if (r > 0) { post(pst); pst = pst + 1 == NS ? 0 : pst + 1; }
  }
}
struct NoPost { DI void operator()(int) const {} };
DI unsigned tn_off(int row, int lane, int ld_bytes) { return (unsigned)row * ld_bytes + ((((lane & 7) ^ ((row >> 1) & 7))) << 4); }
template <int BN> DI unsigned kn_off(int p, int lane, int ld_bytes) {
  constexpr int CPR = BN / 8;
  const int c = 64 * p + lane, kp = c / CPR, cc = c - kp * CPR, odd = (kp >> 3) & 1, k = kp ^ (odd << 2);
  int cs = cc;
  if (BN == 96) { int blk = (cc >> 1) - odd; if (blk < 0) blk += 6; cs = blk * 2 + (cc & 1); }
  return (unsigned)k * ld_bytes + (unsigned)cs * 16;
}

DI float gelu_t(float x) {
  const float c1 = -2.f * 0.7978845608028654f * 1.4426950408889634f, c2 = c1 * 0.044715f;
  const float e = __builtin_amdgcn_exp2f(x * __builtin_fmaf(x * x, c2, c1));
  return x * __builtin_amdgcn_rcpf(1.f + e);
}

constexpr int C8_STG = 272 * 128, C8_W = 16384, C8_NS = 4, C8_LDS = C8_NS * C8_STG;
struct C8Addr { lds_cptr x0, x1, w0, w1; };
template <bool BKN> DI C8Addr c8_addr(char* lds, int lane, int wr, int nb0) {
  lds_cptr L = (lds_cptr)lds; const int fr = lane & 15, fq = lane >> 4, sw = (fr >> 1) & 7; C8Addr a;
  a.x0 = L + (32 * wr + fr) * 128 + ((fq ^ sw) << 4); a.x1 = L + (32 * wr + fr) * 128 + (((4 + fq) ^ sw) << 4);
  if (BKN) { const int q = fr >> 2, p4 = lane & 3, b = C8_W + (8 * fq + q) * 288 + nb0 * 32 + p4 * 8;
             a.w0 = L + b + (fq & 1) * 1152; a.w1 = L + b + ((fq & 1) ^ 1) * 1152; }
  else { a.w0 = L + C8_W + (16 * nb0 + fr) * 128 + ((fq ^ sw) << 4); a.w1 = L + C8_W + (16 * nb0 + fr) * 128 + (((4 + fq) ^ sw) << 4); }
  return a;
}
template <int NTL> struct C8Frag { bf16x8 x[2], w[NTL]; };
template <int NTL, bool BKN> DI void c8_read(C8Frag<NTL>& f, const C8Addr& a, int st, int ks) {
  f.x[0] = LDS_RD128((ks ? a.x1 : a.x0) + st); f.x[1] = LDS_RD128((ks ? a.x1 : a.x0) + st + 2048);
#pragma unroll
  for (int n = 0; n < NTL; ++n) {
    if (BKN) f.w[n] = lds_tr2(a.w0 + st + ks * 9216 + n * 32, a.w1 + st + ks * 9216 + n * 32);
    else f.w[n] = LDS_RD128((ks ? a.w1 : a.w0) + st + n * 2048);
  }
}
template <int NTL, bool WX> DI void c8_mma(f32x4 (&acc)[NTL][2], const C8Frag<NTL>& f) {
#pragma unroll
  for (int n = 0; n < NTL; ++n) {
#pragma unroll
    for (int m = 0; m < 2; ++m) acc[n][m] = WX ? MFMA16(f.w[n], f.x[m], acc[n][m]) : MFMA16(f.x[m], f.w[n], acc[n][m]);
  }
}
template <int NTL, bool BKN> DI void c8_sched() {
  constexpr int NR = 2 + (BKN ? 2 * NTL : NTL), NM = 2 * NTL;
  if (NR <= NM) {
#pragma unroll
    for (int i = 0; i < NR; ++i) { SGB(0x8, 1); SGB(0x100, 1); }
    SGB(0x8, NM - NR);
  } else {
#pragma unroll
    for (int i = 0; i < NM; ++i) { SGB(0x8, 1); SGB(0x100, 1); if (i < NR - NM) SGB(0x100, 1); }
  }
}

constexpr int K1_THREADS = 768;
constexpr int EP_PITCH = 304;
template <int WC, bool WX>
DI void k1_compute(char* lds, int lane, int w, f32x4 (&acc)[WC == 0 ? 3 : 2][4], float* tabs, char* ws) {
  constexpr int NTL = WC == 0 ? 3 : 2, NB0 = WC == 0 ? 0 : 2 * WC + 1, NST = WC == 0 ? 0 : (WC == 1 ? 2 : 1), M0 = WC == 1 ? 0 : WC - 1, M1 = 3;
  const int wr = w & 1, fr = lane & 15, fq = lane >> 4, sw = (fr >> 1) & 7;
  lds_cptr L = (lds_cptr)lds;
  lds_cptr x0 = L + (64 * wr + fr) * 128 + ((fq ^ sw) << 4), x1 = L + (64 * wr + fr) * 128 + (((4 + fq) ^ sw) << 4);
  const int wb = C8_W + (8 * fq + (fr >> 2)) * 288 + NB0 * 32 + (lane & 3) * 8;
  lds_cptr w0 = L + wb + (fq & 1) * 1152, w1 = L + wb + ((fq & 1) ^ 1) * 1152;
  const u32x4 onesv = {0x3f803f80u, 0x3f803f80u, 0x3f803f80u, 0x3f803f80u}; const bf16x8 ones = __builtin_bit_cast(bf16x8, onesv);
  bf16x8 xa[4], wa[NTL], xb[4], wb_[NTL];
  f32x4 cs[NTL] = {}, rsum[2] = {}, rsq[2] = {};
#define K1RD(X, Wf, st, ks) do { _Pragma("unroll") for (int n = 0; n < NTL; ++n) Wf[n] = lds_tr2(w0 + (st) + (ks) * 9216 + n * 32, w1 + (st) + (ks) * 9216 + n * 32); \
    _Pragma("unroll") for (int m = 0; m < 4; ++m) X[m] = LDS_RD128(((ks) ? x1 : x0) + (st) + m * 2048); } while (0)
#define K1MM(X, Wf) do { _Pragma("unroll") for (int n = 0; n < NTL; ++n) { _Pragma("unroll") for (int m = 0; m < 4; ++m) acc[n][m] = WX ? MFMA16(Wf[n], X[m], acc[n][m]) : MFMA16(X[m], Wf[n], acc[n][m]); \
    cs[n] = MFMA16(ones, Wf[n], cs[n]); } \
    if (NST >= 1) { rsum[0] = MFMA16(X[M0], ones, rsum[0]); rsq[0] = MFMA16(X[M0], X[M0], rsq[0]); } \
    if (NST >= 2) { rsum[1] = MFMA16(X[M1], ones, rsum[1]); rsq[1] = MFMA16(X[M1], X[M1], rsq[1]); } } while (0)
#define K1SCHED() do { _Pragma("unroll") for (int i_ = 0; i_ < 2 * NTL + 4; ++i_) { SGB(0x8, 1); SGB(0x100, 1); } SGB(0x8, 5 * NTL + 2 * NST - (2 * NTL + 4)); } while (0)
  WAITV(0); BAR();
  TS(1, 1);
  K1RD(xa, wa, 0, 0);
  int st = 0;
#pragma unroll 1
  for (int kt = 0; kt < 9; ++kt) {
    const int stn = st + C8_STG == C8_LDS ? 0 : st + C8_STG;
    K1RD(xb, wb_, st, 1); K1MM(xa, wa); K1SCHED();
    SB(); BAR(); SB();
    K1RD(xa, wa, stn, 0); K1MM(xb, wb_); K1SCHED();
    SB();
    st = stn;
  }
  TS(1, 2);
  const int l2 = lane_id(), r2 = l2 & 15, q2 = l2 >> 4;
  if (l2 < 16 && wr == 0) {
#pragma unroll
    for (int n = 0; n < NTL; ++n) tabs[256 + 16 * (NB0 + n) + l2] = cs[n][0];
  }
#pragma unroll
  for (int t = 0; t < NST; ++t) {
    const int mrow = 64 * wr + 16 * (t == 0 ? M0 : M1);
    if (r2 == 0) *(f32x4*)(tabs + mrow + 4 * q2) = rsum[t];
    if (q2 == (r2 >> 2)) tabs[128 + mrow + r2] = (r2 & 2) ? ((r2 & 1) ? rsq[t][3] : rsq[t][2]) : ((r2 & 1) ? rsq[t][1] : rsq[t][0]);
  }
}
template <int NTL>
DI void k1_epilogue(char* lds, int lane, int w, int type, int rt, int hg, const float* tabs, f32x4 (&acc)[NTL][4], char* kvimg) {
  asm volatile("" : "+v"(lane));
  const int wr = w & 1, wc = w >> 1, nb0 = wc == 0 ? 0 : 2 * wc + 1, fr = lane & 15, fq = lane >> 4;
  const float* rs = tabs; const float* mr = tabs + 128; const float* cs = tabs + 256;
  if (type == 2) {
#pragma unroll
    for (int n = 0; n < NTL; ++n) {
      const int col = 16 * (nb0 + n) + fr, hh = col / 18, d = col - hh * 18; const float c = cs[col];
      char* hb = kvimg + (size_t)(hg * 8 + hh) * KV_HEAD + (size_t)(rt * 4 + 2 * wr) * KV_TILE + KV_V + (fq & 1) * 304 + (fq >> 1) * 8 + d * 16;
#pragma unroll
      for (int m = 0; m < 4; ++m) {
        const int row = 64 * wr + 16 * m + 4 * fq;
        const f32x4 r4 = *(const f32x4*)(rs + row), m4 = *(const f32x4*)(mr + row);
        u32x2 o; o[0] = pk2(acc[n][m][0] * r4[0] - m4[0] * c, acc[n][m][1] * r4[1] - m4[1] * c); o[1] = pk2(acc[n][m][2] * r4[2] - m4[2] * c, acc[n][m][3] * r4[3] - m4[3] * c);
        *(u32x2*)(hb + (m >> 1) * KV_TILE + (m & 1) * KV_VS) = o;
      }
    }
  } else {
    const float qs = type == 0 ? 0.23570226039551584f * 1.4426950408889634f : 1.f;
#pragma unroll
    for (int m = 0; m < 4; ++m) {
      const int row = 64 * wr + 16 * m + fr; const float r = rs[row] * qs, mm = mr[row] * qs;
#pragma unroll
      for (int n = 0; n < NTL; ++n) {
        const int col = 16 * (nb0 + n) + 4 * fq; const f32x4 c4 = *(const f32x4*)(cs + col);
        u32x2 o; o[0] = pk2(acc[n][m][0] * r - mm * c4[0], acc[n][m][1] * r - mm * c4[1]); o[1] = pk2(acc[n][m][2] * r - mm * c4[2], acc[n][m][3] * r - mm * c4[3]);
        *(u32x2*)(lds + row * EP_PITCH + col * 2) = o;
      }
    }
  }
}
__global__ __launch_bounds__(K1_THREADS, 3) void k1_kernel(const u16* __restrict__ x, const u16* __restrict__ wqkv, char* __restrict__ ws) {
  extern __shared__ __attribute__((aligned(16))) char lds[];
  const int tid = threadIdx.x, lane = tid & 63; const int w = __builtin_amdgcn_readfirstlane(tid >> 6);
  const int b = blockIdx.x;
  TS(1, 0); TS(1, 6);
  if (b >= 192) {
    if (tid < 512) ((u32x4*)(ws + WS_XS))[(b - 192) * 512 + tid] = u32x4{0, 0, 0, 0};
    return;
  }
  const int xc = b & 7, idx = b >> 3;
  const int rt = xc * 2 + (idx & 1), ct = idx >> 1;
  const int type = ct >> 2, hg = ct & 3;
  float* tabs = (float*)(lds + C8_LDS);
  char* kvimg = ws + WS_KV; char* qimg = ws + WS_Q;
  const char* Xb = (const char*)(x + (size_t)rt * 128 * D); const char* Wb = (const char*)(wqkv + ct * 144);
  auto src = [&](int p) { return p < 16 ? Xb + tn_off(p * 8 + (lane >> 3), lane, D * 2) : Wb + kn_off<144>(p - 16, lane, 1728 * 2); };
  first_tile<34, 12>(lds, w, src);
  if (w >= 8) {
    __builtin_amdgcn_s_setprio(2);
    loader_loop<9, C8_NS, C8_STG, 34, 16, 4>(lds, w - 8, src, [&](int kt, long& ox, long& ow) { ox = (long)kt * 128; ow = (long)kt * 64 * 1728 * 2; }, NoPost());
    __syncthreads();
    const int row = (w - 8) * 64 + lane_id();
    if (row < 128) { const float mean = tabs[row] * (1.f / D), var = fmaxf(tabs[128 + row] * (1.f / D) - mean * mean, 0.f), rstd = rsqrtf(var + 1e-5f); tabs[row] = rstd; tabs[128 + row] = mean * rstd; }
    __syncthreads();
  } else {
#define K1_PATH(WC, WX) do { f32x4 acc[WC == 0 ? 3 : 2][4] = {}; \
      k1_compute<WC, WX>(lds, lane, w, acc, tabs, ws); \
      __syncthreads(); __syncthreads(); \
      k1_epilogue<WC == 0 ? 3 : 2>(lds, lane_id(), w, WX ? (type & 1) : 2, rt, hg, tabs, acc, kvimg); } while (0)
#define K1_SEL(WX) do { if (w < 2) K1_PATH(0, WX); else if (w < 4) K1_PATH(1, WX); else if (w < 6) K1_PATH(2, WX); else K1_PATH(3, WX); } while (0)
    if (type == 2) K1_SEL(false); else K1_SEL(true);
  }
  const int tid2 = w * 64 + lane_id();
  if (type == 2) {
    if (tid2 < 192) {
      const int tl = tid2 / 48, r = tid2 - tl * 48, hh = r / 6, c = r - hh * 6;
      char* tb = kvimg + (size_t)(hg * 8 + hh) * KV_HEAD + (size_t)(rt * 4 + tl) * KV_TILE;
      if (c < 4) *(u32x4*)(tb + KV_V + (c >> 1) * KV_VS + (c & 1) * 304 + 18 * 16) = u32x4{0x3f803f80u, 0x3f803f80u, 0x3f803f80u, 0x3f803f80u};
      else *(u32x4*)(tb + KV_Z + (c - 4) * KV_VS) = u32x4{0, 0, 0, 0};
    }
    TS(1, 3); TS(1, 7);
    return;
  }
  __syncthreads();
  for (int it = tid2; it < 1024; it += K1_THREADS) {
    const int tok = it & 127, hh = it >> 7;
    const unsigned* sp = (const unsigned*)(lds + tok * EP_PITCH + hh * 36);
    u32x4 c0 = {sp[0], sp[1], sp[2], sp[3]}, c1 = {sp[4], sp[5], sp[6], sp[7]}; const unsigned c2 = sp[8];
    char* tb = type == 0 ? qimg + (size_t)(hg * 8 + hh) * Q_HEAD + (size_t)(rt * 4 + (tok >> 5)) * Q_TILE
                         : kvimg + (size_t)(hg * 8 + hh) * KV_HEAD + (size_t)(rt * 4 + (tok >> 5)) * KV_TILE;
    const int key = tok & 31;
    *(u32x4*)(tb + key * 16) = c0; *(u32x4*)(tb + 512 + key * 16) = c1; *(unsigned*)(tb + 1024 + key * 4) = c2;
  }
  TS(1, 3); TS(1, 7);
}
static_assert(128 * EP_PITCH <= C8_LDS, "");
constexpr int K1_LDS = C8_LDS + 400 * 4;

constexpr int ATT_LDS = KV_HEAD + 8 * Q_TILE;
#define SWITCH 50
__global__ __launch_bounds__(768, 3) void k2_kernel(const u16* __restrict__ wo, const u16* __restrict__ w_in, const u16* __restrict__ w_out, const u16* __restrict__ w_sp, char* __restrict__ ws) {
  extern __shared__ __attribute__((aligned(16))) char lds[];
  TS(2, 0); TS(2, 6);
  const char* kvimg = ws + WS_KV; const char* qimg = ws + WS_Q; u16* o = (u16*)(ws + WS_O);
  const int tid = threadIdx.x, lane = tid & 63; const int w = __builtin_amdgcn_readfirstlane(tid >> 6);
  const int b = blockIdx.x, xcd = b & 7, idx = b >> 3, h = xcd * 4 + (idx >> 3), qb = idx & 7;
  const char* kvg = kvimg + (size_t)h * KV_HEAD + lane * 16;
  if (w < 8) {
    const char* qg = qimg + (size_t)h * Q_HEAD + (qb * 8 + w) * Q_TILE + lane * 16;
    char* qtile = lds + KV_HEAD + w * Q_TILE;
    glds16(qg, qtile);
    if (lane < 8) glds16(qg + 1024, qtile + 1024);
  }
  glds16(kvg + w * 1024, lds + w * 1024); glds16(kvg + (w + 12) * 1024, lds + (w + 12) * 1024);
  WAITV(0);
  BAR();
  if (w >= 8) {
    const int lw = w - 8;
    __builtin_amdgcn_s_setprio(3);
#pragma unroll 1
    for (int p = 24 + lw; p < 150; p += 4) glds16(kvg + p * 1024, lds + p * 1024);
    WAITV(0);
    BAR();
    {
      const u32x4* wp = (const u32x4*)w_sp + (size_t)(b * 4 + lw) * 512 + lane;
      u32x4 acc = wp[0];
#pragma unroll
      for (int i = 1; i < 8; ++i) acc ^= wp[i * 64];
      if (acc[0] == 0x12345678u && acc[1] == 0x9abcdef0u && acc[2] == 0x0fedcba9u && acc[3] == 0x87654321u) ((unsigned*)(ws + WS_DBG))[0] = 1;
    }
    const int gw = b * 4 + lw, c = lane & 7, nb = lane >> 3;
    if (nb < 6) {
#pragma unroll 1
      for (int blk = gw; blk < 864 + 108 + 432; blk += 1024) {
        const u16* sp; int ld, kt, n0; size_t db;
        if (blk < 864) { kt = blk / 96; const int nbk = blk - kt * 96, z2 = nbk >= 48, nn = z2 ? nbk - 48 : nbk, t = nn / 3; n0 = nbk * 48; sp = w_in; ld = F;
          db = WS_WIN + ((size_t)((z2 ? 0 : 16) + t) * 9 + kt) * 18432 + (nn - t * 3) * 48 * 128; }
        else if (blk < 972) { const int q = blk - 864; kt = q / 12; const int nbk = q - kt * 12; n0 = nbk * 48; sp = wo; ld = D; db = WS_WO + ((size_t)(nbk >> 1) * 9 + kt) * 12288 + (nbk & 1) * 48 * 128; }
        else { const int q = blk - 972; kt = q / 12; const int nbk = q - kt * 12; n0 = nbk * 48; sp = w_out; ld = D; db = WS_WOUT + ((size_t)(nbk >> 1) * 36 + kt) * 12288 + (nbk & 1) * 48 * 128; }
        sp += (size_t)(kt * 64 + c * 8) * ld + n0 + nb * 8;
        u32x4 r[8];
#pragma unroll
        for (int j = 0; j < 8; ++j) r[j] = *(const u32x4*)(sp + (size_t)j * ld);
        const unsigned lo = (unsigned)db + nb * 1024;
#pragma unroll
        for (int e = 0; e < 4; ++e) {
          const unsigned sl = (unsigned)((c ^ ((4 * nb + e) & 7)) << 4);
#pragma unroll
          for (int hi = 0; hi < 2; ++hi) {
            const unsigned sel = hi ? 0x07060302u : 0x05040100u;
            u32x4 o;
#pragma unroll
            for (int q = 0; q < 4; ++q) o[q] = __builtin_amdgcn_perm(r[2 * q + 1][e], r[2 * q][e], sel);
            st16_wt(ws, lo + (2 * e + hi) * 128 + sl, o);
          }
        }
      }
    }
    return;
  }
  TS(2, 1);
  if (w >= 4) __builtin_amdgcn_s_setprio(1);
  const int r32 = lane & 31, hh = lane >> 5;
  lds_cptr L = (lds_cptr)lds;
  const bf16x8 qf0 = LDS_RD128(L + KV_HEAD + w * Q_TILE + lane * 16);
  u32x4 q1v = {0, 0, 0, 0}; { unsigned t = LDS_RD32(L + KV_HEAD + w * Q_TILE + 1024 + r32 * 4); q1v[0] = hh ? 0u : t; }
  const bf16x8 qf1 = __builtin_bit_cast(bf16x8, q1v);
  lds_cptr kp0 = L + lane * 16, kp1 = L + (hh ? KV_Z : KV_K1 + r32 * 4), vp = L + (r32 <= 18 ? KV_V + hh * 304 + r32 * 16 : KV_Z);
  f32x16 negm;
  {
    bf16x8 k0 = LDS_RD128(kp0); u32x4 k1 = {LDS_RD32(kp1), 0, 0, 0};
    f32x16 s = {}; s = MFMA32(k0, qf0, s); s = MFMA32(__builtin_bit_cast(bf16x8, k1), qf1, s);
    float m = s[0];
#pragma unroll
    for (int i = 1; i < 16; ++i) m = fmaxf(m, s[i]);
    auto rr = __builtin_amdgcn_permlane32_swap(__float_as_uint(m), __float_as_uint(m), false, false);
    m = fmaxf(__uint_as_float(rr[0]), __uint_as_float(rr[1]));
#pragma unroll
    for (int i = 0; i < 16; ++i) negm[i] = -m;
  }
  f32x16 O = {};
#define EX2(Sx, i) pk2(__builtin_amdgcn_exp2f(Sx[i]), __builtin_amdgcn_exp2f(Sx[(i) + 1]))
  bf16x8 k0, v0, v1; u32x4 k1 = {0, 0, 0, 0};
  f32x16 SA, SBc; u32x4 pA0, pA1, pB0, pB1;
#define STEP(Sc, Sn, Pc0, Pc1, Pp0, Pp1, koff, voff) do { \
    SB(); Sn = MFMA32(k0, qf0, negm); SB(); \
    Pc0[0] = EX2(Sc, 0); Pc0[1] = EX2(Sc, 2); PIN(Pc0); SB(); \
    Sn = MFMA32(__builtin_bit_cast(bf16x8, k1), qf1, Sn); SB(); \
    k0 = LDS_RD128(kp0 + (koff)); k1[0] = LDS_RD32(kp1 + (koff)); SB(); \
    Pc0[2] = EX2(Sc, 4); Pc0[3] = EX2(Sc, 6); PIN(Pc0); SB(); \
    O = MFMA32(v0, __builtin_bit_cast(bf16x8, Pp0), O); SB(); \
    Pc1[0] = EX2(Sc, 8); Pc1[1] = EX2(Sc, 10); PIN(Pc1); SB(); \
    O = MFMA32(v1, __builtin_bit_cast(bf16x8, Pp1), O); SB(); \
    v0 = LDS_RD128(vp + (voff)); v1 = LDS_RD128(vp + (voff) + KV_VS); SB(); \
    Pc1[2] = EX2(Sc, 12); Pc1[3] = EX2(Sc, 14); PIN(Pc1); SB(); \
  } while (0)
  k0 = LDS_RD128(kp0); k1[0] = LDS_RD32(kp1);
  SA = MFMA32(k0, qf0, negm); SA = MFMA32(__builtin_bit_cast(bf16x8, k1), qf1, SA);
  k0 = LDS_RD128(kp0 + KV_TILE); k1[0] = LDS_RD32(kp1 + KV_TILE);
  v0 = LDS_RD128(vp); v1 = LDS_RD128(vp + KV_VS);
  pB0 = u32x4{0, 0, 0, 0}; pB1 = u32x4{0, 0, 0, 0};
  int off = 0;
  for (int t = 0; t < 8; t += 2) {
    STEP(SA, SBc, pA0, pA1, pB0, pB1, off + 2 * KV_TILE, off);
    STEP(SBc, SA, pB0, pB1, pA0, pA1, off + 3 * KV_TILE, off + KV_TILE);
    off += 2 * KV_TILE;
  }
  BAR();
  for (int t = 8; t < 62; t += 2) {
    if (t == SWITCH) __builtin_amdgcn_s_setprio(0);
    STEP(SA, SBc, pA0, pA1, pB0, pB1, off + 2 * KV_TILE, off);
    STEP(SBc, SA, pB0, pB1, pA0, pA1, off + 3 * KV_TILE, off + KV_TILE);
    off += 2 * KV_TILE;
  }
  STEP(SA, SBc, pA0, pA1, pB0, pB1, off + KV_TILE, off);
  STEP(SBc, SA, pB0, pB1, pA0, pA1, off + KV_TILE, off + KV_TILE);
  O = MFMA32(v0, __builtin_bit_cast(bf16x8, pB0), O); O = MFMA32(v1, __builtin_bit_cast(bf16x8, pB1), O);
  TS(2, 2);
  {
    float mx = 0.f;
#pragma unroll
    for (int i = 0; i < 11; ++i) mx = fmaxf(mx, fabsf(O[i]));
    bool bad = !(mx < 1.2676506e30f);
#pragma unroll
    for (int i = 0; i < 11; ++i) bad |= (O[i] != O[i]);
    if (__any(bad)) {
      float m = -3.0e38f; O = f32x16{};
#pragma unroll 1
      for (int t = 0; t < 64; ++t) {
        const int of = t * KV_TILE;
        const bf16x8 a0 = LDS_RD128(kp0 + of); const u32x4 a1 = {LDS_RD32(kp1 + of), 0, 0, 0};
        f32x16 s = {}; s = MFMA32(a0, qf0, s); s = MFMA32(__builtin_bit_cast(bf16x8, a1), qf1, s);
        float tm = s[0];
#pragma unroll
        for (int i = 1; i < 16; ++i) tm = fmaxf(tm, s[i]);
        auto rr = __builtin_amdgcn_permlane32_swap(__float_as_uint(tm), __float_as_uint(tm), false, false);
        tm = fmaxf(__uint_as_float(rr[0]), __uint_as_float(rr[1]));
        const float mn = fmaxf(m, tm), al = __builtin_amdgcn_exp2f(m - mn); m = mn;
#pragma unroll
        for (int i = 0; i < 16; ++i) { O[i] *= al; s[i] -= mn; }
        u32x4 p0, p1;
#pragma unroll
        for (int i = 0; i < 4; ++i) { p0[i] = EX2(s, 2 * i); p1[i] = EX2(s, 8 + 2 * i); }
        O = MFMA32(LDS_RD128(vp + of), __builtin_bit_cast(bf16x8, p0), O); O = MFMA32(LDS_RD128(vp + of + KV_VS), __builtin_bit_cast(bf16x8, p1), O);
      }
    }
  }
  {
    float l = O[10];
    { auto rr = __builtin_amdgcn_permlane32_swap(__float_as_uint(l), __float_as_uint(l), false, false); l = __uint_as_float(rr[0]); }
    const float inv = 1.f / l;
    u16* orow = o + (size_t)(qb * 256 + w * 32 + r32) * D + h * DH;
    const unsigned a0 = pk2(O[0] * inv, O[1] * inv), a1_ = pk2(O[2] * inv, O[3] * inv), a2_ = pk2(O[4] * inv, O[5] * inv), a3 = pk2(O[6] * inv, O[7] * inv), a4 = pk2(O[8] * inv, O[9] * inv);
    unsigned* op = (unsigned*)(orow + hh * 4);
    op[0] = a0; op[1] = a1_; op[4] = a2_; op[5] = a3;
    if (hh == 0) op[8] = a4;
  }
  TS(2, 3); TS(2, 7);
}

constexpr int G_STG = 160 * 128, G_W = 8192, G_NS = 6, G_LDS = G_NS * G_STG;
template <int K, bool STATS, int KID>
DI void g_body(char* ws, char* lds, const u16* __restrict__ A, const char* __restrict__ W, const u16* __restrict__ res, u16* __restrict__ out, float* __restrict__ st2, int b) {
  constexpr int NT = K / 64;
  const int tid = threadIdx.x, lane = tid & 63; const int w = __builtin_amdgcn_readfirstlane(tid >> 6);
  const int xc = b & 7, idx = b >> 3, rt = xc * 4 + (idx & 3), ct = idx >> 2;
  const int m0 = rt * 64, n0 = ct * 96;
  TS(KID, 0); TS(KID, 6);
  const char* Xb = (const char*)(A + (size_t)m0 * K); const char* Wb = W + (size_t)ct * NT * 12288;
  auto src = [&](int p) { return p < 8 ? Xb + tn_off(p * 8 + (lane >> 3), lane, K * 2) : Wb + (p - 8) * 1024 + lane * 16; };
  first_tile<20, 12>(lds, w, src);
  if (w >= 4) {
    __builtin_amdgcn_s_setprio(2);
    loader_loop<NT, G_NS, G_STG, 20, 8, 8>(lds, w - 4, src, [&](int kt, long& ox, long& ow) { ox = (long)kt * 128; ow = (long)kt * 12288; }, NoPost());
    return;
  }
  const int wr = w & 1, wc = w >> 1, fr = lane & 15, fq = lane >> 4, sw = (fr >> 1) & 7;
  lds_cptr L = (lds_cptr)lds;
  lds_cptr x0 = L + (32 * wr + fr) * 128 + ((fq ^ sw) << 4), x1 = L + (32 * wr + fr) * 128 + (((4 + fq) ^ sw) << 4);
  lds_cptr w0 = L + G_W + (48 * wc + fr) * 128 + ((fq ^ sw) << 4), w1 = L + G_W + (48 * wc + fr) * 128 + (((4 + fq) ^ sw) << 4);
  f32x4 acc[3][2] = {};
  bf16x8 xa[2], wa[3], xb[2], wb_[3];
#define GRD(X, Wf, st, ks) do { X[0] = LDS_RD128(((ks) ? x1 : x0) + (st)); X[1] = LDS_RD128(((ks) ? x1 : x0) + (st) + 2048); \
    _Pragma("unroll") for (int n = 0; n < 3; ++n) Wf[n] = LDS_RD128(((ks) ? w1 : w0) + (st) + n * 2048); } while (0)
#define GSCHED() do { _Pragma("unroll") for (int i_ = 0; i_ < 5; ++i_) { SGB(0x8, 1); SGB(0x100, 1); } SGB(0x8, 1); } while (0)
#define GMM(X, Wf) do { _Pragma("unroll") for (int n = 0; n < 3; ++n) { acc[n][0] = MFMA16(Wf[n], X[0], acc[n][0]); acc[n][1] = MFMA16(Wf[n], X[1], acc[n][1]); } } while (0)
  WAITV(0); BAR();
  TS(KID, 1);
  GRD(xa, wa, 0, 0);
  u32x2 rv[2][3];
#pragma unroll
  for (int m = 0; m < 2; ++m)
#pragma unroll
    for (int n = 0; n < 3; ++n) rv[m][n] = *(const u32x2*)(res + (size_t)(m0 + 32 * wr + 16 * m + fr) * D + n0 + 48 * wc + 16 * n + 4 * fq);
  int st = 0;
#pragma unroll 1
  for (int kt = 0; kt < NT; ++kt) {
    const int stn = st + G_STG == G_LDS ? 0 : st + G_STG;
    GRD(xb, wb_, st, 1);
    GMM(xa, wa);
    GSCHED();
    SB(); BAR(); SB();
    GRD(xa, wa, stn, 0);
    GMM(xb, wb_);
    GSCHED();
    SB();
    st = stn;
  }
  TS(KID, 2);
#pragma unroll
  for (int m = 0; m < 2; ++m) {
    const int row = m0 + 32 * wr + 16 * m + fr;
    float sum = 0.f, ssq = 0.f;
#pragma unroll
    for (int n = 0; n < 3; ++n) {
      const int col = n0 + 48 * wc + 16 * n + 4 * fq;
      const u32x2 r = rv[m][n];
      u32x2 o; o[0] = pk2(bflo(r[0]) + acc[n][m][0], bfhi(r[0]) + acc[n][m][1]); o[1] = pk2(bflo(r[1]) + acc[n][m][2], bfhi(r[1]) + acc[n][m][3]);
      *(u32x2*)(out + (size_t)row * D + col) = o;
      if (STATS) { const float a = bflo(o[0]), bq = bfhi(o[0]), c = bflo(o[1]), d = bfhi(o[1]); sum += (a + bq) + (c + d); ssq += (a * a + bq * bq) + (c * c + d * d); }
    }
    if (STATS) {
      sum += __shfl_xor(sum, 16); sum += __shfl_xor(sum, 32); ssq += __shfl_xor(ssq, 16); ssq += __shfl_xor(ssq, 32);
      if (fq == 0) *(f32x2_t*)(st2 + ((size_t)row * 12 + ct * 2 + wc) * 2) = f32x2_t{sum, ssq};
    }
  }
  TS(KID, 3); TS(KID, 7);
}
__global__ __launch_bounds__(768, 3) void k3_kernel(const u16* __restrict__ x, const u16* __restrict__ wo, const u16* __restrict__ w_in, char* __restrict__ ws) {
  extern __shared__ __attribute__((aligned(16))) char lds[];
  const int b = blockIdx.x;
  if (b >= 192) {
    const int tid = threadIdx.x, c = tid % 9, rg = tid / 9;
    float* red = (float*)lds;
    if (rg < 48) {
      const char* p = (const char*)(w_in + (b - 192) * 72 + c * 8) + (size_t)rg * (F * 2);
      u32x4 v[12];
#pragma unroll
      for (int i = 0; i < 12; ++i) v[i] = *(const u32x4*)(p + (size_t)i * 48 * F * 2);
      float a[8] = {};
#pragma unroll
      for (int i = 0; i < 12; ++i)
#pragma unroll
        for (int e = 0; e < 4; ++e) { a[2 * e] += bflo(v[i][e]); a[2 * e + 1] += bfhi(v[i][e]); }
#pragma unroll
      for (int e = 0; e < 8; ++e) red[rg * 72 + c * 8 + e] = a[e];
    }
    __syncthreads();
    if (tid < 72) { float t = 0.f; for (int g = 0; g < 48; ++g) t += red[g * 72 + tid]; ((float*)(ws + WS_CS))[(b - 192) * 72 + tid] = t; }
    return;
  }
  g_body<D, true, 3>(ws, lds, (const u16*)(ws + WS_O), ws + WS_WO, x, (u16*)(ws + WS_H), (float*)(ws + WS_ST2), b);
}
__global__ __launch_bounds__(768, 3) void k6_kernel(const u16* __restrict__ w_out, u16* __restrict__ out, char* __restrict__ ws) {
  extern __shared__ __attribute__((aligned(16))) char lds[];
  g_body<FH, false, 0>(ws, lds, (const u16*)(ws + WS_U), ws + WS_WOUT, (const u16*)(ws + WS_H), out, nullptr, blockIdx.x);
}

constexpr unsigned XTAG = 0x9E3779B9u;
DI float dpp_sum16(float v) {
  v += __builtin_bit_cast(float, __builtin_amdgcn_mov_dpp(__builtin_bit_cast(int, v), 0xB1, 0xf, 0xf, true));
  v += __builtin_bit_cast(float, __builtin_amdgcn_mov_dpp(__builtin_bit_cast(int, v), 0x4E, 0xf, 0xf, true));
  v += __builtin_bit_cast(float, __builtin_amdgcn_mov_dpp(__builtin_bit_cast(int, v), 0x141, 0xf, 0xf, true));
  v += __builtin_bit_cast(float, __builtin_amdgcn_mov_dpp(__builtin_bit_cast(int, v), 0x140, 0xf, 0xf, true));
  return v;
}
constexpr int K4_STG = (128 + 288) * 128, K4_W2 = 16384, K4_W1 = 16384 + 18432, K4_NS = 3;
constexpr int K4_TAB = K4_NS * K4_STG;
constexpr int K4_PART = 40960, K4_ST2 = 45056;
constexpr int K4_LDS = K4_TAB + 544 * 4;
template <int NTL>
DI void k4_compute(char* lds, int lane, int w, int pi, int ft, char* ws) {
  const int wr = w & 3, nb0 = NTL == 5 ? 0 : 5, wc = NTL == 5 ? 0 : 1, fr = lane & 15, fq = lane >> 4, sw = (fr >> 1) & 7;
  lds_cptr L = (lds_cptr)lds;
  lds_cptr x0 = L + (32 * wr + fr) * 128 + ((fq ^ sw) << 4), x1 = L + (32 * wr + fr) * 128 + (((4 + fq) ^ sw) << 4);
  lds_cptr w0 = L + K4_W2 + (16 * nb0 + fr) * 128 + ((fq ^ sw) << 4), w1 = L + K4_W2 + (16 * nb0 + fr) * 128 + (((4 + fq) ^ sw) << 4);
  f32x4 acc2[NTL][2] = {}, acc1[NTL][2] = {};
  bf16x8 xa[2], xb[2], wa[NTL], wb[NTL];
#define RDX(X, P, st) do { X[0] = LDS_RD128(P + (st)); X[1] = LDS_RD128(P + (st) + 2048); } while (0)
#define RDW(Wf, P, st) do { _Pragma("unroll") for (int n = 0; n < NTL; ++n) Wf[n] = LDS_RD128(P + (st) + n * 2048); } while (0)
#define MM2(X, Wf) do { _Pragma("unroll") for (int n = 0; n < NTL; ++n) { acc2[n][0] = MFMA16(X[0], Wf[n], acc2[n][0]); acc2[n][1] = MFMA16(X[1], Wf[n], acc2[n][1]); } } while (0)
#define MM1(X, Wf) do { _Pragma("unroll") for (int n = 0; n < NTL; ++n) { acc1[n][0] = MFMA16(Wf[n], X[0], acc1[n][0]); acc1[n][1] = MFMA16(Wf[n], X[1], acc1[n][1]); } } while (0)
#define SCH(NR) do { _Pragma("unroll") for (int i_ = 0; i_ < (NR); ++i_) { SGB(0x8, 1); SGB(0x100, 1); } SGB(0x8, 2 * NTL - (NR)); SB(); } while (0)
  WAITV(0); WAITL0();
  if (NTL == 4) BAR();
  int st = 0;
#pragma unroll 1
  for (int kt = 0; kt < 9; ++kt) {
    BAR(); SB();
    if (kt == 0) TS(4, 1);
    RDX(xa, x0, st); RDW(wa, w0, st); SB();
    RDW(wb, w0, st + 18432); MM2(xa, wa); SCH(NTL);
    RDX(xb, x1, st); RDW(wa, w1, st); MM1(xa, wb); SCH(NTL + 2);
    BAR(); SB();
    RDW(wb, w1, st + 18432); MM2(xb, wa); SCH(NTL);
    MM1(xb, wb); SB();
    st = st + K4_STG == K4_TAB ? 0 : st + K4_STG;
  }
  if (NTL == 5) BAR();
  TS(4, 4);
  int lz = lane_id();
  const float* tab = (const float*)(lds + K4_TAB); const float* rs = tab; const float* mr = tab + 128; const float* cs = tab + 256;
  float* part = (float*)(lds + K4_PART); const float* st2 = (const float*)(lds + K4_ST2);
  u32x2 z2p[NTL][2];
  {
    const int fr = lz & 15, fq = lz >> 4;
#pragma unroll
    for (int m = 0; m < 2; ++m) {
      const f32x4 r4 = *(const f32x4*)(rs + 32 * wr + 16 * m + 4 * fq), m4 = *(const f32x4*)(mr + 32 * wr + 16 * m + 4 * fq);
      f32x4 su = {0, 0, 0, 0}, sq = {0, 0, 0, 0};
#pragma unroll
      for (int n = 0; n < NTL; ++n) {
        const float c = cs[16 * (nb0 + n) + fr];
        f32x4 g;
#pragma unroll
        for (int r = 0; r < 4; ++r) { g[r] = gelu_t(acc2[n][m][r] * r4[r] - m4[r] * c); su[r] += g[r]; sq[r] = __builtin_fmaf(g[r], g[r], sq[r]); }
        z2p[n][m][0] = pk2(g[0], g[1]); z2p[n][m][1] = pk2(g[2], g[3]);
      }
#pragma unroll
      for (int r = 0; r < 4; ++r) { su[r] = dpp_sum16(su[r]); sq[r] = dpp_sum16(sq[r]); }
      if (fr == 0) {
#pragma unroll
        for (int r = 0; r < 4; ++r) *(f32x2_t*)(part + (wc * 128 + 32 * wr + 16 * m + 4 * fq + r) * 2) = f32x2_t{su[r], sq[r]};
      }
    }
  }
  WAITL0(); BAR();
  if (wc == 0 && lz < 32) {
    const int row = 32 * wr + lz; const f32x2_t p0 = *(const f32x2_t*)(part + row * 2), p1 = *(const f32x2_t*)(part + (128 + row) * 2);
    const u32x4 g = {__float_as_uint(p0[0] + p1[0]), XTAG, __float_as_uint(p0[1] + p1[1]), XTAG};
    st16_wt(ws, WS_XS + (size_t)(((pi * 16 + ft) * 128 + row) * 16), g);
  }
  TS(4, 2);
  {
    const int fr = lz & 15, fq = lz >> 4;
    const size_t zb = WS_Z1 + ((size_t)(pi * 16 + ft) * 4 + wr) * (9 * 1024) + lz * 16;
    const float r0 = rs[32 * wr + fr], mm0 = mr[32 * wr + fr], r1 = rs[32 * wr + 16 + fr], mm1 = mr[32 * wr + 16 + fr];
#pragma unroll
    for (int n = 0; n < NTL; ++n) {
      const f32x4 c4 = *(const f32x4*)(cs + 144 + 16 * (nb0 + n) + 4 * fq);
      float g0[4], g1[4];
#pragma unroll
      for (int r = 0; r < 4; ++r) { g0[r] = gelu_t(acc1[n][0][r] * r0 - mm0 * c4[r]); g1[r] = gelu_t(acc1[n][1][r] * r1 - mm1 * c4[r]); }
      const u32x4 o = {pk2(g0[0], g0[1]), pk2(g0[2], g0[3]), pk2(g1[0], g1[1]), pk2(g1[2], g1[3])};
      st16_wt(ws, zb + (nb0 + n) * 1024, o);
    }
  }
  TS(4, 5);
  __syncthreads();
  asm volatile("" : "+v"(lz));
#pragma unroll
  for (int m = 0; m < 2; ++m) {
    const int fr = lz & 15, fq = lz >> 4;
    const int sl = 32 * wr + 16 * m + 4 * fq;
    const f32x4 sa = *(const f32x4*)(st2 + sl * 2), sb = *(const f32x4*)(st2 + sl * 2 + 4);
    const float mu[4] = {sa[0], sa[2], sb[0], sb[2]}, rr[4] = {sa[1], sa[3], sb[1], sb[3]};
#pragma unroll
    for (int n = 0; n < NTL; ++n) {
      const int f = 16 * (nb0 + n) + fr; const u32x2 p = z2p[n][m];
      u32x2 o; o[0] = pk2(bflo(p[0]) * rr[0] - mu[0], bfhi(p[0]) * rr[1] - mu[1]); o[1] = pk2(bflo(p[1]) * rr[2] - mu[2], bfhi(p[1]) * rr[3] - mu[3]);
      *(u32x2*)(lds + (sl >> 6) * 18432 + f * 128 + ((((sl & 63) >> 3) ^ ((f >> 1) & 7)) << 4) + (sl & 4) * 2) = o;
    }
  }
}
__global__ __launch_bounds__(768, 3) void k4_kernel(char* __restrict__ ws) {
  extern __shared__ __attribute__((aligned(16))) char lds[];
  const int tid = threadIdx.x, lane = tid & 63; const int w = __builtin_amdgcn_readfirstlane(tid >> 6);
  const int b = blockIdx.x, xc = b & 7, idx = b >> 3;
  const int pi = (xc & 1) * 8 + (idx & 7), ft = (xc >> 1) * 4 + (idx >> 3);
  float* tab = (float*)(lds + K4_TAB);
  TS(4, 0); TS(4, 6);
  const char* Xb = ws + WS_H + (size_t)pi * 128 * D * 2; const char* W2b = ws + WS_WIN + (size_t)ft * 9 * 18432; const char* W1b = W2b + (size_t)16 * 9 * 18432;
  auto src = [&](int p) { return p < 16 ? Xb + tn_off(p * 8 + (lane >> 3), lane, D * 2) : (p < 34 ? W2b + (p - 16) * 1024 : W1b + (p - 34) * 1024) + lane * 16; };
  first_tile<52, 12>(lds, w, src);
  if (w >= 8) {
    const int lw = w - 8;
    __builtin_amdgcn_s_setprio(2);
    const char* base[13];
#pragma unroll
    for (int j = 0; j < 13; ++j) base[j] = src(lw + 4 * j);
    auto issue = [&](int kt, int stage) {
#pragma unroll
      for (int j = 0; j < 13; ++j) glds16(base[j] + (j < 4 ? (long)kt * 128 : (long)kt * 18432), lds + stage * K4_STG + (lw + 4 * j) * 1024);
    };
    issue(1, 1);
    int stg = 2;
#pragma unroll 1
    for (int kt = 0; kt < 7; ++kt) { WAITV(13); BAR(); BAR(); issue(kt + 2, stg); stg = stg == 2 ? 0 : stg + 1; }
    WAITV(13); BAR(); BAR();
    WAITV(0); BAR(); BAR();
    BAR();
    BAR();
    const int row = 32 * lw + (lane >> 1);
    const __amdgpu_buffer_rsrc_t rs_ = __builtin_amdgcn_make_buffer_rsrc((void*)(ws + WS_XS), 0, 2048 * 16 * 16, 0x00020000);
    const unsigned vo = (unsigned)(((pi * 16 + (lane & 1) * 8) * 128 + row) * 16);
    float sum = 0.f, ssq = 0.f;
    for (int spin = 0; spin < (1 << 20); ++spin) {
      bool ok = true; sum = 0.f; ssq = 0.f;
      u32x4 g[8];
#pragma unroll
      for (int t = 0; t < 8; ++t) g[t] = __builtin_amdgcn_raw_buffer_load_b128(rs_, vo + t * 2048, 0, 16);
#pragma unroll
      for (int t = 0; t < 8; ++t) { ok &= g[t][1] == XTAG && g[t][3] == XTAG; sum += __uint_as_float(g[t][0]); ssq += __uint_as_float(g[t][2]); }
      if (__all(ok)) break;
      __builtin_amdgcn_s_sleep(8);
    }
    sum += __shfl_xor(sum, 1); ssq += __shfl_xor(ssq, 1);
    const float mean = sum * (1.f / FH), var = fmaxf(ssq * (1.f / FH) - mean * mean, 0.f), rstd = rsqrtf(var + 1e-5f);
    if ((lane & 1) == 0) *(f32x2_t*)(lds + K4_ST2 + row * 8) = f32x2_t{mean * rstd, rstd};
    __syncthreads();
  } else {
    if (tid < 128) {
      const f32x4* p = (const f32x4*)(ws + WS_ST2) + (size_t)(pi * 128 + tid) * 6;
      float sum = 0.f, ssq = 0.f;
#pragma unroll
      for (int t = 0; t < 6; ++t) { const f32x4 v = p[t]; sum += v[0] + v[2]; ssq += v[1] + v[3]; }
      const float mean = sum * (1.f / D), var = fmaxf(ssq * (1.f / D) - mean * mean, 0.f), rstd = rsqrtf(var + 1e-5f);
      tab[tid] = rstd; tab[128 + tid] = mean * rstd;
    } else if (tid < 416) {
      const int c = tid - 128;
      tab[256 + c] = ((const float*)(ws + WS_CS))[c < 144 ? FH + ft * 144 + c : ft * 144 + c - 144];
    }
    if (w < 4) k4_compute<5>(lds, lane, w, pi, ft, ws); else k4_compute<4>(lds, lane, w, pi, ft, ws);
  }
  __syncthreads();
  {
    const size_t dst = WS_Z2T + ((size_t)ft * 32 + pi * 2) * 18432;
    for (int i = w * 64 + lane_id(); i < 2304; i += 768) st16_wt(ws, dst + i * 16, *(const u32x4*)(lds + i * 16));
  }
  TS(4, 3); TS(4, 7);
}

template <int NTL>
DI void k5_compute(char* lds, int lane, int w, int tt, int ft, const u16* __restrict__ bsp, char* ws) {
  const int wr = w & 3, nb0 = NTL == 5 ? 0 : 5, fr = lane & 15, fq = lane >> 4;
  const C8Addr a = c8_addr<false>(lds, lane, wr, nb0);
  f32x4 acc[NTL][2] = {};
  C8Frag<NTL> f0, f1;
  WAITV(0); BAR();
  TS(5, 1);
  c8_read<NTL, false>(f0, a, 0, 0);
  unsigned z1v[4 * NTL];
  { const char* zb = ws + WS_Z1 + ((size_t)(tt * 16 + ft) * 4 + wr) * (9 * 1024) + lane * 16;
#pragma unroll
    for (int n = 0; n < NTL; ++n) { const u32x4 v = *(const u32x4*)(zb + (nb0 + n) * 1024); z1v[4 * n] = v[0]; z1v[4 * n + 1] = v[1]; z1v[4 * n + 2] = v[2]; z1v[4 * n + 3] = v[3]; } }
  float bb[2];
#pragma unroll
  for (int m = 0; m < 2; ++m) bb[m] = bf2f(bsp[tt * 128 + 32 * wr + 16 * m + fr]);
  int st = 0;
#define C5_ITER(SIDE0, SIDE1) do { const int stn = st + C8_STG == C8_LDS ? 0 : st + C8_STG; \
    c8_read<NTL, false>(f1, a, st, 1); c8_mma<NTL, true>(acc, f0); c8_sched<NTL, false>(); SB(); SIDE0; SB(); BAR(); SB(); \
    c8_read<NTL, false>(f0, a, stn, 0); c8_mma<NTL, true>(acc, f1); c8_sched<NTL, false>(); SB(); SIDE1; SB(); st = stn; } while (0)
#pragma unroll 1
  for (int kt = 0; kt < 32; ++kt) C5_ITER((void)0, (void)0);
  TS(5, 2);
  __syncthreads();
#pragma unroll
  for (int m = 0; m < 2; ++m) {
    const int t = 32 * wr + 16 * m + fr;
#pragma unroll
    for (int n = 0; n < NTL; ++n) {
      const int f = 16 * (nb0 + n) + 4 * fq; const unsigned z0 = z1v[4 * n + 2 * m], z1 = z1v[4 * n + 2 * m + 1];
      u32x2 o; o[0] = pk2(bflo(z0) * (acc[n][m][0] + bb[m]), bfhi(z0) * (acc[n][m][1] + bb[m]));
      o[1] = pk2(bflo(z1) * (acc[n][m][2] + bb[m]), bfhi(z1) * (acc[n][m][3] + bb[m]));
      *(u32x2*)(lds + t * EP_PITCH + f * 2) = o;
    }
  }
  __syncthreads();
  {
    const size_t ub = WS_U + ((size_t)tt * 128 * FH + ft * 144) * 2;
    for (int i = w * 64 + lane; i < 128 * 18; i += 512) { const int t = i / 18, c = i - t * 18; st16_wt(ws, ub + (size_t)t * FH * 2 + c * 16, *(const u32x4*)(lds + t * EP_PITCH + c * 16)); }
  }
}
__global__ __launch_bounds__(768, 3) void k5_kernel(const u16* __restrict__ wsp, const u16* __restrict__ bsp, char* __restrict__ ws) {
  extern __shared__ __attribute__((aligned(16))) char lds[];
  TS(5, 0); TS(5, 6);
  const int tid = threadIdx.x, lane = tid & 63; const int w = __builtin_amdgcn_readfirstlane(tid >> 6);
  const int b = blockIdx.x, xc = b & 7, idx = b >> 3;
  const int tt = (xc & 1) * 8 + (idx & 7), ft = (xc >> 1) * 4 + (idx >> 3);
  const char* Xb = (const char*)(wsp + (size_t)tt * 128 * S); const char* Wb = ws + WS_Z2T + (size_t)ft * 32 * 18432;
  auto src = [&](int p) { return p < 16 ? Xb + tn_off(p * 8 + (lane >> 3), lane, S * 2) : Wb + (p - 16) * 1024 + lane * 16; };
  first_tile<34, 12>(lds, w, src);
  if (w >= 8) {
    __builtin_amdgcn_s_setprio(2);
    loader_loop<32, C8_NS, C8_STG, 34, 16, 4>(lds, w - 8, src, [&](int kt, long& ox, long& ow) { ox = (long)kt * 128; ow = (long)kt * 18432; }, NoPost());
    return;
  }
  if (w < 4) k5_compute<5>(lds, lane, w, tt, ft, bsp, ws); else k5_compute<4>(lds, lane, w, tt, ft, bsp, ws);
  TS(5, 3); TS(5, 7);
}

extern "C" void kernel_launch(void* const* d_in, const int* in_sizes, int n_in, void* d_out, int out_size, void* d_ws, size_t ws_size, hipStream_t stream) {
  const u16* x = (const u16*)d_in[0]; const u16* wqkv = (const u16*)d_in[1]; const u16* wo = (const u16*)d_in[2];
  const u16* w_in = (const u16*)d_in[4]; const u16* w_sp = (const u16*)d_in[5]; const u16* b_sp = (const u16*)d_in[6]; const u16* w_out = (const u16*)d_in[7];
  u16* out = (u16*)d_out; char* ws = (char*)d_ws;
  static bool attr = false;
  if (!attr) {
    (void)hipFuncSetAttribute((const void*)k1_kernel, hipFuncAttributeMaxDynamicSharedMemorySize, K1_LDS);
    (void)hipFuncSetAttribute((const void*)k2_kernel, hipFuncAttributeMaxDynamicSharedMemorySize, ATT_LDS);
    (void)hipFuncSetAttribute((const void*)k3_kernel, hipFuncAttributeMaxDynamicSharedMemorySize, G_LDS);
    (void)hipFuncSetAttribute((const void*)k4_kernel, hipFuncAttributeMaxDynamicSharedMemorySize, K4_LDS);
    (void)hipFuncSetAttribute((const void*)k5_kernel, hipFuncAttributeMaxDynamicSharedMemorySize, C8_LDS);
    (void)hipFuncSetAttribute((const void*)k6_kernel, hipFuncAttributeMaxDynamicSharedMemorySize, G_LDS);
    attr = true;
  }
  k1_kernel<<<256, K1_THREADS, K1_LDS, stream>>>(x, wqkv, ws);
  k2_kernel<<<256, 768, ATT_LDS, stream>>>(wo, w_in, w_out, w_sp, ws);
  k3_kernel<<<256, 768, G_LDS, stream>>>(x, wo, w_in, ws);
  k4_kernel<<<256, 768, K4_LDS, stream>>>(ws);
  k5_kernel<<<256, 768, C8_LDS, stream>>>(w_sp, b_sp, ws);
  k6_kernel<<<192, 768, G_LDS, stream>>>(w_out, out, ws);
}
```

```cpp
#define PSTRIDE 4096
#include <hip/hip_runtime.h>
#include <hip/hip_bf16.h>
#include <stdint.h>
typedef unsigned short u16;
using bf16x8 = __attribute__((ext_vector_type(8))) short;
using s16x4 = __attribute__((ext_vector_type(4))) short;
using f32x4 = __attribute__((ext_vector_type(4))) float;
using f32x16 = __attribute__((ext_vector_type(16))) float;
using u32x4 = __attribute__((ext_vector_type(4))) unsigned;
using u32x2 = __attribute__((ext_vector_type(2))) unsigned;
using u32x32 = __attribute__((ext_vector_type(32))) unsigned;
typedef __bf16 bf16x2_t __attribute__((ext_vector_type(2)));
typedef float f32x2_t __attribute__((ext_vector_type(2)));
constexpr int S = 2048, D = 576, H = 32, DH = 18, F = 4608, FH = 2304;
#define DI __device__ __forceinline__
#define LAS __attribute__((address_space(3)))
#define GAS __attribute__((address_space(1)))
DI int lane_id() { int l = __builtin_amdgcn_mbcnt_hi(~0u, __builtin_amdgcn_mbcnt_lo(~0u, 0u)); asm volatile("" : "+v"(l)); return l; }
DI float bf2f(u16 x) { return __uint_as_float((unsigned)x << 16); }
DI float bflo(unsigned x) { return __uint_as_float(x << 16); }
DI float bfhi(unsigned x) { return __uint_as_float(x & 0xffff0000u); }
DI unsigned pk2(float lo, float hi) { f32x2_t v = {lo, hi}; return __builtin_bit_cast(unsigned, __builtin_convertvector(v, bf16x2_t)); }
#define MFMA32(a, b, c) __builtin_amdgcn_mfma_f32_32x32x16_bf16((a), (b), (c), 0, 0, 0)
#define MFMA16(a, b, c) __builtin_amdgcn_mfma_f32_16x16x32_bf16((a), (b), (c), 0, 0, 0)
#define SB() __builtin_amdgcn_sched_barrier(0)
#define SGB(mask, n) __builtin_amdgcn_sched_group_barrier(mask, n, 0)
#define PIN(x) asm volatile("" : "+v"(x))
#define BAR() __builtin_amdgcn_s_barrier()
#define WAITV(n) asm volatile("s_waitcnt vmcnt(%0)" :: "i"(n) : "memory")
#define WAITL0() asm volatile("s_waitcnt lgkmcnt(0)" ::: "memory")
DI void glds16(const void* g, void* l) { __builtin_amdgcn_global_load_lds((const GAS unsigned*)g, (LAS unsigned*)l, 16, 0, 0); }
DI void st16_wt(char* ws, size_t off, u32x4 v) {
  const __amdgpu_buffer_rsrc_t r = __builtin_amdgcn_make_buffer_rsrc((void*)ws, 0, 128 << 20, 0x00020000);
  __builtin_amdgcn_raw_buffer_store_b128(v, r, (unsigned)off, 0, 16);
}
typedef LAS const char* lds_cptr;
typedef LAS char* lds_ptr;
#define LDS_RD128(p) (*(const LAS bf16x8*)(p))
#define LDS_RD32(p) (*(const LAS unsigned*)(p))
typedef short v4i16_t __attribute__((ext_vector_type(4)));
DI s16x4 lds_tr(lds_cptr p) { return __builtin_bit_cast(s16x4, __builtin_amdgcn_ds_read_tr16_b64_v4i16((LAS v4i16_t*)p)); }
DI bf16x8 lds_tr2(lds_cptr p0, lds_cptr p1) { const s16x4 a = lds_tr(p0), b = lds_tr(p1); return __builtin_shufflevector(a, b, 0, 1, 2, 3, 4, 5, 6, 7); }

#define TIMING 0
#define TS(k, i)
#define TSTAMP(v)
#define RSTAMP(v)

constexpr int KV_TILE = 2400, KV_K1 = 1024, KV_V = 1152, KV_VS = 624, KV_Z = KV_V + 608, KV_HEAD = 64 * KV_TILE;
constexpr int Q_TILE = 1152, Q_HEAD = 64 * Q_TILE;
constexpr size_t WS_KV = 0;
constexpr size_t WS_Q = 5u << 20;
constexpr size_t WS_O = 8u << 20;
constexpr size_t WS_H = 11u << 20;
constexpr size_t WS_ST2 = 14u << 20;
constexpr size_t WS_CS = 15u << 20;
constexpr size_t WS_XS = 16u << 20;
constexpr size_t WS_Z2T = 17u << 20;
constexpr size_t WS_Z1 = 27u << 20;
constexpr size_t WS_U = 37u << 20;
constexpr size_t WS_WIN = 47u << 20;
constexpr size_t WS_WO = 53u << 20;
constexpr size_t WS_WOUT = 54u << 20;
constexpr size_t WS_DBG = 64u << 20;

template <int NP, int NW, class SRC>
DI void first_tile(char* lds, int w, SRC src) {
#pragma unroll
  for (int j = 0; j < (NP + NW - 1) / NW; ++j) { const int p = w + NW * j; if (p < NP) glds16(src(p), lds + p * 1024); }
}
template <int NT, int NS, int STG, int NP, int NPX, int NLD, class SRC, class OFF, class POST>
DI void loader_loop(char* lds, int lw, SRC src, OFF off, POST post) {
  constexpr int PPW = (NP + NLD - 1) / NLD, PMIN = NP / NLD;
  static_assert((NS - 1) * PPW + 4 <= 63, "vmcnt");
  static_assert(NPX % NLD == 0, "X pieces");
  const char* base[PPW];
#pragma unroll
  for (int j = 0; j < PPW; ++j) { const int p = lw + NLD * j; base[j] = src(p < NP ? p : NP - 1); }
  auto issue = [&](int kt, int stage) {
    long ox, ow; off(kt, ox, ow);
#pragma unroll
    for (int j = 0; j < PPW; ++j) if (j < PMIN || lw + NLD * j < NP) glds16(base[j] + (j < NPX / NLD ? ox : ow), lds + stage * STG + (lw + NLD * j) * 1024);
  };
  issue(1, 1);
  WAITV(PMIN);
  BAR();
#pragma unroll
  for (int t = 2; t < NS - 1; ++t) issue(t, t);
  post(0);
  int stg = NS - 1, pst = 1;
#pragma unroll 1
  for (int kt = 0; kt < NT - (NS - 1); ++kt) {
    WAITV((NS - 3) * PMIN);
    BAR();
    issue(kt + NS - 1, stg); stg = stg + 1 == NS ? 0 : stg + 1;
    post(pst); pst = pst + 1 == NS ? 0 : pst + 1;
  }
#pragma unroll
  for (int r = NS - 2; r >= 0; --r) {
    WAITV((r - 1 > 0 ? r - 1 : 0) * PMIN); BAR();
    if (r > 0) { post(pst); pst = pst + 1 == NS ? 0 : pst + 1; }
  }
}
struct NoPost { DI void operator()(int) const {} };
DI unsigned tn_off(int row, int lane, int ld_bytes) { return (unsigned)row * ld_bytes + ((((lane & 7) ^ ((row >> 1) & 7))) << 4); }
template <int BN> DI unsigned kn_off(int p, int lane, int ld_bytes) {
  constexpr int CPR = BN / 8;
  const int c = 64 * p + lane, kp = c / CPR, cc = c - kp * CPR, odd = (kp >> 3) & 1, k = kp ^ (odd << 2);
  int cs = cc;
  if (BN == 96) { int blk = (cc >> 1) - odd; if (blk < 0) blk += 6; cs = blk * 2 + (cc & 1); }
  return (unsigned)k * ld_bytes + (unsigned)cs * 16;
}

DI float gelu_t(float x) {
  const float c1 = -2.f * 0.7978845608028654f * 1.4426950408889634f, c2 = c1 * 0.044715f;
  const float e = __builtin_amdgcn_exp2f(x * __builtin_fmaf(x * x, c2, c1));
  return x * __builtin_amdgcn_rcpf(1.f + e);
}

constexpr int C8_STG = 272 * 128, C8_W = 16384, C8_NS = 4, C8_LDS = C8_NS * C8_STG;
struct C8Addr { lds_cptr x0, x1, w0, w1; };
template <bool BKN> DI C8Addr c8_addr(char* lds, int lane, int wr, int nb0) {
  lds_cptr L = (lds_cptr)lds; const int fr = lane & 15, fq = lane >> 4, sw = (fr >> 1) & 7; C8Addr a;
  a.x0 = L + (32 * wr + fr) * 128 + ((fq ^ sw) << 4); a.x1 = L + (32 * wr + fr) * 128 + (((4 + fq) ^ sw) << 4);
  if (BKN) { const int q = fr >> 2, p4 = lane & 3, b = C8_W + (8 * fq + q) * 288 + nb0 * 32 + p4 * 8;
             a.w0 = L + b + (fq & 1) * 1152; a.w1 = L + b + ((fq & 1) ^ 1) * 1152; }
  else { a.w0 = L + C8_W + (16 * nb0 + fr) * 128 + ((fq ^ sw) << 4); a.w1 = L + C8_W + (16 * nb0 + fr) * 128 + (((4 + fq) ^ sw) << 4); }
  return a;
}
template <int NTL> struct C8Frag { bf16x8 x[2], w[NTL]; };
template <int NTL, bool BKN> DI void c8_read(C8Frag<NTL>& f, const C8Addr& a, int st, int ks) {
  f.x[0] = LDS_RD128((ks ? a.x1 : a.x0) + st); f.x[1] = LDS_RD128((ks ? a.x1 : a.x0) + st + 2048);
#pragma unroll
  for (int n = 0; n < NTL; ++n) {
    if (BKN) f.w[n] = lds_tr2(a.w0 + st + ks * 9216 + n * 32, a.w1 + st + ks * 9216 + n * 32);
    else f.w[n] = LDS_RD128((ks ? a.w1 : a.w0) + st + n * 2048);
  }
}
template <int NTL, bool WX> DI void c8_mma(f32x4 (&acc)[NTL][2], const C8Frag<NTL>& f) {
#pragma unroll
  for (int n = 0; n < NTL; ++n) {
#pragma unroll
    for (int m = 0; m < 2; ++m) acc[n][m] = WX ? MFMA16(f.w[n], f.x[m], acc[n][m]) : MFMA16(f.x[m], f.w[n], acc[n][m]);
  }
}
template <int NTL, bool BKN> DI void c8_sched() {
  constexpr int NR = 2 + (BKN ? 2 * NTL : NTL), NM = 2 * NTL;
  if (NR <= NM) {
#pragma unroll
    for (int i = 0; i < NR; ++i) { SGB(0x8, 1); SGB(0x100, 1); }
    SGB(0x8, NM - NR);
  } else {
#pragma unroll
    for (int i = 0; i < NM; ++i) { SGB(0x8, 1); SGB(0x100, 1); if (i < NR - NM) SGB(0x100, 1); }
  }
}

constexpr int K1_THREADS = 768;
constexpr int EP_PITCH = 304;
template <int WC, bool WX>
DI void k1_compute(char* lds, int lane, int w, f32x4 (&acc)[WC == 0 ? 3 : 2][4], float* tabs, char* ws) {
  constexpr int NTL = WC == 0 ? 3 : 2, NB0 = WC == 0 ? 0 : 2 * WC + 1, NST = WC == 0 ? 0 : (WC == 1 ? 2 : 1), M0 = WC == 1 ? 0 : WC - 1, M1 = 3;
  const int wr = w & 1, fr = lane & 15, fq = lane >> 4, sw = (fr >> 1) & 7;
  lds_cptr L = (lds_cptr)lds;
  lds_cptr x0 = L + (64 * wr + fr) * 128 + ((fq ^ sw) << 4), x1 = L + (64 * wr + fr) * 128 + (((4 + fq) ^ sw) << 4);
  const int wb = C8_W + (8 * fq + (fr >> 2)) * 288 + NB0 * 32 + (lane & 3) * 8;
  lds_cptr w0 = L + wb + (fq & 1) * 1152, w1 = L + wb + ((fq & 1) ^ 1) * 1152;
  const u32x4 onesv = {0x3f803f80u, 0x3f803f80u, 0x3f803f80u, 0x3f803f80u}; const bf16x8 ones = __builtin_bit_cast(bf16x8, onesv);
  bf16x8 xa[4], wa[NTL], xb[4], wb_[NTL];
  f32x4 cs[NTL] = {}, rsum[2] = {}, rsq[2] = {};
#define K1RD(X, Wf, st, ks) do { _Pragma("unroll") for (int n = 0; n < NTL; ++n) Wf[n] = lds_tr2(w0 + (st) + (ks) * 9216 + n * 32, w1 + (st) + (ks) * 9216 + n * 32); \
    _Pragma("unroll") for (int m = 0; m < 4; ++m) X[m] = LDS_RD128(((ks) ? x1 : x0) + (st) + m * 2048); } while (0)
#define K1MM(X, Wf) do { _Pragma("unroll") for (int n = 0; n < NTL; ++n) { _Pragma("unroll") for (int m = 0; m < 4; ++m) acc[n][m] = WX ? MFMA16(Wf[n], X[m], acc[n][m]) : MFMA16(X[m], Wf[n], acc[n][m]); \
    cs[n] = MFMA16(ones, Wf[n], cs[n]); } \
    if (NST >= 1) { rsum[0] = MFMA16(X[M0], ones, rsum[0]); rsq[0] = MFMA16(X[M0], X[M0], rsq[0]); } \
    if (NST >= 2) { rsum[1] = MFMA16(X[M1], ones, rsum[1]); rsq[1] = MFMA16(X[M1], X[M1], rsq[1]); } } while (0)
#define K1SCHED() do { _Pragma("unroll") for (int i_ = 0; i_ < 2 * NTL + 4; ++i_) { SGB(0x8, 1); SGB(0x100, 1); } SGB(0x8, 5 * NTL + 2 * NST - (2 * NTL + 4)); } while (0)
  WAITV(0); BAR();
  TS(1, 1);
  K1RD(xa, wa, 0, 0);
  int st = 0;
#pragma unroll 1
  for (int kt = 0; kt < 9; ++kt) {
    const int stn = st + C8_STG == C8_LDS ? 0 : st + C8_STG;
    K1RD(xb, wb_, st, 1); K1MM(xa, wa); K1SCHED();
    SB(); BAR(); SB();
    K1RD(xa, wa, stn, 0); K1MM(xb, wb_); K1SCHED();
    SB();
    st = stn;
  }
  TS(1, 2);
  const int l2 = lane_id(), r2 = l2 & 15, q2 = l2 >> 4;
  if (l2 < 16 && wr == 0) {
#pragma unroll
    for (int n = 0; n < NTL; ++n) tabs[256 + 16 * (NB0 + n) + l2] = cs[n][0];
  }
#pragma unroll
  for (int t = 0; t < NST; ++t) {
    const int mrow = 64 * wr + 16 * (t == 0 ? M0 : M1);
    if (r2 == 0) *(f32x4*)(tabs + mrow + 4 * q2) = rsum[t];
    if (q2 == (r2 >> 2)) tabs[128 + mrow + r2] = (r2 & 2) ? ((r2 & 1) ? rsq[t][3] : rsq[t][2]) : ((r2 & 1) ? rsq[t][1] : rsq[t][0]);
  }
}
template <int NTL>
DI void k1_epilogue(char* lds, int lane, int w, int type, int rt, int hg, const float* tabs, f32x4 (&acc)[NTL][4], char* kvimg) {
  asm volatile("" : "+v"(lane));
  const int wr = w & 1, wc = w >> 1, nb0 = wc == 0 ? 0 : 2 * wc + 1, fr = lane & 15, fq = lane >> 4;
  const float* rs = tabs; const float* mr = tabs + 128; const float* cs = tabs + 256;
  if (type == 2) {
#pragma unroll
    for (int n = 0; n < NTL; ++n) {
      const int col = 16 * (nb0 + n) + fr, hh = col / 18, d = col - hh * 18; const float c = cs[col];
      char* hb = kvimg + (size_t)(hg * 8 + hh) * KV_HEAD + (size_t)(rt * 4 + 2 * wr) * KV_TILE + KV_V + (fq & 1) * 304 + (fq >> 1) * 8 + d * 16;
#pragma unroll
      for (int m = 0; m < 4; ++m) {
        const int row = 64 * wr + 16 * m + 4 * fq;
        const f32x4 r4 = *(const f32x4*)(rs + row), m4 = *(const f32x4*)(mr + row);
        u32x2 o; o[0] = pk2(acc[n][m][0] * r4[0] - m4[0] * c, acc[n][m][1] * r4[1] - m4[1] * c); o[1] = pk2(acc[n][m][2] * r4[2] - m4[2] * c, acc[n][m][3] * r4[3] - m4[3] * c);
        *(u32x2*)(hb + (m >> 1) * KV_TILE + (m & 1) * KV_VS) = o;
      }
    }
  } else {
    const float qs = type == 0 ? 0.23570226039551584f * 1.4426950408889634f : 1.f;
#pragma unroll
    for (int m = 0; m < 4; ++m) {
      const int row = 64 * wr + 16 * m + fr; const float r = rs[row] * qs, mm = mr[row] * qs;
#pragma unroll
      for (int n = 0; n < NTL; ++n) {
        const int col = 16 * (nb0 + n) + 4 * fq; const f32x4 c4 = *(const f32x4*)(cs + col);
        u32x2 o; o[0] = pk2(acc[n][m][0] * r - mm * c4[0], acc[n][m][1] * r - mm * c4[1]); o[1] = pk2(acc[n][m][2] * r - mm * c4[2], acc[n][m][3] * r - mm * c4[3]);
        *(u32x2*)(lds + row * EP_PITCH + col * 2) = o;
      }
    }
  }
}
__global__ __launch_bounds__(K1_THREADS, 3) void k1_kernel(const u16* __restrict__ x, const u16* __restrict__ wqkv, char* __restrict__ ws,
    const char* __restrict__ p_wo, const char* __restrict__ p_win, const char* __restrict__ p_wsp, const char* __restrict__ p_wout, const char* __restrict__ p_out) {
  extern __shared__ __attribute__((aligned(16))) char lds[];
  const int tid = threadIdx.x, lane = tid & 63; const int w = __builtin_amdgcn_readfirstlane(tid >> 6);
  const int b = blockIdx.x;
  TS(1, 0); TS(1, 6);
  if (b >= 192) {
    if (tid < 512) ((u32x4*)(ws + WS_XS))[(b - 192) * 512 + tid] = u32x4{0, 0, 0, 0};
    {
      const int part = (b - 192) >> 3;
      const int t = part * K1_THREADS + tid;
      unsigned acc = 0;
      auto touch = [&](const char* p, size_t bytes) { for (size_t o = (size_t)t * PSTRIDE; o < bytes; o += (size_t)8 * K1_THREADS * PSTRIDE) acc ^= *(const volatile unsigned*)(p + o); };
      touch(ws, 57u << 20); touch(p_wo, (size_t)D * D * 2); touch(p_win, (size_t)D * F * 2); touch(p_wsp, (size_t)S * S * 2); touch(p_wout, (size_t)FH * D * 2); touch(p_out, (size_t)S * D * 2);
      if (acc == 0x13572468u && tid == 777) ((unsigned*)(ws + WS_DBG))[1] = acc;
    }
    return;
  }
  const int xc = b & 7, idx = b >> 3;
  const int rt = xc * 2 + (idx & 1), ct = idx >> 1;
  const int type = ct >> 2, hg = ct & 3;
  float* tabs = (float*)(lds + C8_LDS);
  char* kvimg = ws + WS_KV; char* qimg = ws + WS_Q;
  const char* Xb = (const char*)(x + (size_t)rt * 128 * D); const char* Wb = (const char*)(wqkv + ct * 144);
  auto src = [&](int p) { return p < 16 ? Xb + tn_off(p * 8 + (lane >> 3), lane, D * 2) : Wb + kn_off<144>(p - 16, lane, 1728 * 2); };
  first_tile<34, 12>(lds, w, src);
  if (w >= 8) {
    __builtin_amdgcn_s_setprio(2);
    loader_loop<9, C8_NS, C8_STG, 34, 16, 4>(lds, w - 8, src, [&](int kt, long& ox, long& ow) { ox = (long)kt * 128; ow = (long)kt * 64 * 1728 * 2; }, NoPost());
    __syncthreads();
    const int row = (w - 8) * 64 + lane_id();
    if (row < 128) { const float mean = tabs[row] * (1.f / D), var = fmaxf(tabs[128 + row] * (1.f / D) - mean * mean, 0.f), rstd = rsqrtf(var + 1e-5f); tabs[row] = rstd; tabs[128 + row] = mean * rstd; }
    __syncthreads();
  } else {
#define K1_PATH(WC, WX) do { f32x4 acc[WC == 0 ? 3 : 2][4] = {}; \
      k1_compute<WC, WX>(lds, lane, w, acc, tabs, ws); \
      __syncthreads(); __syncthreads(); \
      k1_epilogue<WC == 0 ? 3 : 2>(lds, lane_id(), w, WX ? (type & 1) : 2, rt, hg, tabs, acc, kvimg); } while (0)
#define K1_SEL(WX) do { if (w < 2) K1_PATH(0, WX); else if (w < 4) K1_PATH(1, WX); else if (w < 6) K1_PATH(2, WX); else K1_PATH(3, WX); } while (0)
    if (type == 2) K1_SEL(false); else K1_SEL(true);
  }
  const int tid2 = w * 64 + lane_id();
  if (type == 2) {
    if (tid2 < 192) {
      const int tl = tid2 / 48, r = tid2 - tl * 48, hh = r / 6, c = r - hh * 6;
      char* tb = kvimg + (size_t)(hg * 8 + hh) * KV_HEAD + (size_t)(rt * 4 + tl) * KV_TILE;
      if (c < 4) *(u32x4*)(tb + KV_V + (c >> 1) * KV_VS + (c & 1) * 304 + 18 * 16) = u32x4{0x3f803f80u, 0x3f803f80u, 0x3f803f80u, 0x3f803f80u};
      else *(u32x4*)(tb + KV_Z + (c - 4) * KV_VS) = u32x4{0, 0, 0, 0};
    }
    TS(1, 3); TS(1, 7);
    return;
  }
  __syncthreads();
  for (int it = tid2; it < 1024; it += K1_THREADS) {
    const int tok = it & 127, hh = it >> 7;
    const unsigned* sp = (const unsigned*)(lds + tok * EP_PITCH + hh * 36);
    u32x4 c0 = {sp[0], sp[1], sp[2], sp[3]}, c1 = {sp[4], sp[5], sp[6], sp[7]}; const unsigned c2 = sp[8];
    char* tb = type == 0 ? qimg + (size_t)(hg * 8 + hh) * Q_HEAD + (size_t)(rt * 4 + (tok >> 5)) * Q_TILE
                         : kvimg + (size_t)(hg * 8 + hh) * KV_HEAD + (size_t)(rt * 4 + (tok >> 5)) * KV_TILE;
    const int key = tok & 31;
    *(u32x4*)(tb + key * 16) = c0; *(u32x4*)(tb + 512 + key * 16) = c1; *(unsigned*)(tb + 1024 + key * 4) = c2;
  }
  TS(1, 3); TS(1, 7);
}
static_assert(128 * EP_PITCH <= C8_LDS, "");
constexpr int K1_LDS = C8_LDS + 400 * 4;

constexpr int ATT_LDS = KV_HEAD + 8 * Q_TILE;
#define SWITCH 50
__global__ __launch_bounds__(768, 3) void k2_kernel(const u16* __restrict__ wo, const u16* __restrict__ w_in, const u16* __restrict__ w_out, const u16* __restrict__ w_sp, char* __restrict__ ws) {
  extern __shared__ __attribute__((aligned(16))) char lds[];
  TS(2, 0); TS(2, 6);
  const char* kvimg = ws + WS_KV; const char* qimg = ws + WS_Q; u16* o = (u16*)(ws + WS_O);
  const int tid = threadIdx.x, lane = tid & 63; const int w = __builtin_amdgcn_readfirstlane(tid >> 6);
  const int b = blockIdx.x, xcd = b & 7, idx = b >> 3, h = xcd * 4 + (idx >> 3), qb = idx & 7;
  const char* kvg = kvimg + (size_t)h * KV_HEAD + lane * 16;
  if (w < 8) {
    const char* qg = qimg + (size_t)h * Q_HEAD + (qb * 8 + w) * Q_TILE + lane * 16;
    char* qtile = lds + KV_HEAD + w * Q_TILE;
    glds16(qg, qtile);
    if (lane < 8) glds16(qg + 1024, qtile + 1024);
  }
  glds16(kvg + w * 1024, lds + w * 1024); glds16(kvg + (w + 12) * 1024, lds + (w + 12) * 1024);
  WAITV(0);
  BAR();
  if (w >= 8) {
    const int lw = w - 8;
    __builtin_amdgcn_s_setprio(3);
#pragma unroll 1
    for (int p = 24 + lw; p < 150; p += 4) glds16(kvg + p * 1024, lds + p * 1024);
    WAITV(0);
    BAR();
    {
      const u32x4* wp = (const u32x4*)w_sp + (size_t)(b * 4 + lw) * 512 + lane;
      u32x4 acc = wp[0];
#pragma unroll
      for (int i = 1; i < 8; ++i) acc ^= wp[i * 64];
      if (acc[0] == 0x12345678u && acc[1] == 0x9abcdef0u && acc[2] == 0x0fedcba9u && acc[3] == 0x87654321u) ((unsigned*)(ws + WS_DBG))[0] = 1;
    }
    const int gw = b * 4 + lw, c = lane & 7, nb = lane >> 3;
    if (nb < 6) {
#pragma unroll 1
      for (int blk = gw; blk < 864 + 108 + 432; blk += 1024) {
        const u16* sp; int ld, kt, n0; size_t db;
        if (blk < 864) { kt = blk / 96; const int nbk = blk - kt * 96, z2 = nbk >= 48, nn = z2 ? nbk - 48 : nbk, t = nn / 3; n0 = nbk * 48; sp = w_in; ld = F;
          db = WS_WIN + ((size_t)((z2 ? 0 : 16) + t) * 9 + kt) * 18432 + (nn - t * 3) * 48 * 128; }
        else if (blk < 972) { const int q = blk - 864; kt = q / 12; const int nbk = q - kt * 12; n0 = nbk * 48; sp = wo; ld = D; db = WS_WO + ((size_t)(nbk >> 1) * 9 + kt) * 12288 + (nbk & 1) * 48 * 128; }
        else { const int q = blk - 972; kt = q / 12; const int nbk = q - kt * 12; n0 = nbk * 48; sp = w_out; ld = D; db = WS_WOUT + ((size_t)(nbk >> 1) * 36 + kt) * 12288 + (nbk & 1) * 48 * 128; }
        sp += (size_t)(kt * 64 + c * 8) * ld + n0 + nb * 8;
        u32x4 r[8];
#pragma unroll
        for (int j = 0; j < 8; ++j) r[j] = *(const u32x4*)(sp + (size_t)j * ld);
        const unsigned lo = (unsigned)db + nb * 1024;
#pragma unroll
        for (int e = 0; e < 4; ++e) {
          const unsigned sl = (unsigned)((c ^ ((4 * nb + e) & 7)) << 4);
#pragma unroll
          for (int hi = 0; hi < 2; ++hi) {
            const unsigned sel = hi ? 0x07060302u : 0x05040100u;
            u32x4 o;
#pragma unroll
            for (int q = 0; q < 4; ++q) o[q] = __builtin_amdgcn_perm(r[2 * q + 1][e], r[2 * q][e], sel);
            st16_wt(ws, lo + (2 * e + hi) * 128 + sl, o);
          }
        }
      }
    }
    return;
  }
  TS(2, 1);
  if (w >= 4) __builtin_amdgcn_s_setprio(1);
  const int r32 = lane & 31, hh = lane >> 5;
  lds_cptr L = (lds_cptr)lds;
  const bf16x8 qf0 = LDS_RD128(L + KV_HEAD + w * Q_TILE + lane * 16);
  u32x4 q1v = {0, 0, 0, 0}; { unsigned t = LDS_RD32(L + KV_HEAD + w * Q_TILE + 1024 + r32 * 4); q1v[0] = hh ? 0u : t; }
  const bf16x8 qf1 = __builtin_bit_cast(bf16x8, q1v);
  lds_cptr kp0 = L + lane * 16, kp1 = L + (hh ? KV_Z : KV_K1 + r32 * 4), vp = L + (r32 <= 18 ? KV_V + hh * 304 + r32 * 16 : KV_Z);
  f32x16 negm;
  {
    bf16x8 k0 = LDS_RD128(kp0); u32x4 k1 = {LDS_RD32(kp1), 0, 0, 0};
    f32x16 s = {}; s = MFMA32(k0, qf0, s); s = MFMA32(__builtin_bit_cast(bf16x8, k1), qf1, s);
    float m = s[0];
#pragma unroll
    for (int i = 1; i < 16; ++i) m = fmaxf(m, s[i]);
    auto rr = __builtin_amdgcn_permlane32_swap(__float_as_uint(m), __float_as_uint(m), false, false);
    m = fmaxf(__uint_as_float(rr[0]), __uint_as_float(rr[1]));
#pragma unroll
    for (int i = 0; i < 16; ++i) negm[i] = -m;
  }
  f32x16 O = {};
#define EX2(Sx, i) pk2(__builtin_amdgcn_exp2f(Sx[i]), __builtin_amdgcn_exp2f(Sx[(i) + 1]))
  bf16x8 k0, v0, v1; u32x4 k1 = {0, 0, 0, 0};
  f32x16 SA, SBc; u32x4 pA0, pA1, pB0, pB1;
#define STEP(Sc, Sn, Pc0, Pc1, Pp0, Pp1, koff, voff) do { \
    SB(); Sn = MFMA32(k0, qf0, negm); SB(); \
    Pc0[0] = EX2(Sc, 0); Pc0[1] = EX2(Sc, 2); PIN(Pc0); SB(); \
    Sn = MFMA32(__builtin_bit_cast(bf16x8, k1), qf1, Sn); SB(); \
    k0 = LDS_RD128(kp0 + (koff)); k1[0] = LDS_RD32(kp1 + (koff)); SB(); \
    Pc0[2] = EX2(Sc, 4); Pc0[3] = EX2(Sc, 6); PIN(Pc0); SB(); \
    O = MFMA32(v0, __builtin_bit_cast(bf16x8, Pp0), O); SB(); \
    Pc1[0] = EX2(Sc, 8); Pc1[1] = EX2(Sc, 10); PIN(Pc1); SB(); \
    O = MFMA32(v1, __builtin_bit_cast(bf16x8, Pp1), O); SB(); \
    v0 = LDS_RD128(vp + (voff)); v1 = LDS_RD128(vp + (voff) + KV_VS); SB(); \
    Pc1[2] = EX2(Sc, 12); Pc1[3] = EX2(Sc, 14); PIN(Pc1); SB(); \
  } while (0)
  k0 = LDS_RD128(kp0); k1[0] = LDS_RD32(kp1);
  SA = MFMA32(k0, qf0, negm); SA = MFMA32(__builtin_bit_cast(bf16x8, k1), qf1, SA);
  k0 = LDS_RD128(kp0 + KV_TILE); k1[0] = LDS_RD32(kp1 + KV_TILE);
  v0 = LDS_RD128(vp); v1 = LDS_RD128(vp + KV_VS);
  pB0 = u32x4{0, 0, 0, 0}; pB1 = u32x4{0, 0, 0, 0};
  int off = 0;
  for (int t = 0; t < 8; t += 2) {
    STEP(SA, SBc, pA0, pA1, pB0, pB1, off + 2 * KV_TILE, off);
    STEP(SBc, SA, pB0, pB1, pA0, pA1, off + 3 * KV_TILE, off + KV_TILE);
    off += 2 * KV_TILE;
  }
  BAR();
  for (int t = 8; t < 62; t += 2) {
    if (t == SWITCH) __builtin_amdgcn_s_setprio(0);
    STEP(SA, SBc, pA0, pA1, pB0, pB1, off + 2 * KV_TILE, off);
    STEP(SBc, SA, pB0, pB1, pA0, pA1, off + 3 * KV_TILE, off + KV_TILE);
    off += 2 * KV_TILE;
  }
  STEP(SA, SBc, pA0, pA1, pB0, pB1, off + KV_TILE, off);
  STEP(SBc, SA, pB0, pB1, pA0, pA1, off + KV_TILE, off + KV_TILE);
  O = MFMA32(v0, __builtin_bit_cast(bf16x8, pB0), O); O = MFMA32(v1, __builtin_bit_cast(bf16x8, pB1), O);
  TS(2, 2);
  {
    float mx = 0.f;
#pragma unroll
    for (int i = 0; i < 11; ++i) mx = fmaxf(mx, fabsf(O[i]));
    bool bad = !(mx < 1.2676506e30f);
#pragma unroll
    for (int i = 0; i < 11; ++i) bad |= (O[i] != O[i]);
    if (__any(bad)) {
      float m = -3.0e38f; O = f32x16{};
#pragma unroll 1
      for (int t = 0; t < 64; ++t) {
        const int of = t * KV_TILE;
        const bf16x8 a0 = LDS_RD128(kp0 + of); const u32x4 a1 = {LDS_RD32(kp1 + of), 0, 0, 0};
        f32x16 s = {}; s = MFMA32(a0, qf0, s); s = MFMA32(__builtin_bit_cast(bf16x8, a1), qf1, s);
        float tm = s[0];
#pragma unroll
        for (int i = 1; i < 16; ++i) tm = fmaxf(tm, s[i]);
        auto rr = __builtin_amdgcn_permlane32_swap(__float_as_uint(tm), __float_as_uint(tm), false, false);
        tm = fmaxf(__uint_as_float(rr[0]), __uint_as_float(rr[1]));
        const float mn = fmaxf(m, tm), al = __builtin_amdgcn_exp2f(m - mn); m = mn;
#pragma unroll
        for (int i = 0; i < 16; ++i) { O[i] *= al; s[i] -= mn; }
        u32x4 p0, p1;
#pragma unroll
        for (int i = 0; i < 4; ++i) { p0[i] = EX2(s, 2 * i); p1[i] = EX2(s, 8 + 2 * i); }
        O = MFMA32(LDS_RD128(vp + of), __builtin_bit_cast(bf16x8, p0), O); O = MFMA32(LDS_RD128(vp + of + KV_VS), __builtin_bit_cast(bf16x8, p1), O);
      }
    }
  }
  {
    float l = O[10];
    { auto rr = __builtin_amdgcn_permlane32_swap(__float_as_uint(l), __float_as_uint(l), false, false); l = __uint_as_float(rr[0]); }
    const float inv = 1.f / l;
    u16* orow = o + (size_t)(qb * 256 + w * 32 + r32) * D + h * DH;
    const unsigned a0 = pk2(O[0] * inv, O[1] * inv), a1_ = pk2(O[2] * inv, O[3] * inv), a2_ = pk2(O[4] * inv, O[5] * inv), a3 = pk2(O[6] * inv, O[7] * inv), a4 = pk2(O[8] * inv, O[9] * inv);
    unsigned* op = (unsigned*)(orow + hh * 4);
    op[0] = a0; op[1] = a1_; op[4] = a2_; op[5] = a3;
    if (hh == 0) op[8] = a4;
  }
  TS(2, 3); TS(2, 7);
}

constexpr int G_STG = 160 * 128, G_W = 8192, G_NS = 6, G_LDS = G_NS * G_STG;
template <int K, bool STATS, int KID>
DI void g_body(char* ws, char* lds, const u16* __restrict__ A, const char* __restrict__ W, const u16* __restrict__ res, u16* __restrict__ out, float* __restrict__ st2, int b) {
  constexpr int NT = K / 64;
  const int tid = threadIdx.x, lane = tid & 63; const int w = __builtin_amdgcn_readfirstlane(tid >> 6);
  const int xc = b & 7, idx = b >> 3, rt = xc * 4 + (idx & 3), ct = idx >> 2;
  const int m0 = rt * 64, n0 = ct * 96;
  TS(KID, 0); TS(KID, 6);
  const char* Xb = (const char*)(A + (size_t)m0 * K); const char* Wb = W + (size_t)ct * NT * 12288;
  auto src = [&](int p) { return p < 8 ? Xb + tn_off(p * 8 + (lane >> 3), lane, K * 2) : Wb + (p - 8) * 1024 + lane * 16; };
  first_tile<20, 12>(lds, w, src);
  if (w >= 4) {
    __builtin_amdgcn_s_setprio(2);
    loader_loop<NT, G_NS, G_STG, 20, 8, 8>(lds, w - 4, src, [&](int kt, long& ox, long& ow) { ox = (long)kt * 128; ow = (long)kt * 12288; }, NoPost());
    return;
  }
  const int wr = w & 1, wc = w >> 1, fr = lane & 15, fq = lane >> 4, sw = (fr >> 1) & 7;
  lds_cptr L = (lds_cptr)lds;
  lds_cptr x0 = L + (32 * wr + fr) * 128 + ((fq ^ sw) << 4), x1 = L + (32 * wr + fr) * 128 + (((4 + fq) ^ sw) << 4);
  lds_cptr w0 = L + G_W + (48 * wc + fr) * 128 + ((fq ^ sw) << 4), w1 = L + G_W + (48 * wc + fr) * 128 + (((4 + fq) ^ sw) << 4);
  f32x4 acc[3][2] = {};
  bf16x8 xa[2], wa[3], xb[2], wb_[3];
#define GRD(X, Wf, st, ks) do { X[0] = LDS_RD128(((ks) ? x1 : x0) + (st)); X[1] = LDS_RD128(((ks) ? x1 : x0) + (st) + 2048); \
    _Pragma("unroll") for (int n = 0; n < 3; ++n) Wf[n] = LDS_RD128(((ks) ? w1 : w0) + (st) + n * 2048); } while (0)
#define GSCHED() do { _Pragma("unroll") for (int i_ = 0; i_ < 5; ++i_) { SGB(0x8, 1); SGB(0x100, 1); } SGB(0x8, 1); } while (0)
#define GMM(X, Wf) do { _Pragma("unroll") for (int n = 0; n < 3; ++n) { acc[n][0] = MFMA16(Wf[n], X[0], acc[n][0]); acc[n][1] = MFMA16(Wf[n], X[1], acc[n][1]); } } while (0)
  WAITV(0); BAR();
  TS(KID, 1);
  GRD(xa, wa, 0, 0);
  u32x2 rv[2][3];
#pragma unroll
  for (int m = 0; m < 2; ++m)
#pragma unroll
    for (int n = 0; n < 3; ++n) rv[m][n] = *(const u32x2*)(res + (size_t)(m0 + 32 * wr + 16 * m + fr) * D + n0 + 48 * wc + 16 * n + 4 * fq);
  int st = 0;
#pragma unroll 1
  for (int kt = 0; kt < NT; ++kt) {
    const int stn = st + G_STG == G_LDS ? 0 : st + G_STG;
    GRD(xb, wb_, st, 1);
    GMM(xa, wa);
    GSCHED();
    SB(); BAR(); SB();
    GRD(xa, wa, stn, 0);
    GMM(xb, wb_);
    GSCHED();
    SB();
    st = stn;
  }
  TS(KID, 2);
#pragma unroll
  for (int m = 0; m < 2; ++m) {
    const int row = m0 + 32 * wr + 16 * m + fr;
    float sum = 0.f, ssq = 0.f;
#pragma unroll
    for (int n = 0; n < 3; ++n) {
      const int col = n0 + 48 * wc + 16 * n + 4 * fq;
      const u32x2 r = rv[m][n];
      u32x2 o; o[0] = pk2(bflo(r[0]) + acc[n][m][0], bfhi(r[0]) + acc[n][m][1]); o[1] = pk2(bflo(r[1]) + acc[n][m][2], bfhi(r[1]) + acc[n][m][3]);
      *(u32x2*)(out + (size_t)row * D + col) = o;
      if (STATS) { const float a = bflo(o[0]), bq = bfhi(o[0]), c = bflo(o[1]), d = bfhi(o[1]); sum += (a + bq) + (c + d); ssq += (a * a + bq * bq) + (c * c + d * d); }
    }
    if (STATS) {
      sum += __shfl_xor(sum, 16); sum += __shfl_xor(sum, 32); ssq += __shfl_xor(ssq, 16); ssq += __shfl_xor(ssq, 32);
      if (fq == 0) *(f32x2_t*)(st2 + ((size_t)row * 12 + ct * 2 + wc) * 2) = f32x2_t{sum, ssq};
    }
  }
  TS(KID, 3); TS(KID, 7);
}
__global__ __launch_bounds__(768, 3) void k3_kernel(const u16* __restrict__ x, const u16* __restrict__ wo, const u16* __restrict__ w_in, char* __restrict__ ws) {
  extern __shared__ __attribute__((aligned(16))) char lds[];
  const int b = blockIdx.x;
  if (b >= 192) {
    const int tid = threadIdx.x, c = tid % 9, rg = tid / 9;
    float* red = (float*)lds;
    if (rg < 48) {
      const char* p = (const char*)(w_in + (b - 192) * 72 + c * 8) + (size_t)rg * (F * 2);
      u32x4 v[12];
#pragma unroll
      for (int i = 0; i < 12; ++i) v[i] = *(const u32x4*)(p + (size_t)i * 48 * F * 2);
      float a[8] = {};
#pragma unroll
      for (int i = 0; i < 12; ++i)
#pragma unroll
        for (int e = 0; e < 4; ++e) { a[2 * e] += bflo(v[i][e]); a[2 * e + 1] += bfhi(v[i][e]); }
#pragma unroll
      for (int e = 0; e < 8; ++e) red[rg * 72 + c * 8 + e] = a[e];
    }
    __syncthreads();
    if (tid < 72) { float t = 0.f; for (int g = 0; g < 48; ++g) t += red[g * 72 + tid]; ((float*)(ws + WS_CS))[(b - 192) * 72 + tid] = t; }
    return;
  }
  g_body<D, true, 3>(ws, lds, (const u16*)(ws + WS_O), ws + WS_WO, x, (u16*)(ws + WS_H), (float*)(ws + WS_ST2), b);
}
__global__ __launch_bounds__(768, 3) void k6_kernel(const u16* __restrict__ w_out, u16* __restrict__ out, char* __restrict__ ws) {
  extern __shared__ __attribute__((aligned(16))) char lds[];
  g_body<FH, false, 0>(ws, lds, (const u16*)(ws + WS_U), ws + WS_WOUT, (const u16*)(ws + WS_H), out, nullptr, blockIdx.x);
}

constexpr unsigned XTAG = 0x9E3779B9u;
DI float dpp_sum16(float v) {
  v += __builtin_bit_cast(float, __builtin_amdgcn_mov_dpp(__builtin_bit_cast(int, v), 0xB1, 0xf, 0xf, true));
  v += __builtin_bit_cast(float, __builtin_amdgcn_mov_dpp(__builtin_bit_cast(int, v), 0x4E, 0xf, 0xf, true));
  v += __builtin_bit_cast(float, __builtin_amdgcn_mov_dpp(__builtin_bit_cast(int, v), 0x141, 0xf, 0xf, true));
  v += __builtin_bit_cast(float, __builtin_amdgcn_mov_dpp(__builtin_bit_cast(int, v), 0x140, 0xf, 0xf, true));
  return v;
}
constexpr int K4_STG = (128 + 288) * 128, K4_W2 = 16384, K4_W1 = 16384 + 18432, K4_NS = 3;
constexpr int K4_TAB = K4_NS * K4_STG;
constexpr int K4_PART = 40960, K4_ST2 = 45056;
constexpr int K4_LDS = K4_TAB + 544 * 4;
template <int NTL>
DI void k4_compute(char* lds, int lane, int w, int pi, int ft, char* ws) {
  const int wr = w & 3, nb0 = NTL == 5 ? 0 : 5, wc = NTL == 5 ? 0 : 1, fr = lane & 15, fq = lane >> 4, sw = (fr >> 1) & 7;
  lds_cptr L = (lds_cptr)lds;
  lds_cptr x0 = L + (32 * wr + fr) * 128 + ((fq ^ sw) << 4), x1 = L + (32 * wr + fr) * 128 + (((4 + fq) ^ sw) << 4);
  lds_cptr w0 = L + K4_W2 + (16 * nb0 + fr) * 128 + ((fq ^ sw) << 4), w1 = L + K4_W2 + (16 * nb0 + fr) * 128 + (((4 + fq) ^ sw) << 4);
  f32x4 acc2[NTL][2] = {}, acc1[NTL][2] = {};
  bf16x8 xa[2], xb[2], wa[NTL], wb[NTL];
#define RDX(X, P, st) do { X[0] = LDS_RD128(P + (st)); X[1] = LDS_RD128(P + (st) + 2048); } while (0)
#define RDW(Wf, P, st) do { _Pragma("unroll") for (int n = 0; n < NTL; ++n) Wf[n] = LDS_RD128(P + (st) + n * 2048); } while (0)
#define MM2(X, Wf) do { _Pragma("unroll") for (int n = 0; n < NTL; ++n) { acc2[n][0] = MFMA16(X[0], Wf[n], acc2[n][0]); acc2[n][1] = MFMA16(X[1], Wf[n], acc2[n][1]); } } while (0)
#define MM1(X, Wf) do { _Pragma("unroll") for (int n = 0; n < NTL; ++n) { acc1[n][0] = MFMA16(Wf[n], X[0], acc1[n][0]); acc1[n][1] = MFMA16(Wf[n], X[1], acc1[n][1]); } } while (0)
#define SCH(NR) do { _Pragma("unroll") for (int i_ = 0; i_ < (NR); ++i_) { SGB(0x8, 1); SGB(0x100, 1); } SGB(0x8, 2 * NTL - (NR)); SB(); } while (0)
  WAITV(0); WAITL0();
  if (NTL == 4) BAR();
  int st = 0;
#pragma unroll 1
  for (int kt = 0; kt < 9; ++kt) {
    BAR(); SB();
    if (kt == 0) TS(4, 1);
    RDX(xa, x0, st); RDW(wa, w0, st); SB();
    RDW(wb, w0, st + 18432); MM2(xa, wa); SCH(NTL);
    RDX(xb, x1, st); RDW(wa, w1, st); MM1(xa, wb); SCH(NTL + 2);
    BAR(); SB();
    RDW(wb, w1, st + 18432); MM2(xb, wa); SCH(NTL);
    MM1(xb, wb); SB();
    st = st + K4_STG == K4_TAB ? 0 : st + K4_STG;
  }
  if (NTL == 5) BAR();
  TS(4, 4);
  int lz = lane_id();
  const float* tab = (const float*)(lds + K4_TAB); const float* rs = tab; const float* mr = tab + 128; const float* cs = tab + 256;
  float* part = (float*)(lds + K4_PART); const float* st2 = (const float*)(lds + K4_ST2);
  u32x2 z2p[NTL][2];
  {
    const int fr = lz & 15, fq = lz >> 4;
#pragma unroll
    for (int m = 0; m < 2; ++m) {
      const f32x4 r4 = *(const f32x4*)(rs + 32 * wr + 16 * m + 4 * fq), m4 = *(const f32x4*)(mr + 32 * wr + 16 * m + 4 * fq);
      f32x4 su = {0, 0, 0, 0}, sq = {0, 0, 0, 0};
#pragma unroll
      for (int n = 0; n < NTL; ++n) {
        const float c = cs[16 * (nb0 + n) + fr];
        f32x4 g;
#pragma unroll
        for (int r = 0; r < 4; ++r) { g[r] = gelu_t(acc2[n][m][r] * r4[r] - m4[r] * c); su[r] += g[r]; sq[r] = __builtin_fmaf(g[r], g[r], sq[r]); }
        z2p[n][m][0] = pk2(g[0], g[1]); z2p[n][m][1] = pk2(g[2], g[3]);
      }
#pragma unroll
      for (int r = 0; r < 4; ++r) { su[r] = dpp_sum16(su[r]); sq[r] = dpp_sum16(sq[r]); }
      if (fr == 0) {
#pragma unroll
        for (int r = 0; r < 4; ++r) *(f32x2_t*)(part + (wc * 128 + 32 * wr + 16 * m + 4 * fq + r) * 2) = f32x2_t{su[r], sq[r]};
      }
    }
  }
  WAITL0(); BAR();
  if (wc == 0 && lz < 32) {
    const int row = 32 * wr + lz; const f32x2_t p0 = *(const f32x2_t*)(part + row * 2), p1 = *(const f32x2_t*)(part + (128 + row) * 2);
    const u32x4 g = {__float_as_uint(p0[0] + p1[0]), XTAG, __float_as_uint(p0[1] + p1[1]), XTAG};
    st16_wt(ws, WS_XS + (size_t)(((pi * 16 + ft) * 128 + row) * 16), g);
  }
  TS(4, 2);
  {
    const int fr = lz & 15, fq = lz >> 4;
    const size_t zb = WS_Z1 + ((size_t)(pi * 16 + ft) * 4 + wr) * (9 * 1024) + lz * 16;
    const float r0 = rs[32 * wr + fr], mm0 = mr[32 * wr + fr], r1 = rs[32 * wr + 16 + fr], mm1 = mr[32 * wr + 16 + fr];
#pragma unroll
    for (int n = 0; n < NTL; ++n) {
      const f32x4 c4 = *(const f32x4*)(cs + 144 + 16 * (nb0 + n) + 4 * fq);
      float g0[4], g1[4];
#pragma unroll
      for (int r = 0; r < 4; ++r) { g0[r] = gelu_t(acc1[n][0][r] * r0 - mm0 * c4[r]); g1[r] = gelu_t(acc1[n][1][r] * r1 - mm1 * c4[r]); }
      const u32x4 o = {pk2(g0[0], g0[1]), pk2(g0[2], g0[3]), pk2(g1[0], g1[1]), pk2(g1[2], g1[3])};
      st16_wt(ws, zb + (nb0 + n) * 1024, o);
    }
  }
  TS(4, 5);
  __syncthreads();
  asm volatile("" : "+v"(lz));
#pragma unroll
  for (int m = 0; m < 2; ++m) {
    const int fr = lz & 15, fq = lz >> 4;
    const int sl = 32 * wr + 16 * m + 4 * fq;
    const f32x4 sa = *(const f32x4*)(st2 + sl * 2), sb = *(const f32x4*)(st2 + sl * 2 + 4);
    const float mu[4] = {sa[0], sa[2], sb[0], sb[2]}, rr[4] = {sa[1], sa[3], sb[1], sb[3]};
#pragma unroll
    for (int n = 0; n < NTL; ++n) {
      const int f = 16 * (nb0 + n) + fr; const u32x2 p = z2p[n][m];
      u32x2 o; o[0] = pk2(bflo(p[0]) * rr[0] - mu[0], bfhi(p[0]) * rr[1] - mu[1]); o[1] = pk2(bflo(p[1]) * rr[2] - mu[2], bfhi(p[1]) * rr[3] - mu[3]);
      *(u32x2*)(lds + (sl >> 6) * 18432 + f * 128 + ((((sl & 63) >> 3) ^ ((f >> 1) & 7)) << 4) + (sl & 4) * 2) = o;
    }
  }
}
__global__ __launch_bounds__(768, 3) void k4_kernel(char* __restrict__ ws) {
  extern __shared__ __attribute__((aligned(16))) char lds[];
  const int tid = threadIdx.x, lane = tid & 63; const int w = __builtin_amdgcn_readfirstlane(tid >> 6);
  const int b = blockIdx.x, xc = b & 7, idx = b >> 3;
  const int pi = (xc & 1) * 8 + (idx & 7), ft = (xc >> 1) * 4 + (idx >> 3);
  float* tab = (float*)(lds + K4_TAB);
  TS(4, 0); TS(4, 6);
  const char* Xb = ws + WS_H + (size_t)pi * 128 * D * 2; const char* W2b = ws + WS_WIN + (size_t)ft * 9 * 18432; const char* W1b = W2b + (size_t)16 * 9 * 18432;
  auto src = [&](int p) { return p < 16 ? Xb + tn_off(p * 8 + (lane >> 3), lane, D * 2) : (p < 34 ? W2b + (p - 16) * 1024 : W1b + (p - 34) * 1024) + lane * 16; };
  first_tile<52, 12>(lds, w, src);
  if (w >= 8) {
    const int lw = w - 8;
    __builtin_amdgcn_s_setprio(2);
    const char* base[13];
#pragma unroll
    for (int j = 0; j < 13; ++j) base[j] = src(lw + 4 * j);
    auto issue = [&](int kt, int stage) {
#pragma unroll
      for (int j = 0; j < 13; ++j) glds16(base[j] + (j < 4 ? (long)kt * 128 : (long)kt * 18432), lds + stage * K4_STG + (lw + 4 * j) * 1024);
    };
    issue(1, 1);
    int stg = 2;
#pragma unroll 1
    for (int kt = 0; kt < 7; ++kt) { WAITV(13); BAR(); BAR(); issue(kt + 2, stg); stg = stg == 2 ? 0 : stg + 1; }
    WAITV(13); BAR(); BAR();
    WAITV(0); BAR(); BAR();
    BAR();
    BAR();
    const int row = 32 * lw + (lane >> 1);
    const __amdgpu_buffer_rsrc_t rs_ = __builtin_amdgcn_make_buffer_rsrc((void*)(ws + WS_XS), 0, 2048 * 16 * 16, 0x00020000);
    const unsigned vo = (unsigned)(((pi * 16 + (lane & 1) * 8) * 128 + row) * 16);
    float sum = 0.f, ssq = 0.f;
    for (int spin = 0; spin < (1 << 20); ++spin) {
      bool ok = true; sum = 0.f; ssq = 0.f;
      u32x4 g[8];
#pragma unroll
      for (int t = 0; t < 8; ++t) g[t] = __builtin_amdgcn_raw_buffer_load_b128(rs_, vo + t * 2048, 0, 16);
#pragma unroll
      for (int t = 0; t < 8; ++t) { ok &= g[t][1] == XTAG && g[t][3] == XTAG; sum += __uint_as_float(g[t][0]); ssq += __uint_as_float(g[t][2]); }
      if (__all(ok)) break;
      __builtin_amdgcn_s_sleep(8);
    }
    sum += __shfl_xor(sum, 1); ssq += __shfl_xor(ssq, 1);
    const float mean = sum * (1.f / FH), var = fmaxf(ssq * (1.f / FH) - mean * mean, 0.f), rstd = rsqrtf(var + 1e-5f);
    if ((lane & 1) == 0) *(f32x2_t*)(lds + K4_ST2 + row * 8) = f32x2_t{mean * rstd, rstd};
    __syncthreads();
  } else {
    if (tid < 128) {
      const f32x4* p = (const f32x4*)(ws + WS_ST2) + (size_t)(pi * 128 + tid) * 6;
      float sum = 0.f, ssq = 0.f;
#pragma unroll
      for (int t = 0; t < 6; ++t) { const f32x4 v = p[t]; sum += v[0] + v[2]; ssq += v[1] + v[3]; }
      const float mean = sum * (1.f / D), var = fmaxf(ssq * (1.f / D) - mean * mean, 0.f), rstd = rsqrtf(var + 1e-5f);
      tab[tid] = rstd; tab[128 + tid] = mean * rstd;
    } else if (tid < 416) {
      const int c = tid - 128;
      tab[256 + c] = ((const float*)(ws + WS_CS))[c < 144 ? FH + ft * 144 + c : ft * 144 + c - 144];
    }
    if (w < 4) k4_compute<5>(lds, lane, w, pi, ft, ws); else k4_compute<4>(lds, lane, w, pi, ft, ws);
  }
  __syncthreads();
  {
    const size_t dst = WS_Z2T + ((size_t)ft * 32 + pi * 2) * 18432;
    for (int i = w * 64 + lane_id(); i < 2304; i += 768) st16_wt(ws, dst + i * 16, *(const u32x4*)(lds + i * 16));
  }
  TS(4, 3); TS(4, 7);
}

template <int NTL>
DI void k5_compute(char* lds, int lane, int w, int tt, int ft, const u16* __restrict__ bsp, char* ws) {
  const int wr = w & 3, nb0 = NTL == 5 ? 0 : 5, fr = lane & 15, fq = lane >> 4;
  const C8Addr a = c8_addr<false>(lds, lane, wr, nb0);
  f32x4 acc[NTL][2] = {};
  C8Frag<NTL> f0, f1;
  WAITV(0); BAR();
  TS(5, 1);
  c8_read<NTL, false>(f0, a, 0, 0);
  unsigned z1v[4 * NTL];
  { const char* zb = ws + WS_Z1 + ((size_t)(tt * 16 + ft) * 4 + wr) * (9 * 1024) + lane * 16;
#pragma unroll
    for (int n = 0; n < NTL; ++n) { const u32x4 v = *(const u32x4*)(zb + (nb0 + n) * 1024); z1v[4 * n] = v[0]; z1v[4 * n + 1] = v[1]; z1v[4 * n + 2] = v[2]; z1v[4 * n + 3] = v[3]; } }
  float bb[2];
#pragma unroll
  for (int m = 0; m < 2; ++m) bb[m] = bf2f(bsp[tt * 128 + 32 * wr + 16 * m + fr]);
  int st = 0;
#define C5_ITER(SIDE0, SIDE1) do { const int stn = st + C8_STG == C8_LDS ? 0 : st + C8_STG; \
    c8_read<NTL, false>(f1, a, st, 1); c8_mma<NTL, true>(acc, f0); c8_sched<NTL, false>(); SB(); SIDE0; SB(); BAR(); SB(); \
    c8_read<NTL, false>(f0, a, stn, 0); c8_mma<NTL, true>(acc, f1); c8_sched<NTL, false>(); SB(); SIDE1; SB(); st = stn; } while (0)
#pragma unroll 1
  for (int kt = 0; kt < 32; ++kt) C5_ITER((void)0, (void)0);
  TS(5, 2);
  __syncthreads();
#pragma unroll
  for (int m = 0; m < 2; ++m) {
    const int t = 32 * wr + 16 * m + fr;
#pragma unroll
    for (int n = 0; n < NTL; ++n) {
      const int f = 16 * (nb0 + n) + 4 * fq; const unsigned z0 = z1v[4 * n + 2 * m], z1 = z1v[4 * n + 2 * m + 1];
      u32x2 o; o[0] = pk2(bflo(z0) * (acc[n][m][0] + bb[m]), bfhi(z0) * (acc[n][m][1] + bb[m]));
      o[1] = pk2(bflo(z1) * (acc[n][m][2] + bb[m]), bfhi(z1) * (acc[n][m][3] + bb[m]));
      *(u32x2*)(lds + t * EP_PITCH + f * 2) = o;
    }
  }
  __syncthreads();
  {
    const size_t ub = WS_U + ((size_t)tt * 128 * FH + ft * 144) * 2;
    for (int i = w * 64 + lane; i < 128 * 18; i += 512) { const int t = i / 18, c = i - t * 18; st16_wt(ws, ub + (size_t)t * FH * 2 + c * 16, *(const u32x4*)(lds + t * EP_PITCH + c * 16)); }
  }
}
__global__ __launch_bounds__(768, 3) void k5_kernel(const u16* __restrict__ wsp, const u16* __restrict__ bsp, char* __restrict__ ws) {
  extern __shared__ __attribute__((aligned(16))) char lds[];
  TS(5, 0); TS(5, 6);
  const int tid = threadIdx.x, lane = tid & 63; const int w = __builtin_amdgcn_readfirstlane(tid >> 6);
  const int b = blockIdx.x, xc = b & 7, idx = b >> 3;
  const int tt = (xc & 1) * 8 + (idx & 7), ft = (xc >> 1) * 4 + (idx >> 3);
  const char* Xb = (const char*)(wsp + (size_t)tt * 128 * S); const char* Wb = ws + WS_Z2T + (size_t)ft * 32 * 18432;
  auto src = [&](int p) { return p < 16 ? Xb + tn_off(p * 8 + (lane >> 3), lane, S * 2) : Wb + (p - 16) * 1024 + lane * 16; };
  first_tile<34, 12>(lds, w, src);
  if (w >= 8) {
    __builtin_amdgcn_s_setprio(2);
    loader_loop<32, C8_NS, C8_STG, 34, 16, 4>(lds, w - 8, src, [&](int kt, long& ox, long& ow) { ox = (long)kt * 128; ow = (long)kt * 18432; }, NoPost());
    return;
  }
  if (w < 4) k5_compute<5>(lds, lane, w, tt, ft, bsp, ws); else k5_compute<4>(lds, lane, w, tt, ft, bsp, ws);
  TS(5, 3); TS(5, 7);
}

extern "C" void kernel_launch(void* const* d_in, const int* in_sizes, int n_in, void* d_out, int out_size, void* d_ws, size_t ws_size, hipStream_t stream) {
  const u16* x = (const u16*)d_in[0]; const u16* wqkv = (const u16*)d_in[1]; const u16* wo = (const u16*)d_in[2];
  const u16* w_in = (const u16*)d_in[4]; const u16* w_sp = (const u16*)d_in[5]; const u16* b_sp = (const u16*)d_in[6]; const u16* w_out = (const u16*)d_in[7];
  u16* out = (u16*)d_out; char* ws = (char*)d_ws;
  static bool attr = false;
  if (!attr) {
    (void)hipFuncSetAttribute((const void*)k1_kernel, hipFuncAttributeMaxDynamicSharedMemorySize, K1_LDS);
    (void)hipFuncSetAttribute((const void*)k2_kernel, hipFuncAttributeMaxDynamicSharedMemorySize, ATT_LDS);
    (void)hipFuncSetAttribute((const void*)k3_kernel, hipFuncAttributeMaxDynamicSharedMemorySize, G_LDS);
    (void)hipFuncSetAttribute((const void*)k4_kernel, hipFuncAttributeMaxDynamicSharedMemorySize, K4_LDS);
    (void)hipFuncSetAttribute((const void*)k5_kernel, hipFuncAttributeMaxDynamicSharedMemorySize, C8_LDS);
    (void)hipFuncSetAttribute((const void*)k6_kernel, hipFuncAttributeMaxDynamicSharedMemorySize, G_LDS);
    attr = true;
  }
  k1_kernel<<<256, K1_THREADS, K1_LDS, stream>>>(x, wqkv, ws, (const char*)wo, (const char*)w_in, (const char*)w_sp, (const char*)w_out, (const char*)out);
  k2_kernel<<<256, 768, ATT_LDS, stream>>>(wo, w_in, w_out, w_sp, ws);
  k3_kernel<<<256, 768, G_LDS, stream>>>(x, wo, w_in, ws);
  k4_kernel<<<256, 768, K4_LDS, stream>>>(ws);
  k5_kernel<<<256, 768, C8_LDS, stream>>>(w_sp, b_sp, ws);
  k6_kernel<<<192, 768, G_LDS, stream>>>(w_out, out, ws);
}
```

```cpp
#include <hip/hip_runtime.h>
#include <hip/hip_bf16.h>
#include <stdint.h>
typedef unsigned short u16;
using bf16x8 = __attribute__((ext_vector_type(8))) short;
using s16x4 = __attribute__((ext_vector_type(4))) short;
using f32x4 = __attribute__((ext_vector_type(4))) float;
using f32x16 = __attribute__((ext_vector_type(16))) float;
using u32x4 = __attribute__((ext_vector_type(4))) unsigned;
using u32x2 = __attribute__((ext_vector_type(2))) unsigned;
using u32x32 = __attribute__((ext_vector_type(32))) unsigned;
typedef __bf16 bf16x2_t __attribute__((ext_vector_type(2)));
typedef float f32x2_t __attribute__((ext_vector_type(2)));
constexpr int S = 2048, D = 576, H = 32, DH = 18, F = 4608, FH = 2304;
#define DI __device__ __forceinline__
#define LAS __attribute__((address_space(3)))
#define GAS __attribute__((address_space(1)))
DI int lane_id() { int l = __builtin_amdgcn_mbcnt_hi(~0u, __builtin_amdgcn_mbcnt_lo(~0u, 0u)); asm volatile("" : "+v"(l)); return l; }
DI float bf2f(u16 x) { return __uint_as_float((unsigned)x << 16); }
DI float bflo(unsigned x) { return __uint_as_float(x << 16); }
DI float bfhi(unsigned x) { return __uint_as_float(x & 0xffff0000u); }
DI unsigned pk2(float lo, float hi) { f32x2_t v = {lo, hi}; return __builtin_bit_cast(unsigned, __builtin_convertvector(v, bf16x2_t)); }
#define MFMA32(a, b, c) __builtin_amdgcn_mfma_f32_32x32x16_bf16((a), (b), (c), 0, 0, 0)
#define MFMA16(a, b, c) __builtin_amdgcn_mfma_f32_16x16x32_bf16((a), (b), (c), 0, 0, 0)
#define SB() __builtin_amdgcn_sched_barrier(0)
#define SGB(mask, n) __builtin_amdgcn_sched_group_barrier(mask, n, 0)
#define PIN(x) asm volatile("" : "+v"(x))
#define BAR() __builtin_amdgcn_s_barrier()
#define WAITV(n) asm volatile("s_waitcnt vmcnt(%0)" :: "i"(n) : "memory")
#define WAITL0() asm volatile("s_waitcnt lgkmcnt(0)" ::: "memory")
DI void glds16(const void* g, void* l) { __builtin_amdgcn_global_load_lds((const GAS unsigned*)g, (LAS unsigned*)l, 16, 0, 0); }
template <int AUX> DI void glds16a(const void* g, void* l) { __builtin_amdgcn_global_load_lds((const GAS unsigned*)g, (LAS unsigned*)l, 16, 0, AUX); }
DI void st16_wt(char* ws, size_t off, u32x4 v) {
  const __amdgpu_buffer_rsrc_t r = __builtin_amdgcn_make_buffer_rsrc((void*)ws, 0, 128 << 20, 0x00020000);
  __builtin_amdgcn_raw_buffer_store_b128(v, r, (unsigned)off, 0, 16);
}
typedef LAS const char* lds_cptr;
typedef LAS char* lds_ptr;
#define LDS_RD128(p) (*(const LAS bf16x8*)(p))
#define LDS_RD32(p) (*(const LAS unsigned*)(p))
typedef short v4i16_t __attribute__((ext_vector_type(4)));
DI s16x4 lds_tr(lds_cptr p) { return __builtin_bit_cast(s16x4, __builtin_amdgcn_ds_read_tr16_b64_v4i16((LAS v4i16_t*)p)); }
DI bf16x8 lds_tr2(lds_cptr p0, lds_cptr p1) { const s16x4 a = lds_tr(p0), b = lds_tr(p1); return __builtin_shufflevector(a, b, 0, 1, 2, 3, 4, 5, 6, 7); }

#define TIMING 0
#define TS(k, i)
#define TR(k, i)
#define TSTAMP(v)
#define RSTAMP(v)

constexpr int KV_TILE = 2400, KV_K1 = 1024, KV_V = 1152, KV_VS = 624, KV_Z = KV_V + 608, KV_HEAD = 64 * KV_TILE;
constexpr int Q_TILE = 1152, Q_HEAD = 64 * Q_TILE;
constexpr size_t WS_KV = 0;
constexpr size_t WS_Q = 5u << 20;
constexpr size_t WS_O = 8u << 20;
constexpr size_t WS_H = 11u << 20;
constexpr size_t WS_ST2 = 14u << 20;
constexpr size_t WS_CS = 15u << 20;
constexpr size_t WS_XS = 16u << 20;
constexpr size_t WS_FLAG = WS_XS + (512u << 10);
constexpr size_t WS_Z2T = 17u << 20;
constexpr size_t WS_Z1 = 27u << 20;
constexpr size_t WS_U = 37u << 20;
constexpr size_t WS_WIN = 47u << 20;
constexpr size_t WS_WO = 53u << 20;
constexpr size_t WS_WOUT = 54u << 20;
constexpr size_t WS_DBG = 64u << 20;

constexpr int F_O = 0, F_W = 8, F_H = 9, F_CS = 25, F_Z = 26, F_U = 42;
typedef GAS unsigned gu32;
DI gu32* flagp(char* ws, int i) { return (gu32*)(ws + WS_FLAG + (size_t)i * 256); }
DI void flag_add(char* ws, int i) { __hip_atomic_fetch_add(flagp(ws, i), 1u, __ATOMIC_RELAXED, __HIP_MEMORY_SCOPE_AGENT); }
DI void flag_wait(char* ws, int i, unsigned target) {
  gu32* p = flagp(ws, i);
  for (int spin = 0; spin < (1 << 22); ++spin) {
    if ((unsigned)__builtin_amdgcn_readfirstlane(__hip_atomic_load(p, __ATOMIC_RELAXED, __HIP_MEMORY_SCOPE_AGENT)) >= target) break;
    __builtin_amdgcn_s_sleep(2);
  }
}
DI void st4_wt(void* p, unsigned v) { __hip_atomic_store((gu32*)p, v, __ATOMIC_RELAXED, __HIP_MEMORY_SCOPE_AGENT); }
DI void st8_wt(void* p, u32x2 v) { __hip_atomic_store((GAS unsigned long long*)p, ((unsigned long long)v[1] << 32) | v[0], __ATOMIC_RELAXED, __HIP_MEMORY_SCOPE_AGENT); }
DI u32x2 ld8_c(const void* p) { const unsigned long long v = __hip_atomic_load((GAS unsigned long long*)p, __ATOMIC_RELAXED, __HIP_MEMORY_SCOPE_AGENT); return u32x2{(unsigned)v, (unsigned)(v >> 32)}; }
DI float ld4f_c(const void* p) { return __uint_as_float(__hip_atomic_load((gu32*)p, __ATOMIC_RELAXED, __HIP_MEMORY_SCOPE_AGENT)); }
template <int NP, int NW, int NPX = 0, int AX = 0, int AW = 0, class SRC>
DI void first_tile(char* lds, int w, SRC src) {
#pragma unroll
  for (int j = 0; j < (NP + NW - 1) / NW; ++j) { const int p = w + NW * j; if (p < NP) { if (p < NPX) glds16a<AX>(src(p), lds + p * 1024); else glds16a<AW>(src(p), lds + p * 1024); } }
}
template <int NT, int NS, int STG, int NP, int NPX, int NLD, int AX = 0, int AW = 0, class SRC, class OFF, class POST>
DI void loader_loop(char* lds, int lw, SRC src, OFF off, POST post) {
  constexpr int PPW = (NP + NLD - 1) / NLD, PMIN = NP / NLD;
  static_assert((NS - 1) * PPW + 4 <= 63, "vmcnt");
  static_assert(NPX % NLD == 0, "X pieces");
  const char* base[PPW];
#pragma unroll
  for (int j = 0; j < PPW; ++j) { const int p = lw + NLD * j; base[j] = src(p < NP ? p : NP - 1); }
  auto issue = [&](int kt, int stage) {
    long ox, ow; off(kt, ox, ow);
#pragma unroll
    for (int j = 0; j < PPW; ++j) if (j < PMIN || lw + NLD * j < NP) {
      if (j < NPX / NLD) glds16a<AX>(base[j] + ox, lds + stage * STG + (lw + NLD * j) * 1024); else glds16a<AW>(base[j] + ow, lds + stage * STG + (lw + NLD * j) * 1024); }
  };
  issue(1, 1);
  WAITV(PMIN);
  BAR();
#pragma unroll
  for (int t = 2; t < NS - 1; ++t) issue(t, t);
  post(0);
  int stg = NS - 1, pst = 1;
#pragma unroll 1
  for (int kt = 0; kt < NT - (NS - 1); ++kt) {
    WAITV((NS - 3) * PMIN);
    BAR();
    issue(kt + NS - 1, stg); stg = stg + 1 == NS ? 0 : stg + 1;
    post(pst); pst = pst + 1 == NS ? 0 : pst + 1;
  }
#pragma unroll
  for (int r = NS - 2; r >= 0; --r) {
    WAITV((r - 1 > 0 ? r - 1 : 0) * PMIN); BAR();
    if (r > 0) { post(pst); pst = pst + 1 == NS ? 0 : pst + 1; }
  }
}
struct NoPost { DI void operator()(int) const {} };
DI unsigned tn_off(int row, int lane, int ld_bytes) { return (unsigned)row * ld_bytes + ((((lane & 7) ^ ((row >> 1) & 7))) << 4); }
template <int BN> DI unsigned kn_off(int p, int lane, int ld_bytes) {
  constexpr int CPR = BN / 8;
  const int c = 64 * p + lane, kp = c / CPR, cc = c - kp * CPR, odd = (kp >> 3) & 1, k = kp ^ (odd << 2);
  int cs = cc;
  if (BN == 96) { int blk = (cc >> 1) - odd; if (blk < 0) blk += 6; cs = blk * 2 + (cc & 1); }
  return (unsigned)k * ld_bytes + (unsigned)cs * 16;
}

DI float gelu_t(float x) {
  const float c1 = -2.f * 0.7978845608028654f * 1.4426950408889634f, c2 = c1 * 0.044715f;
  const float e = __builtin_amdgcn_exp2f(x * __builtin_fmaf(x * x, c2, c1));
  return x * __builtin_amdgcn_rcpf(1.f + e);
}

constexpr int C8_STG = 272 * 128, C8_W = 16384, C8_NS = 4, C8_LDS = C8_NS * C8_STG;
struct C8Addr { lds_cptr x0, x1, w0, w1; };
template <bool BKN> DI C8Addr c8_addr(char* lds, int lane, int wr, int nb0) {
  lds_cptr L = (lds_cptr)lds; const int fr = lane & 15, fq = lane >> 4, sw = (fr >> 1) & 7; C8Addr a;
  a.x0 = L + (32 * wr + fr) * 128 + ((fq ^ sw) << 4); a.x1 = L + (32 * wr + fr) * 128 + (((4 + fq) ^ sw) << 4);
  if (BKN) { const int q = fr >> 2, p4 = lane & 3, b = C8_W + (8 * fq + q) * 288 + nb0 * 32 + p4 * 8;
             a.w0 = L + b + (fq & 1) * 1152; a.w1 = L + b + ((fq & 1) ^ 1) * 1152; }
  else { a.w0 = L + C8_W + (16 * nb0 + fr) * 128 + ((fq ^ sw) << 4); a.w1 = L + C8_W + (16 * nb0 + fr) * 128 + (((4 + fq) ^ sw) << 4); }
  return a;
}
template <int NTL> struct C8Frag { bf16x8 x[2], w[NTL]; };
template <int NTL, bool BKN> DI void c8_read(C8Frag<NTL>& f, const C8Addr& a, int st, int ks) {
  f.x[0] = LDS_RD128((ks ? a.x1 : a.x0) + st); f.x[1] = LDS_RD128((ks ? a.x1 : a.x0) + st + 2048);
#pragma unroll
  for (int n = 0; n < NTL; ++n) {
    if (BKN) f.w[n] = lds_tr2(a.w0 + st + ks * 9216 + n * 32, a.w1 + st + ks * 9216 + n * 32);
    else f.w[n] = LDS_RD128((ks ? a.w1 : a.w0) + st + n * 2048);
  }
}
template <int NTL, bool WX> DI void c8_mma(f32x4 (&acc)[NTL][2], const C8Frag<NTL>& f) {
#pragma unroll
  for (int n = 0; n < NTL; ++n) {
#pragma unroll
    for (int m = 0; m < 2; ++m) acc[n][m] = WX ? MFMA16(f.w[n], f.x[m], acc[n][m]) : MFMA16(f.x[m], f.w[n], acc[n][m]);
  }
}
template <int NTL, bool BKN> DI void c8_sched() {
  constexpr int NR = 2 + (BKN ? 2 * NTL : NTL), NM = 2 * NTL;
  if (NR <= NM) {
#pragma unroll
    for (int i = 0; i < NR; ++i) { SGB(0x8, 1); SGB(0x100, 1); }
    SGB(0x8, NM - NR);
  } else {
#pragma unroll
    for (int i = 0; i < NM; ++i) { SGB(0x8, 1); SGB(0x100, 1); if (i < NR - NM) SGB(0x100, 1); }
  }
}

constexpr int K1_THREADS = 768;
constexpr int EP_PITCH = 304;
template <int WC, bool WX>
DI void k1_compute(char* lds, int lane, int w, f32x4 (&acc)[WC == 0 ? 3 : 2][4], float* tabs, char* ws) {
  constexpr int NTL = WC == 0 ? 3 : 2, NB0 = WC == 0 ? 0 : 2 * WC + 1, NST = WC == 0 ? 0 : (WC == 1 ? 2 : 1), M0 = WC == 1 ? 0 : WC - 1, M1 = 3;
  const int wr = w & 1, fr = lane & 15, fq = lane >> 4, sw = (fr >> 1) & 7;
  lds_cptr L = (lds_cptr)lds;
  lds_cptr x0 = L + (64 * wr + fr) * 128 + ((fq ^ sw) << 4), x1 = L + (64 * wr + fr) * 128 + (((4 + fq) ^ sw) << 4);
  const int wb = C8_W + (8 * fq + (fr >> 2)) * 288 + NB0 * 32 + (lane & 3) * 8;
  lds_cptr w0 = L + wb + (fq & 1) * 1152, w1 = L + wb + ((fq & 1) ^ 1) * 1152;
  const u32x4 onesv = {0x3f803f80u, 0x3f803f80u, 0x3f803f80u, 0x3f803f80u}; const bf16x8 ones = __builtin_bit_cast(bf16x8, onesv);
  bf16x8 xa[4], wa[NTL], xb[4], wb_[NTL];
  f32x4 cs[NTL] = {}, rsum[2] = {}, rsq[2] = {};
#define K1RD(X, Wf, st, ks) do { _Pragma("unroll") for (int n = 0; n < NTL; ++n) Wf[n] = lds_tr2(w0 + (st) + (ks) * 9216 + n * 32, w1 + (st) + (ks) * 9216 + n * 32); \
    _Pragma("unroll") for (int m = 0; m < 4; ++m) X[m] = LDS_RD128(((ks) ? x1 : x0) + (st) + m * 2048); } while (0)
#define K1MM(X, Wf) do { _Pragma("unroll") for (int n = 0; n < NTL; ++n) { _Pragma("unroll") for (int m = 0; m < 4; ++m) acc[n][m] = WX ? MFMA16(Wf[n], X[m], acc[n][m]) : MFMA16(X[m], Wf[n], acc[n][m]); \
    cs[n] = MFMA16(ones, Wf[n], cs[n]); } \
    if (NST >= 1) { rsum[0] = MFMA16(X[M0], ones, rsum[0]); rsq[0] = MFMA16(X[M0], X[M0], rsq[0]); } \
    if (NST >= 2) { rsum[1] = MFMA16(X[M1], ones, rsum[1]); rsq[1] = MFMA16(X[M1], X[M1], rsq[1]); } } while (0)
#define K1SCHED() do { _Pragma("unroll") for (int i_ = 0; i_ < 2 * NTL + 4; ++i_) { SGB(0x8, 1); SGB(0x100, 1); } SGB(0x8, 5 * NTL + 2 * NST - (2 * NTL + 4)); } while (0)
  WAITV(0); BAR();
  TS(1, 1);
  K1RD(xa, wa, 0, 0);
  int st = 0;
#pragma unroll 1
  for (int kt = 0; kt < 9; ++kt) {
    const int stn = st + C8_STG == C8_LDS ? 0 : st + C8_STG;
    K1RD(xb, wb_, st, 1); K1MM(xa, wa); K1SCHED();
    SB(); BAR(); SB();
    K1RD(xa, wa, stn, 0); K1MM(xb, wb_); K1SCHED();
    SB();
    st = stn;
  }
  TS(1, 2);
  const int l2 = lane_id(), r2 = l2 & 15, q2 = l2 >> 4;
  if (l2 < 16 && wr == 0) {
#pragma unroll
    for (int n = 0; n < NTL; ++n) tabs[256 + 16 * (NB0 + n) + l2] = cs[n][0];
  }
#pragma unroll
  for (int t = 0; t < NST; ++t) {
    const int mrow = 64 * wr + 16 * (t == 0 ? M0 : M1);
    if (r2 == 0) *(f32x4*)(tabs + mrow + 4 * q2) = rsum[t];
    if (q2 == (r2 >> 2)) tabs[128 + mrow + r2] = (r2 & 2) ? ((r2 & 1) ? rsq[t][3] : rsq[t][2]) : ((r2 & 1) ? rsq[t][1] : rsq[t][0]);
  }
}
template <int NTL>
DI void k1_epilogue(char* lds, int lane, int w, int type, int rt, int hg, const float* tabs, f32x4 (&acc)[NTL][4], char* kvimg) {
  asm volatile("" : "+v"(lane));
  const int wr = w & 1, wc = w >> 1, nb0 = wc == 0 ? 0 : 2 * wc + 1, fr = lane & 15, fq = lane >> 4;
  const float* rs = tabs; const float* mr = tabs + 128; const float* cs = tabs + 256;
  if (type == 2) {
#pragma unroll
    for (int n = 0; n < NTL; ++n) {
      const int col = 16 * (nb0 + n) + fr, hh = col / 18, d = col - hh * 18; const float c = cs[col];
      char* hb = kvimg + (size_t)(hg * 8 + hh) * KV_HEAD + (size_t)(rt * 4 + 2 * wr) * KV_TILE + KV_V + (fq & 1) * 304 + (fq >> 1) * 8 + d * 16;
#pragma unroll
      for (int m = 0; m < 4; ++m) {
        const int row = 64 * wr + 16 * m + 4 * fq;
        const f32x4 r4 = *(const f32x4*)(rs + row), m4 = *(const f32x4*)(mr + row);
        u32x2 o; o[0] = pk2(acc[n][m][0] * r4[0] - m4[0] * c, acc[n][m][1] * r4[1] - m4[1] * c); o[1] = pk2(acc[n][m][2] * r4[2] - m4[2] * c, acc[n][m][3] * r4[3] - m4[3] * c);
        *(u32x2*)(hb + (m >> 1) * KV_TILE + (m & 1) * KV_VS) = o;
      }
    }
  } else {
    const float qs = type == 0 ? 0.23570226039551584f * 1.4426950408889634f : 1.f;
#pragma unroll
    for (int m = 0; m < 4; ++m) {
      const int row = 64 * wr + 16 * m + fr; const float r = rs[row] * qs, mm = mr[row] * qs;
#pragma unroll
      for (int n = 0; n < NTL; ++n) {
        const int col = 16 * (nb0 + n) + 4 * fq; const f32x4 c4 = *(const f32x4*)(cs + col);
        u32x2 o; o[0] = pk2(acc[n][m][0] * r - mm * c4[0], acc[n][m][1] * r - mm * c4[1]); o[1] = pk2(acc[n][m][2] * r - mm * c4[2], acc[n][m][3] * r - mm * c4[3]);
        *(u32x2*)(lds + row * EP_PITCH + col * 2) = o;
      }
    }
  }
}
__global__ __launch_bounds__(K1_THREADS, 3) void k1_kernel(const u16* __restrict__ x, const u16* __restrict__ wqkv, char* __restrict__ ws) {
  extern __shared__ __attribute__((aligned(16))) char lds[];
  const int tid = threadIdx.x, lane = tid & 63; const int w = __builtin_amdgcn_readfirstlane(tid >> 6);
  const int b = blockIdx.x;
  TS(1, 0); TS(1, 6);
  if (b >= 192) {
    if (tid < 512) ((u32x4*)(ws + WS_XS))[(b - 192) * 512 + tid] = u32x4{0, 0, 0, 0};
    else if (tid < 528) ((u32x4*)(ws + WS_FLAG))[(b - 192) * 16 + tid - 512] = u32x4{0, 0, 0, 0};
    return;
  }
  const int xc = b & 7, idx = b >> 3;
  const int rt = xc * 2 + (idx & 1), ct = idx >> 1;
  const int type = ct >> 2, hg = ct & 3;
  float* tabs = (float*)(lds + C8_LDS);
  char* kvimg = ws + WS_KV; char* qimg = ws + WS_Q;
  const char* Xb = (const char*)(x + (size_t)rt * 128 * D); const char* Wb = (const char*)(wqkv + ct * 144);
  auto src = [&](int p) { return p < 16 ? Xb + tn_off(p * 8 + (lane >> 3), lane, D * 2) : Wb + kn_off<144>(p - 16, lane, 1728 * 2); };
  first_tile<34, 12>(lds, w, src);
  if (w >= 8) {
    __builtin_amdgcn_s_setprio(2);
    loader_loop<9, C8_NS, C8_STG, 34, 16, 4>(lds, w - 8, src, [&](int kt, long& ox, long& ow) { ox = (long)kt * 128; ow = (long)kt * 64 * 1728 * 2; }, NoPost());
    __syncthreads();
    const int row = (w - 8) * 64 + lane_id();
    if (row < 128) { const float mean = tabs[row] * (1.f / D), var = fmaxf(tabs[128 + row] * (1.f / D) - mean * mean, 0.f), rstd = rsqrtf(var + 1e-5f); tabs[row] = rstd; tabs[128 + row] = mean * rstd; }
    __syncthreads();
  } else {
#define K1_PATH(WC, WX) do { f32x4 acc[WC == 0 ? 3 : 2][4] = {}; \
      k1_compute<WC, WX>(lds, lane, w, acc, tabs, ws); \
      __syncthreads(); __syncthreads(); \
      k1_epilogue<WC == 0 ? 3 : 2>(lds, lane_id(), w, WX ? (type & 1) : 2, rt, hg, tabs, acc, kvimg); } while (0)
#define K1_SEL(WX) do { if (w < 2) K1_PATH(0, WX); else if (w < 4) K1_PATH(1, WX); else if (w < 6) K1_PATH(2, WX); else K1_PATH(3, WX); } while (0)
    if (type == 2) K1_SEL(false); else K1_SEL(true);
  }
  const int tid2 = w * 64 + lane_id();
  if (type == 2) {
    if (tid2 < 192) {
      const int tl = tid2 / 48, r = tid2 - tl * 48, hh = r / 6, c = r - hh * 6;
      char* tb = kvimg + (size_t)(hg * 8 + hh) * KV_HEAD + (size_t)(rt * 4 + tl) * KV_TILE;
      if (c < 4) *(u32x4*)(tb + KV_V + (c >> 1) * KV_VS + (c & 1) * 304 + 18 * 16) = u32x4{0x3f803f80u, 0x3f803f80u, 0x3f803f80u, 0x3f803f80u};
      else *(u32x4*)(tb + KV_Z + (c - 4) * KV_VS) = u32x4{0, 0, 0, 0};
    }
    TS(1, 3); TS(1, 7);
    return;
  }
  __syncthreads();
  for (int it = tid2; it < 1024; it += K1_THREADS) {
    const int tok = it & 127, hh = it >> 7;
    const unsigned* sp = (const unsigned*)(lds + tok * EP_PITCH + hh * 36);
    u32x4 c0 = {sp[0], sp[1], sp[2], sp[3]}, c1 = {sp[4], sp[5], sp[6], sp[7]}; const unsigned c2 = sp[8];
    char* tb = type == 0 ? qimg + (size_t)(hg * 8 + hh) * Q_HEAD + (size_t)(rt * 4 + (tok >> 5)) * Q_TILE
                         : kvimg + (size_t)(hg * 8 + hh) * KV_HEAD + (size_t)(rt * 4 + (tok >> 5)) * KV_TILE;
    const int key = tok & 31;
    *(u32x4*)(tb + key * 16) = c0; *(u32x4*)(tb + 512 + key * 16) = c1; *(unsigned*)(tb + 1024 + key * 4) = c2;
  }
  TS(1, 3); TS(1, 7);
}
static_assert(128 * EP_PITCH <= C8_LDS, "");
constexpr int K1_LDS = C8_LDS + 400 * 4;

constexpr int ATT_LDS = KV_HEAD + 8 * Q_TILE;
#define SWITCH 50
DI void p2_attention(const u16* __restrict__ wo, const u16* __restrict__ w_in, const u16* __restrict__ w_out, const u16* __restrict__ w_sp, char* __restrict__ ws, char* lds) {
  TS(2, 0); TS(2, 6);
  const char* kvimg = ws + WS_KV; const char* qimg = ws + WS_Q; u16* o = (u16*)(ws + WS_O);
  const int tid = threadIdx.x, lane = tid & 63; const int w = __builtin_amdgcn_readfirstlane(tid >> 6);
  const int b = blockIdx.x, xcd = b & 7, idx = b >> 3, h = xcd * 4 + (idx >> 3), qb = idx & 7;
  const char* kvg = kvimg + (size_t)h * KV_HEAD + lane * 16;
  if (w < 8) {
    const char* qg = qimg + (size_t)h * Q_HEAD + (qb * 8 + w) * Q_TILE + lane * 16;
    char* qtile = lds + KV_HEAD + w * Q_TILE;
    glds16(qg, qtile);
    if (lane < 8) glds16(qg + 1024, qtile + 1024);
  }
  glds16(kvg + w * 1024, lds + w * 1024); glds16(kvg + (w + 12) * 1024, lds + (w + 12) * 1024);
  WAITV(0);
  BAR();
  if (w >= 8) {
    const int lw = w - 8;
    __builtin_amdgcn_s_setprio(3);
#pragma unroll 1
    for (int p = 24 + lw; p < 150; p += 4) glds16(kvg + p * 1024, lds + p * 1024);
    WAITV(0);
    BAR();
    {
      const u32x4* wp = (const u32x4*)w_sp + (size_t)(b * 4 + lw) * 512 + lane;
      u32x4 acc = wp[0];
#pragma unroll
      for (int i = 1; i < 8; ++i) acc ^= wp[i * 64];
      if (acc[0] == 0x12345678u && acc[1] == 0x9abcdef0u && acc[2] == 0x0fedcba9u && acc[3] == 0x87654321u) ((unsigned*)(ws + WS_DBG))[0] = 1;
    }
    const int gw = b * 4 + lw, c = lane & 7, nb = lane >> 3;
    if (nb < 6) {
#pragma unroll 1
      for (int blk = gw; blk < 864 + 144 + 432; blk += 1024) {
        const u16* sp; int ld, kt, n0; size_t db;
        if (blk < 864) { kt = blk / 96; const int nbk = blk - kt * 96, z2 = nbk >= 48, nn = z2 ? nbk - 48 : nbk, t = nn / 3; n0 = nbk * 48; sp = w_in; ld = F;
          db = WS_WIN + ((size_t)((z2 ? 0 : 16) + t) * 9 + kt) * 18432 + (nn - t * 3) * 48 * 128; }
        else if (blk < 1008) { const int q = blk - 864; kt = q / 12; const int nbk = q - kt * 12; n0 = nbk * 48; sp = wo; ld = D; db = WS_WO + ((size_t)(nbk >> 1) * 12 + kt) * 12288 + (nbk & 1) * 48 * 128; }
        else { const int q = blk - 1008; kt = q / 12; const int nbk = q - kt * 12; n0 = nbk * 48; sp = w_out; ld = D; db = WS_WOUT + ((size_t)(nbk >> 1) * 36 + kt) * 12288 + (nbk & 1) * 48 * 128; }
        u32x4 r[8];
        if (blk >= 864 && blk < 1008) {
          const int kc = kt * 8 + c, hh = kc / 3, part = kc - hh * 3;
          const u16* sq = sp + (size_t)(hh * DH) * ld + n0 + nb * 8;
#pragma unroll
          for (int j = 0; j < 8; ++j) {
            const int d = part == 2 ? 16 + j : (j & 3) + 8 * (j >> 2) + 4 * part;
            r[j] = (part == 2 && j >= 2) ? u32x4{0, 0, 0, 0} : *(const u32x4*)(sq + (size_t)(d < DH ? d : 0) * ld);
          }
        } else {
          sp += (size_t)(kt * 64 + c * 8) * ld + n0 + nb * 8;
#pragma unroll
          for (int j = 0; j < 8; ++j) r[j] = *(const u32x4*)(sp + (size_t)j * ld);
        }
        const unsigned lo = (unsigned)db + nb * 1024;
#pragma unroll
        for (int e = 0; e < 4; ++e) {
          const unsigned sl = (unsigned)((c ^ ((4 * nb + e) & 7)) << 4);
#pragma unroll
          for (int hi = 0; hi < 2; ++hi) {
            const unsigned sel = hi ? 0x07060302u : 0x05040100u;
            u32x4 o;
#pragma unroll
            for (int q = 0; q < 4; ++q) o[q] = __builtin_amdgcn_perm(r[2 * q + 1][e], r[2 * q][e], sel);
            st16_wt(ws, lo + (2 * e + hi) * 128 + sl, o);
          }
        }
      }
    }
    WAITV(0);
    if (lane == 0) flag_add(ws, F_W);
    __builtin_amdgcn_s_setprio(0);
  } else {
  TS(2, 1); TR(2, 4);
  if (w >= 4) __builtin_amdgcn_s_setprio(1);
  const int r32 = lane & 31, hh = lane >> 5;
  lds_cptr L = (lds_cptr)lds;
  const bf16x8 qf0 = LDS_RD128(L + KV_HEAD + w * Q_TILE + lane * 16);
  u32x4 q1v = {0, 0, 0, 0}; { unsigned t = LDS_RD32(L + KV_HEAD + w * Q_TILE + 1024 + r32 * 4); q1v[0] = hh ? 0u : t; }
  const bf16x8 qf1 = __builtin_bit_cast(bf16x8, q1v);
  lds_cptr kp0 = L + lane * 16, kp1 = L + (hh ? KV_Z : KV_K1 + r32 * 4), vp = L + (r32 <= 18 ? KV_V + hh * 304 + r32 * 16 : KV_Z);
  f32x16 negm;
  {
    bf16x8 k0 = LDS_RD128(kp0); u32x4 k1 = {LDS_RD32(kp1), 0, 0, 0};
    f32x16 s = {}; s = MFMA32(k0, qf0, s); s = MFMA32(__builtin_bit_cast(bf16x8, k1), qf1, s);
    float m = s[0];
#pragma unroll
    for (int i = 1; i < 16; ++i) m = fmaxf(m, s[i]);
    auto rr = __builtin_amdgcn_permlane32_swap(__float_as_uint(m), __float_as_uint(m), false, false);
    m = fmaxf(__uint_as_float(rr[0]), __uint_as_float(rr[1]));
#pragma unroll
    for (int i = 0; i < 16; ++i) negm[i] = -m;
  }
  f32x16 O = {};
#define EX2(Sx, i) pk2(__builtin_amdgcn_exp2f(Sx[i]), __builtin_amdgcn_exp2f(Sx[(i) + 1]))
  bf16x8 k0, v0, v1; u32x4 k1 = {0, 0, 0, 0};
  f32x16 SA, SBc; u32x4 pA0, pA1, pB0, pB1;
#define STEP(Sc, Sn, Pc0, Pc1, Pp0, Pp1, koff, voff) do { \
    SB(); Sn = MFMA32(k0, qf0, negm); SB(); \
    Pc0[0] = EX2(Sc, 0); Pc0[1] = EX2(Sc, 2); PIN(Pc0); SB(); \
    Sn = MFMA32(__builtin_bit_cast(bf16x8, k1), qf1, Sn); SB(); \
    k0 = LDS_RD128(kp0 + (koff)); k1[0] = LDS_RD32(kp1 + (koff)); SB(); \
    Pc0[2] = EX2(Sc, 4); Pc0[3] = EX2(Sc, 6); PIN(Pc0); SB(); \
    O = MFMA32(v0, __builtin_bit_cast(bf16x8, Pp0), O); SB(); \
    Pc1[0] = EX2(Sc, 8); Pc1[1] = EX2(Sc, 10); PIN(Pc1); SB(); \
    O = MFMA32(v1, __builtin_bit_cast(bf16x8, Pp1), O); SB(); \
    v0 = LDS_RD128(vp + (voff)); v1 = LDS_RD128(vp + (voff) + KV_VS); SB(); \
    Pc1[2] = EX2(Sc, 12); Pc1[3] = EX2(Sc, 14); PIN(Pc1); SB(); \
  } while (0)
  k0 = LDS_RD128(kp0); k1[0] = LDS_RD32(kp1);
  SA = MFMA32(k0, qf0, negm); SA = MFMA32(__builtin_bit_cast(bf16x8, k1), qf1, SA);
  k0 = LDS_RD128(kp0 + KV_TILE); k1[0] = LDS_RD32(kp1 + KV_TILE);
  v0 = LDS_RD128(vp); v1 = LDS_RD128(vp + KV_VS);
  pB0 = u32x4{0, 0, 0, 0}; pB1 = u32x4{0, 0, 0, 0};
  int off = 0;
  for (int t = 0; t < 8; t += 2) {
    STEP(SA, SBc, pA0, pA1, pB0, pB1, off + 2 * KV_TILE, off);
    STEP(SBc, SA, pB0, pB1, pA0, pA1, off + 3 * KV_TILE, off + KV_TILE);
    off += 2 * KV_TILE;
  }
  BAR();
  for (int t = 8; t < 62; t += 2) {
    if (t == SWITCH) __builtin_amdgcn_s_setprio(0);
    STEP(SA, SBc, pA0, pA1, pB0, pB1, off + 2 * KV_TILE, off);
    STEP(SBc, SA, pB0, pB1, pA0, pA1, off + 3 * KV_TILE, off + KV_TILE);
    off += 2 * KV_TILE;
  }
  STEP(SA, SBc, pA0, pA1, pB0, pB1, off + KV_TILE, off);
  STEP(SBc, SA, pB0, pB1, pA0, pA1, off + KV_TILE, off + KV_TILE);
  O = MFMA32(v0, __builtin_bit_cast(bf16x8, pB0), O); O = MFMA32(v1, __builtin_bit_cast(bf16x8, pB1), O);
  TS(2, 2); TR(2, 5);
  {
    float mx = 0.f;
#pragma unroll
    for (int i = 0; i < 11; ++i) mx = fmaxf(mx, fabsf(O[i]));
    bool bad = !(mx < 1.2676506e30f);
#pragma unroll
    for (int i = 0; i < 11; ++i) bad |= (O[i] != O[i]);
    if (__any(bad)) {
      float m = -3.0e38f; O = f32x16{};
#pragma unroll 1
      for (int t = 0; t < 64; ++t) {
        const int of = t * KV_TILE;
        const bf16x8 a0 = LDS_RD128(kp0 + of); const u32x4 a1 = {LDS_RD32(kp1 + of), 0, 0, 0};
        f32x16 s = {}; s = MFMA32(a0, qf0, s); s = MFMA32(__builtin_bit_cast(bf16x8, a1), qf1, s);
        float tm = s[0];
#pragma unroll
        for (int i = 1; i < 16; ++i) tm = fmaxf(tm, s[i]);
        auto rr = __builtin_amdgcn_permlane32_swap(__float_as_uint(tm), __float_as_uint(tm), false, false);
        tm = fmaxf(__uint_as_float(rr[0]), __uint_as_float(rr[1]));
        const float mn = fmaxf(m, tm), al = __builtin_amdgcn_exp2f(m - mn); m = mn;
#pragma unroll
        for (int i = 0; i < 16; ++i) { O[i] *= al; s[i] -= mn; }
        u32x4 p0, p1;
#pragma unroll
        for (int i = 0; i < 4; ++i) { p0[i] = EX2(s, 2 * i); p1[i] = EX2(s, 8 + 2 * i); }
        O = MFMA32(LDS_RD128(vp + of), __builtin_bit_cast(bf16x8, p0), O); O = MFMA32(LDS_RD128(vp + of + KV_VS), __builtin_bit_cast(bf16x8, p1), O);
      }
    }
  }
  {
    float l = O[10];
    { auto rr = __builtin_amdgcn_permlane32_swap(__float_as_uint(l), __float_as_uint(l), false, false); l = __uint_as_float(rr[0]); }
    const float inv = 1.f / l;
    const size_t ob = WS_O + (size_t)(qb * 256 + w * 32 + r32) * 1536 + h * 48;
    const u32x4 c0 = {pk2(O[0] * inv, O[1] * inv), pk2(O[2] * inv, O[3] * inv), pk2(O[4] * inv, O[5] * inv), pk2(O[6] * inv, O[7] * inv)};
    st16_wt(ws, ob + hh * 16, c0);
    if (hh == 0) st16_wt(ws, ob + 32, u32x4{pk2(O[8] * inv, O[9] * inv), 0, 0, 0});
  }
  TS(2, 3);
  }
  WAITV(0); __syncthreads();
  if (tid == 0) flag_add(ws, F_O + qb);
  TS(2, 7);
}

constexpr int G_STG = 160 * 128, G_W = 8192, G_NS = 6, G_LDS = G_NS * G_STG;
template <int K, bool STATS, int KID>
DI void g_body(char* ws, char* lds, const u16* __restrict__ A, const char* __restrict__ W, const u16* __restrict__ res, u16* __restrict__ out, float* __restrict__ st2, int b) {
  constexpr int NT = K / 64;
  const int lane = lane_id(); const int w = __builtin_amdgcn_readfirstlane(threadIdx.x >> 6);
  const int xc = b & 7, idx = b >> 3, rt = xc * 4 + (idx & 3), ct = idx >> 2;
  const int m0 = rt * 64, n0 = ct * 96;
  TS(KID, 0); TS(KID, 6);
  const char* Xb = (const char*)(A + (size_t)m0 * K); const char* Wb = W + (size_t)ct * NT * 12288;
  auto src = [&](int p) { return p < 8 ? Xb + tn_off(p * 8 + (lane >> 3), lane, K * 2) : Wb + (p - 8) * 1024 + lane * 16; };
  if (STATS) { if (w == 11) flag_wait(ws, F_W, 1024); __syncthreads(); }
  glds16a<16>(src(8 + w), lds + (8 + w) * 1024);
  if (w == 11) { if (STATS) flag_wait(ws, F_O + (rt >> 2), 32); else flag_wait(ws, F_U + (rt >> 1), 16); }
  __syncthreads();
  if (w < 8) glds16a<16>(src(w), lds + w * 1024);
  if (w >= 4) {
    __builtin_amdgcn_s_setprio(2);
    loader_loop<NT, G_NS, G_STG, 20, 8, 8, 16, 16>(lds, w - 4, src, [&](int kt, long& ox, long& ow) { ox = (long)kt * 128; ow = (long)kt * 12288; }, NoPost());
    __builtin_amdgcn_s_setprio(0);
  } else {
  const int wr = w & 1, wc = w >> 1, fr = lane & 15, fq = lane >> 4, sw = (fr >> 1) & 7;
  lds_cptr L = (lds_cptr)lds;
  lds_cptr x0 = L + (32 * wr + fr) * 128 + ((fq ^ sw) << 4), x1 = L + (32 * wr + fr) * 128 + (((4 + fq) ^ sw) << 4);
  lds_cptr w0 = L + G_W + (48 * wc + fr) * 128 + ((fq ^ sw) << 4), w1 = L + G_W + (48 * wc + fr) * 128 + (((4 + fq) ^ sw) << 4);
  f32x4 acc[3][2] = {};
  bf16x8 xa[2], wa[3], xb[2], wb_[3];
#define GRD(X, Wf, st, ks) do { X[0] = LDS_RD128(((ks) ? x1 : x0) + (st)); X[1] = LDS_RD128(((ks) ? x1 : x0) + (st) + 2048); \
    _Pragma("unroll") for (int n = 0; n < 3; ++n) Wf[n] = LDS_RD128(((ks) ? w1 : w0) + (st) + n * 2048); } while (0)
#define GSCHED() do { _Pragma("unroll") for (int i_ = 0; i_ < 5; ++i_) { SGB(0x8, 1); SGB(0x100, 1); } SGB(0x8, 1); } while (0)
#define GMM(X, Wf) do { _Pragma("unroll") for (int n = 0; n < 3; ++n) { acc[n][0] = MFMA16(Wf[n], X[0], acc[n][0]); acc[n][1] = MFMA16(Wf[n], X[1], acc[n][1]); } } while (0)
  WAITV(0); BAR();
  TS(KID, 1);
  GRD(xa, wa, 0, 0);
  u32x2 rv[2][3];
#pragma unroll
  for (int m = 0; m < 2; ++m)
#pragma unroll
    for (int n = 0; n < 3; ++n) rv[m][n] = ld8_c(res + (size_t)(m0 + 32 * wr + 16 * m + fr) * D + n0 + 48 * wc + 16 * n + 4 * fq);
  int st = 0;
#pragma unroll 1
  for (int kt = 0; kt < NT; ++kt) {
    const int stn = st + G_STG == G_LDS ? 0 : st + G_STG;
    GRD(xb, wb_, st, 1);
    GMM(xa, wa);
    GSCHED();
    SB(); BAR(); SB();
    GRD(xa, wa, stn, 0);
    GMM(xb, wb_);
    GSCHED();
    SB();
    st = stn;
  }
  TS(KID, 2);
#pragma unroll
  for (int m = 0; m < 2; ++m) {
    const int row = m0 + 32 * wr + 16 * m + fr;
    float sum = 0.f, ssq = 0.f;
#pragma unroll
    for (int n = 0; n < 3; ++n) {
      const int col = n0 + 48 * wc + 16 * n + 4 * fq;
      const u32x2 r = rv[m][n];
      u32x2 o; o[0] = pk2(bflo(r[0]) + acc[n][m][0], bfhi(r[0]) + acc[n][m][1]); o[1] = pk2(bflo(r[1]) + acc[n][m][2], bfhi(r[1]) + acc[n][m][3]);
      if (STATS) st8_wt(out + (size_t)row * D + col, o); else *(u32x2*)(out + (size_t)row * D + col) = o;
      if (STATS) { const float a = bflo(o[0]), bq = bfhi(o[0]), c = bflo(o[1]), d = bfhi(o[1]); sum += (a + bq) + (c + d); ssq += (a * a + bq * bq) + (c * c + d * d); }
    }
    if (STATS) {
      sum += __shfl_xor(sum, 16); sum += __shfl_xor(sum, 32); ssq += __shfl_xor(ssq, 16); ssq += __shfl_xor(ssq, 32);
      if (fq == 0) st8_wt(st2 + ((size_t)row * 12 + ct * 2 + wc) * 2, u32x2{__float_as_uint(sum), __float_as_uint(ssq)});
    }
  }
  TS(KID, 3);
  }
  if (STATS) { WAITV(0); __syncthreads(); if (threadIdx.x == 0) flag_add(ws, F_H + (rt >> 1)); }
  TS(KID, 7);
}
DI void p3_colsum(const u16* __restrict__ w_in, char* __restrict__ ws, char* lds, int b) {
    const int tid = threadIdx.x, c = tid % 9, rg = tid / 9;
    float* red = (float*)lds;
    if (rg < 48) {
      const char* p = (const char*)(w_in + (b - 192) * 72 + c * 8) + (size_t)rg * (F * 2);
      u32x4 v[12];
#pragma unroll
      for (int i = 0; i < 12; ++i) v[i] = *(const u32x4*)(p + (size_t)i * 48 * F * 2);
      float a[8] = {};
#pragma unroll
      for (int i = 0; i < 12; ++i)
#pragma unroll
        for (int e = 0; e < 4; ++e) { a[2 * e] += bflo(v[i][e]); a[2 * e + 1] += bfhi(v[i][e]); }
#pragma unroll
      for (int e = 0; e < 8; ++e) red[rg * 72 + c * 8 + e] = a[e];
    }
    __syncthreads();
    if (tid < 72) { float t = 0.f; for (int g = 0; g < 48; ++g) t += red[g * 72 + tid]; st4_wt((float*)(ws + WS_CS) + (b - 192) * 72 + tid, __float_as_uint(t)); }
    WAITV(0); __syncthreads();
    if (tid == 0) flag_add(ws, F_CS);
}
constexpr unsigned XTAG = 0x9E3779B9u;
DI float dpp_sum16(float v) {
  v += __builtin_bit_cast(float, __builtin_amdgcn_mov_dpp(__builtin_bit_cast(int, v), 0xB1, 0xf, 0xf, true));
  v += __builtin_bit_cast(float, __builtin_amdgcn_mov_dpp(__builtin_bit_cast(int, v), 0x4E, 0xf, 0xf, true));
  v += __builtin_bit_cast(float, __builtin_amdgcn_mov_dpp(__builtin_bit_cast(int, v), 0x141, 0xf, 0xf, true));
  v += __builtin_bit_cast(float, __builtin_amdgcn_mov_dpp(__builtin_bit_cast(int, v), 0x140, 0xf, 0xf, true));
  return v;
}
constexpr int K4_STG = (128 + 288) * 128, K4_W2 = 16384, K4_W1 = 16384 + 18432, K4_NS = 3;
constexpr int K4_TAB = K4_NS * K4_STG;
constexpr int K4_PART = 40960, K4_ST2 = 45056;
constexpr int K4_LDS = K4_TAB + 544 * 4;
template <int NTL>
DI void k4_compute(char* lds, int lane, int w, int pi, int ft, char* ws, unsigned (&z1v)[4 * NTL]) {
  const int wr = w & 3, nb0 = NTL == 5 ? 0 : 5, wc = NTL == 5 ? 0 : 1, fr = lane & 15, fq = lane >> 4, sw = (fr >> 1) & 7;
  lds_cptr L = (lds_cptr)lds;
  lds_cptr x0 = L + (32 * wr + fr) * 128 + ((fq ^ sw) << 4), x1 = L + (32 * wr + fr) * 128 + (((4 + fq) ^ sw) << 4);
  lds_cptr w0 = L + K4_W2 + (16 * nb0 + fr) * 128 + ((fq ^ sw) << 4), w1 = L + K4_W2 + (16 * nb0 + fr) * 128 + (((4 + fq) ^ sw) << 4);
  f32x4 acc2[NTL][2] = {}, acc1[NTL][2] = {};
  bf16x8 xa[2], xb[2], wa[NTL], wb[NTL];
#define RDX(X, P, st) do { X[0] = LDS_RD128(P + (st)); X[1] = LDS_RD128(P + (st) + 2048); } while (0)
#define RDW(Wf, P, st) do { _Pragma("unroll") for (int n = 0; n < NTL; ++n) Wf[n] = LDS_RD128(P + (st) + n * 2048); } while (0)
#define MM2(X, Wf) do { _Pragma("unroll") for (int n = 0; n < NTL; ++n) { acc2[n][0] = MFMA16(X[0], Wf[n], acc2[n][0]); acc2[n][1] = MFMA16(X[1], Wf[n], acc2[n][1]); } } while (0)
#define MM1(X, Wf) do { _Pragma("unroll") for (int n = 0; n < NTL; ++n) { acc1[n][0] = MFMA16(Wf[n], X[0], acc1[n][0]); acc1[n][1] = MFMA16(Wf[n], X[1], acc1[n][1]); } } while (0)
#define SCH(NR) do { _Pragma("unroll") for (int i_ = 0; i_ < (NR); ++i_) { SGB(0x8, 1); SGB(0x100, 1); } SGB(0x8, 2 * NTL - (NR)); SB(); } while (0)
  WAITV(0); WAITL0();
  if (NTL == 4) BAR();
  int st = 0;
#pragma unroll 1
  for (int kt = 0; kt < 9; ++kt) {
    BAR(); SB();
    if (kt == 0) TS(4, 1);
    RDX(xa, x0, st); RDW(wa, w0, st); SB();
    RDW(wb, w0, st + 18432); MM2(xa, wa); SCH(NTL);
    RDX(xb, x1, st); RDW(wa, w1, st); MM1(xa, wb); SCH(NTL + 2);
    BAR(); SB();
    RDW(wb, w1, st + 18432); MM2(xb, wa); SCH(NTL);
    MM1(xb, wb); SB();
    st = st + K4_STG == K4_TAB ? 0 : st + K4_STG;
  }
  if (NTL == 5) BAR();
  TS(4, 4);
  int lz = lane_id();
  const float* tab = (const float*)(lds + K4_TAB); const float* rs = tab; const float* mr = tab + 128; const float* cs = tab + 256;
  float* part = (float*)(lds + K4_PART); const float* st2 = (const float*)(lds + K4_ST2);
  u32x2 z2p[NTL][2];
  {
    const int fr = lz & 15, fq = lz >> 4;
#pragma unroll
    for (int m = 0; m < 2; ++m) {
      const f32x4 r4 = *(const f32x4*)(rs + 32 * wr + 16 * m + 4 * fq), m4 = *(const f32x4*)(mr + 32 * wr + 16 * m + 4 * fq);
      f32x4 su = {0, 0, 0, 0}, sq = {0, 0, 0, 0};
#pragma unroll
      for (int n = 0; n < NTL; ++n) {
        const float c = cs[16 * (nb0 + n) + fr];
        f32x4 g;
#pragma unroll
        for (int r = 0; r < 4; ++r) { g[r] = gelu_t(acc2[n][m][r] * r4[r] - m4[r] * c); su[r] += g[r]; sq[r] = __builtin_fmaf(g[r], g[r], sq[r]); }
        z2p[n][m][0] = pk2(g[0], g[1]); z2p[n][m][1] = pk2(g[2], g[3]); PIN(z2p[n][m]);
      }
#pragma unroll
      for (int r = 0; r < 4; ++r) { su[r] = dpp_sum16(su[r]); sq[r] = dpp_sum16(sq[r]); }
      if (fr == 0) {
#pragma unroll
        for (int r = 0; r < 4; ++r) *(f32x2_t*)(part + (wc * 128 + 32 * wr + 16 * m + 4 * fq + r) * 2) = f32x2_t{su[r], sq[r]};
      }
    }
  }
  WAITL0(); BAR();
  if (wc == 0 && lz < 32) {
    const int row = 32 * wr + lz; const f32x2_t p0 = *(const f32x2_t*)(part + row * 2), p1 = *(const f32x2_t*)(part + (128 + row) * 2);
    const u32x4 g = {__float_as_uint(p0[0] + p1[0]), XTAG, __float_as_uint(p0[1] + p1[1]), XTAG};
    st16_wt(ws, WS_XS + (size_t)(((pi * 16 + ft) * 128 + row) * 16), g);
  }
  TS(4, 2);
  {
    const int fr = lz & 15, fq = lz >> 4;
    const float r0 = rs[32 * wr + fr], mm0 = mr[32 * wr + fr], r1 = rs[32 * wr + 16 + fr], mm1 = mr[32 * wr + 16 + fr];
#pragma unroll
    for (int n = 0; n < NTL; ++n) {
      const f32x4 c4 = *(const f32x4*)(cs + 144 + 16 * (nb0 + n) + 4 * fq);
      float g0[4], g1[4];
#pragma unroll
      for (int r = 0; r < 4; ++r) { g0[r] = gelu_t(acc1[n][0][r] * r0 - mm0 * c4[r]); g1[r] = gelu_t(acc1[n][1][r] * r1 - mm1 * c4[r]); }
      z1v[4 * n] = pk2(g0[0], g0[1]); z1v[4 * n + 1] = pk2(g0[2], g0[3]); z1v[4 * n + 2] = pk2(g1[0], g1[1]); z1v[4 * n + 3] = pk2(g1[2], g1[3]);
      PIN(z1v[4 * n]); PIN(z1v[4 * n + 1]); PIN(z1v[4 * n + 2]); PIN(z1v[4 * n + 3]);
    }
  }
  TS(4, 5);
  __syncthreads();
  asm volatile("" : "+v"(lz));
#pragma unroll
  for (int m = 0; m < 2; ++m) {
    const int fr = lz & 15, fq = lz >> 4;
    const int sl = 32 * wr + 16 * m + 4 * fq;
    const f32x4 sa = *(const f32x4*)(st2 + sl * 2), sb = *(const f32x4*)(st2 + sl * 2 + 4);
    const float mu[4] = {sa[0], sa[2], sb[0], sb[2]}, rr[4] = {sa[1], sa[3], sb[1], sb[3]};
#pragma unroll
    for (int n = 0; n < NTL; ++n) {
      const int f = 16 * (nb0 + n) + fr; const u32x2 p = z2p[n][m];
      u32x2 o; o[0] = pk2(bflo(p[0]) * rr[0] - mu[0], bfhi(p[0]) * rr[1] - mu[1]); o[1] = pk2(bflo(p[1]) * rr[2] - mu[2], bfhi(p[1]) * rr[3] - mu[3]);
      *(u32x2*)(lds + (sl >> 6) * 18432 + f * 128 + ((((sl & 63) >> 3) ^ ((f >> 1) & 7)) << 4) + (sl & 4) * 2) = o;
    }
  }
}
template <int NTL>
DI void k5_compute(char* lds, int lane, int w, int tt, int ft, const u16* __restrict__ bsp, char* ws, const unsigned (&z1v)[4 * NTL]) {
  const int wr = w & 3, nb0 = NTL == 5 ? 0 : 5, fr = lane & 15, fq = lane >> 4;
  const C8Addr a = c8_addr<false>(lds, lane, wr, nb0);
  f32x4 acc[NTL][2] = {};
  C8Frag<NTL> f0, f1;
  WAITV(0); BAR();
  TS(5, 1);
  c8_read<NTL, false>(f0, a, 0, 0);
  float bb[2];
#pragma unroll
  for (int m = 0; m < 2; ++m) bb[m] = bf2f(bsp[tt * 128 + 32 * wr + 16 * m + fr]);
  int st = 0;
#define C5_ITER(SIDE0, SIDE1) do { const int stn = st + C8_STG == C8_LDS ? 0 : st + C8_STG; \
    c8_read<NTL, false>(f1, a, st, 1); c8_mma<NTL, true>(acc, f0); c8_sched<NTL, false>(); SB(); SIDE0; SB(); BAR(); SB(); \
    c8_read<NTL, false>(f0, a, stn, 0); c8_mma<NTL, true>(acc, f1); c8_sched<NTL, false>(); SB(); SIDE1; SB(); st = stn; } while (0)
#pragma unroll 1
  for (int kt = 0; kt < 32; ++kt) C5_ITER((void)0, (void)0);
  TS(5, 2);
  __syncthreads();
#pragma unroll
  for (int m = 0; m < 2; ++m) {
    const int t = 32 * wr + 16 * m + fr;
#pragma unroll
    for (int n = 0; n < NTL; ++n) {
      const int f = 16 * (nb0 + n) + 4 * fq; const unsigned z0 = z1v[4 * n + 2 * m], z1 = z1v[4 * n + 2 * m + 1];
      u32x2 o; o[0] = pk2(bflo(z0) * (acc[n][m][0] + bb[m]), bfhi(z0) * (acc[n][m][1] + bb[m]));
      o[1] = pk2(bflo(z1) * (acc[n][m][2] + bb[m]), bfhi(z1) * (acc[n][m][3] + bb[m]));
      *(u32x2*)(lds + t * EP_PITCH + f * 2) = o;
    }
  }
}

DI void p4_copyout(char* ws, char* lds, int w, int pi, int ft) {
  __syncthreads();
  const size_t dst = WS_Z2T + ((size_t)ft * 32 + pi * 2) * 18432;
  for (int i = w * 64 + lane_id(); i < 2304; i += 768) st16_wt(ws, dst + i * 16, *(const u32x4*)(lds + i * 16));
  WAITV(0); __syncthreads();
  if (threadIdx.x == 0) flag_add(ws, F_Z + ft);
  TS(4, 3); TS(4, 7);
}
DI void p5_copyout(char* ws, char* lds, int w, int tt, int ft) {
  __syncthreads();
  const size_t ub = WS_U + ((size_t)tt * 128 * FH + ft * 144) * 2;
  for (int i = w * 64 + lane_id(); i < 128 * 18; i += 768) { const int t = i / 18, c = i - t * 18; st16_wt(ws, ub + (size_t)t * FH * 2 + c * 16, *(const u32x4*)(lds + t * EP_PITCH + c * 16)); }
  WAITV(0); __syncthreads();
  if (threadIdx.x == 0) flag_add(ws, F_U + tt);
  TS(5, 3); TS(5, 7);
}
DI void p45(const u16* __restrict__ wsp, const u16* __restrict__ bsp, char* __restrict__ ws, char* lds) {
  const int lane = lane_id(); const int w = __builtin_amdgcn_readfirstlane(threadIdx.x >> 6); const int tid = w * 64 + lane;
  const int b = blockIdx.x, xc = b & 7, idx = b >> 3;
  const int pi = (xc & 1) * 8 + (idx & 7), ft = (xc >> 1) * 4 + (idx >> 3);
  float* tab = (float*)(lds + K4_TAB);
  TS(4, 0); TS(4, 6);
  const char* Xb = ws + WS_H + (size_t)pi * 128 * D * 2; const char* W2b = ws + WS_WIN + (size_t)ft * 9 * 18432; const char* W1b = W2b + (size_t)16 * 9 * 18432;
  auto src = [&](int p) { return p < 16 ? Xb + tn_off(p * 8 + (lane >> 3), lane, D * 2) : (p < 34 ? W2b + (p - 16) * 1024 : W1b + (p - 34) * 1024) + lane * 16; };
  if (b >= 192) { if (w == 11) flag_wait(ws, F_W, 1024); __syncthreads(); }
#pragma unroll
  for (int j = 0; j < 3; ++j) glds16a<16>(src(16 + w + 12 * j), lds + (16 + w + 12 * j) * 1024);
  if (w == 11) { flag_wait(ws, F_H + pi, 12); flag_wait(ws, F_CS, 64); }
  __syncthreads();
  glds16a<16>(src(w), lds + w * 1024);
  if (w < 4) glds16a<16>(src(12 + w), lds + (12 + w) * 1024);
  const char* Xb5 = (const char*)(wsp + (size_t)pi * 128 * S); const char* Wb5 = ws + WS_Z2T + (size_t)ft * 32 * 18432;
  auto src5 = [&](int p) { return p < 16 ? Xb5 + tn_off(p * 8 + (lane >> 3), lane, S * 2) : Wb5 + (p - 16) * 1024 + lane * 16; };
  if (w >= 8) {
    const int lw = w - 8;
    __builtin_amdgcn_s_setprio(2);
    {
      const char* base[13];
#pragma unroll
      for (int j = 0; j < 13; ++j) base[j] = src(lw + 4 * j);
      auto issue = [&](int kt, int stage) {
#pragma unroll
        for (int j = 0; j < 13; ++j) glds16a<16>(base[j] + (j < 4 ? (long)kt * 128 : (long)kt * 18432), lds + stage * K4_STG + (lw + 4 * j) * 1024);
      };
      issue(1, 1);
      int stg = 2;
#pragma unroll 1
      for (int kt = 0; kt < 7; ++kt) { WAITV(13); BAR(); BAR(); issue(kt + 2, stg); stg = stg == 2 ? 0 : stg + 1; }
      WAITV(13); BAR(); BAR();
      WAITV(0); BAR(); BAR();
      BAR();
      BAR();
      const int row = 32 * lw + (lane >> 1);
      const __amdgpu_buffer_rsrc_t rs_ = __builtin_amdgcn_make_buffer_rsrc((void*)(ws + WS_XS), 0, 2048 * 16 * 16, 0x00020000);
      const unsigned vo = (unsigned)(((pi * 16 + (lane & 1) * 8) * 128 + row) * 16);
      float sum = 0.f, ssq = 0.f;
      for (int spin = 0; spin < (1 << 20); ++spin) {
        bool ok = true; sum = 0.f; ssq = 0.f;
        u32x4 g[8];
#pragma unroll
        for (int t = 0; t < 8; ++t) g[t] = __builtin_amdgcn_raw_buffer_load_b128(rs_, vo + t * 2048, 0, 16);
#pragma unroll
        for (int t = 0; t < 8; ++t) { ok &= g[t][1] == XTAG && g[t][3] == XTAG; sum += __uint_as_float(g[t][0]); ssq += __uint_as_float(g[t][2]); }
        if (__all(ok)) break;
        __builtin_amdgcn_s_sleep(8);
      }
      sum += __shfl_xor(sum, 1); ssq += __shfl_xor(ssq, 1);
      const float mean = sum * (1.f / FH), var = fmaxf(ssq * (1.f / FH) - mean * mean, 0.f), rstd = rsqrtf(var + 1e-5f);
      if ((lane & 1) == 0) *(f32x2_t*)(lds + K4_ST2 + row * 8) = f32x2_t{mean * rstd, rstd};
      __syncthreads();
    }
    p4_copyout(ws, lds, w, pi, ft);
    TS(5, 0); TS(5, 6);
    glds16(src5(w), lds + w * 1024); if (w < 4) glds16(src5(12 + w), lds + (12 + w) * 1024);
    if (w == 11) flag_wait(ws, F_Z + ft, 16);
    __syncthreads();
    glds16a<16>(src5(16 + w), lds + (16 + w) * 1024); if (w < 6) glds16a<16>(src5(28 + w), lds + (28 + w) * 1024);
    loader_loop<32, C8_NS, C8_STG, 34, 16, 4, 0, 16>(lds, lw, src5, [&](int kt, long& ox, long& ow) { ox = (long)kt * 128; ow = (long)kt * 18432; }, NoPost());
    __builtin_amdgcn_s_setprio(0);
    __syncthreads();
    p5_copyout(ws, lds, w, pi, ft);
  } else {
    if (tid < 128) {
      const char* p = ws + WS_ST2 + (size_t)(pi * 128 + tid) * 96;
      float sum = 0.f, ssq = 0.f;
#pragma unroll
      for (int t = 0; t < 12; ++t) { const u32x2 v = ld8_c(p + t * 8); sum += __uint_as_float(v[0]); ssq += __uint_as_float(v[1]); }
      const float mean = sum * (1.f / D), var = fmaxf(ssq * (1.f / D) - mean * mean, 0.f), rstd = rsqrtf(var + 1e-5f);
      tab[tid] = rstd; tab[128 + tid] = mean * rstd;
    } else if (tid < 416) {
      const int c = tid - 128;
      tab[256 + c] = ld4f_c((const float*)(ws + WS_CS) + (c < 144 ? FH + ft * 144 + c : ft * 144 + c - 144));
    }
#define P45_PATH(NTL) do { unsigned z1v[4 * NTL]; \
      k4_compute<NTL>(lds, lane, w, pi, ft, ws, z1v); \
      p4_copyout(ws, lds, w, pi, ft); \
      TS(5, 0); TS(5, 6); \
      glds16(src5(w), lds + w * 1024); if (w < 4) glds16(src5(12 + w), lds + (12 + w) * 1024); \
      __syncthreads(); \
      glds16a<16>(src5(16 + w), lds + (16 + w) * 1024); if (w < 6) glds16a<16>(src5(28 + w), lds + (28 + w) * 1024); \
      k5_compute<NTL>(lds, lane_id(), w, pi, ft, bsp, ws, z1v); \
      p5_copyout(ws, lds, w, pi, ft); } while (0)
    if (w < 4) P45_PATH(5); else P45_PATH(4);
  }
}

__global__ __launch_bounds__(768, 3) void mega_kernel(const u16* __restrict__ x, const u16* __restrict__ wo, const u16* __restrict__ w_in, const u16* __restrict__ w_sp,
                                                      const u16* __restrict__ b_sp, const u16* __restrict__ w_out, u16* __restrict__ out, char* __restrict__ ws) {
  extern __shared__ __attribute__((aligned(16))) char lds[];
  const int b = blockIdx.x;
  p2_attention(wo, w_in, w_out, w_sp, ws, lds);
  if (b < 192) g_body<768, true, 3>(ws, lds, (const u16*)(ws + WS_O), ws + WS_WO, x, (u16*)(ws + WS_H), (float*)(ws + WS_ST2), b);
  else p3_colsum(w_in, ws, lds, b);
  __syncthreads();
  p45(w_sp, b_sp, ws, lds);
  if (b < 192) g_body<FH, false, 0>(ws, lds, (const u16*)(ws + WS_U), ws + WS_WOUT, (const u16*)(ws + WS_H), out, nullptr, b);
}

extern "C" void kernel_launch(void* const* d_in, const int* in_sizes, int n_in, void* d_out, int out_size, void* d_ws, size_t ws_size, hipStream_t stream) {
  const u16* x = (const u16*)d_in[0]; const u16* wqkv = (const u16*)d_in[1]; const u16* wo = (const u16*)d_in[2];
  const u16* w_in = (const u16*)d_in[4]; const u16* w_sp = (const u16*)d_in[5]; const u16* b_sp = (const u16*)d_in[6]; const u16* w_out = (const u16*)d_in[7];
  u16* out = (u16*)d_out; char* ws = (char*)d_ws;
  static bool attr = false;
  if (!attr) {
    (void)hipFuncSetAttribute((const void*)k1_kernel, hipFuncAttributeMaxDynamicSharedMemorySize, K1_LDS);
    (void)hipFuncSetAttribute((const void*)mega_kernel, hipFuncAttributeMaxDynamicSharedMemorySize, ATT_LDS);
    attr = true;
  }
  k1_kernel<<<256, K1_THREADS, K1_LDS, stream>>>(x, wqkv, ws);
  mega_kernel<<<256, 768, ATT_LDS, stream>>>(x, wo, w_in, w_sp, b_sp, w_out, out, ws);
}
```

```cpp
#include <hip/hip_runtime.h>
#include <hip/hip_bf16.h>
#include <stdint.h>
typedef unsigned short u16;
using bf16x8 = __attribute__((ext_vector_type(8))) short;
using s16x4 = __attribute__((ext_vector_type(4))) short;
using f32x4 = __attribute__((ext_vector_type(4))) float;
using f32x16 = __attribute__((ext_vector_type(16))) float;
using u32x4 = __attribute__((ext_vector_type(4))) unsigned;
using u32x2 = __attribute__((ext_vector_type(2))) unsigned;
using u32x32 = __attribute__((ext_vector_type(32))) unsigned;
typedef __bf16 bf16x2_t __attribute__((ext_vector_type(2)));
typedef float f32x2_t __attribute__((ext_vector_type(2)));
constexpr int S = 2048, D = 576, H = 32, DH = 18, F = 4608, FH = 2304;
#define DI __device__ __forceinline__
#define LAS __attribute__((address_space(3)))
#define GAS __attribute__((address_space(1)))
DI int lane_id() { int l = __builtin_amdgcn_mbcnt_hi(~0u, __builtin_amdgcn_mbcnt_lo(~0u, 0u)); asm volatile("" : "+v"(l)); return l; }
DI float bf2f(u16 x) { return __uint_as_float((unsigned)x << 16); }
DI float bflo(unsigned x) { return __uint_as_float(x << 16); }
DI float bfhi(unsigned x) { return __uint_as_float(x & 0xffff0000u); }
DI unsigned pk2(float lo, float hi) { f32x2_t v = {lo, hi}; return __builtin_bit_cast(unsigned, __builtin_convertvector(v, bf16x2_t)); }
#define MFMA32(a, b, c) __builtin_amdgcn_mfma_f32_32x32x16_bf16((a), (b), (c), 0, 0, 0)
#define MFMA16(a, b, c) __builtin_amdgcn_mfma_f32_16x16x32_bf16((a), (b), (c), 0, 0, 0)
#define SB() __builtin_amdgcn_sched_barrier(0)
#define SGB(mask, n) __builtin_amdgcn_sched_group_barrier(mask, n, 0)
#define PIN(x) asm volatile("" : "+v"(x))
#define BAR() __builtin_amdgcn_s_barrier()
#define WAITV(n) asm volatile("s_waitcnt vmcnt(%0)" :: "i"(n) : "memory")
#define WAITL0() asm volatile("s_waitcnt lgkmcnt(0)" ::: "memory")
DI void glds16(const void* g, void* l) { __builtin_amdgcn_global_load_lds((const GAS unsigned*)g, (LAS unsigned*)l, 16, 0, 0); }
template <int AUX> DI void glds16a(const void* g, void* l) { __builtin_amdgcn_global_load_lds((const GAS unsigned*)g, (LAS unsigned*)l, 16, 0, AUX); }
DI void st16_wt(char* ws, size_t off, u32x4 v) {
  const __amdgpu_buffer_rsrc_t r = __builtin_amdgcn_make_buffer_rsrc((void*)ws, 0, 128 << 20, 0x00020000);
  __builtin_amdgcn_raw_buffer_store_b128(v, r, (unsigned)off, 0, 16);
}
typedef LAS const char* lds_cptr;
typedef LAS char* lds_ptr;
#define LDS_RD128(p) (*(const LAS bf16x8*)(p))
#define LDS_RD32(p) (*(const LAS unsigned*)(p))
typedef short v4i16_t __attribute__((ext_vector_type(4)));
DI s16x4 lds_tr(lds_cptr p) { return __builtin_bit_cast(s16x4, __builtin_amdgcn_ds_read_tr16_b64_v4i16((LAS v4i16_t*)p)); }
DI bf16x8 lds_tr2(lds_cptr p0, lds_cptr p1) { const s16x4 a = lds_tr(p0), b = lds_tr(p1); return __builtin_shufflevector(a, b, 0, 1, 2, 3, 4, 5, 6, 7); }

#define TIMING 0
#define TS(k, i)
#define TR(k, i)
#define TSTAMP(v)
#define RSTAMP(v)

constexpr int KV_TILE = 2400, KV_K1 = 1024, KV_V = 1152, KV_VS = 624, KV_Z = KV_V + 608, KV_HEAD = 64 * KV_TILE;
constexpr int Q_TILE = 1152, Q_HEAD = 64 * Q_TILE;
constexpr size_t WS_KV = 0;
constexpr size_t WS_Q = 5u << 20;
constexpr size_t WS_O = 8u << 20;
constexpr size_t WS_H = 11u << 20;
constexpr size_t WS_ST2 = 14u << 20;
constexpr size_t WS_CS = 15u << 20;
constexpr size_t WS_XS = 16u << 20;
constexpr size_t WS_FLAG = WS_XS + (512u << 10);
constexpr size_t WS_Z2T = 17u << 20;
constexpr size_t WS_Z1 = 27u << 20;
constexpr size_t WS_U = 37u << 20;
constexpr size_t WS_WIN = 47u << 20;
constexpr size_t WS_WO = 53u << 20;
constexpr size_t WS_WOUT = 54u << 20;
constexpr size_t WS_DBG = 64u << 20;

constexpr int F_O = 0, F_W = 8, F_H = 9, F_CS = 25, F_Z = 26, F_U = 42;
typedef GAS unsigned gu32;
DI gu32* flagp(char* ws, int i) { return (gu32*)(ws + WS_FLAG + (size_t)i * 256); }
DI void flag_add(char* ws, int i) { __hip_atomic_fetch_add(flagp(ws, i), 1u, __ATOMIC_RELAXED, __HIP_MEMORY_SCOPE_AGENT); }
DI void flag_wait(char* ws, int i, unsigned target) {
  gu32* p = flagp(ws, i);
  for (int spin = 0; spin < (1 << 22); ++spin) {
    if ((unsigned)__builtin_amdgcn_readfirstlane(__hip_atomic_load(p, __ATOMIC_RELAXED, __HIP_MEMORY_SCOPE_AGENT)) >= target) break;
    __builtin_amdgcn_s_sleep(2);
  }
}
DI void st4_wt(void* p, unsigned v) { __hip_atomic_store((gu32*)p, v, __ATOMIC_RELAXED, __HIP_MEMORY_SCOPE_AGENT); }
DI void st8_wt(void* p, u32x2 v) { __hip_atomic_store((GAS unsigned long long*)p, ((unsigned long long)v[1] << 32) | v[0], __ATOMIC_RELAXED, __HIP_MEMORY_SCOPE_AGENT); }
DI u32x2 ld8_c(const void* p) { const unsigned long long v = __hip_atomic_load((GAS unsigned long long*)p, __ATOMIC_RELAXED, __HIP_MEMORY_SCOPE_AGENT); return u32x2{(unsigned)v, (unsigned)(v >> 32)}; }
DI float ld4f_c(const void* p) { return __uint_as_float(__hip_atomic_load((gu32*)p, __ATOMIC_RELAXED, __HIP_MEMORY_SCOPE_AGENT)); }
template <int NP, int NW, int NPX = 0, int AX = 0, int AW = 0, class SRC>
DI void first_tile(char* lds, int w, SRC src) {
#pragma unroll
  for (int j = 0; j < (NP + NW - 1) / NW; ++j) { const int p = w + NW * j; if (p < NP) { if (p < NPX) glds16a<AX>(src(p), lds + p * 1024); else glds16a<AW>(src(p), lds + p * 1024); } }
}
template <int NT, int NS, int STG, int NP, int NPX, int NLD, int AX = 0, int AW = 0, class SRC, class OFF, class POST>
DI void loader_loop(char* lds, int lw, SRC src, OFF off, POST post) {
  constexpr int PPW = (NP + NLD - 1) / NLD, PMIN = NP / NLD;
  static_assert((NS - 1) * PPW + 4 <= 63, "vmcnt");
  static_assert(NPX % NLD == 0, "X pieces");
  const char* base[PPW];
#pragma unroll
  for (int j = 0; j < PPW; ++j) { const int p = lw + NLD * j; base[j] = src(p < NP ? p : NP - 1); }
  auto issue = [&](int kt, int stage) {
    long ox, ow; off(kt, ox, ow);
#pragma unroll
    for (int j = 0; j < PPW; ++j) if (j < PMIN || lw + NLD * j < NP) {
      if (j < NPX / NLD) glds16a<AX>(base[j] + ox, lds + stage * STG + (lw + NLD * j) * 1024); else glds16a<AW>(base[j] + ow, lds + stage * STG + (lw + NLD * j) * 1024); }
  };
  issue(1, 1);
  WAITV(PMIN);
  BAR();
#pragma unroll
  for (int t = 2; t < NS - 1; ++t) issue(t, t);
  post(0);
  int stg = NS - 1, pst = 1;
#pragma unroll 1
  for (int kt = 0; kt < NT - (NS - 1); ++kt) {
    WAITV((NS - 3) * PMIN);
    BAR();
    issue(kt + NS - 1, stg); stg = stg + 1 == NS ? 0 : stg + 1;
    post(pst); pst = pst + 1 == NS ? 0 : pst + 1;
  }
#pragma unroll
  for (int r = NS - 2; r >= 0; --r) {
    WAITV((r - 1 > 0 ? r - 1 : 0) * PMIN); BAR();
    if (r > 0) { post(pst); pst = pst + 1 == NS ? 0 : pst + 1; }
  }
}
struct NoPost { DI void operator()(int) const {} };
DI unsigned tn_off(int row, int lane, int ld_bytes) { return (unsigned)row * ld_bytes + ((((lane & 7) ^ ((row >> 1) & 7))) << 4); }
template <int BN> DI unsigned kn_off(int p, int lane, int ld_bytes) {
  constexpr int CPR = BN / 8;
  const int c = 64 * p + lane, kp = c / CPR, cc = c - kp * CPR, odd = (kp >> 3) & 1, k = kp ^ (odd << 2);
  int cs = cc;
  if (BN == 96) { int blk = (cc >> 1) - odd; if (blk < 0) blk += 6; cs = blk * 2 + (cc & 1); }
  return (unsigned)k * ld_bytes + (unsigned)cs * 16;
}

DI float gelu_t(float x) {
  const float c1 = -2.f * 0.7978845608028654f * 1.4426950408889634f, c2 = c1 * 0.044715f;
  const float e = __builtin_amdgcn_exp2f(x * __builtin_fmaf(x * x, c2, c1));
  return x * __builtin_amdgcn_rcpf(1.f + e);
}

constexpr int C8_STG = 272 * 128, C8_W = 16384, C8_NS = 4, C8_LDS = C8_NS * C8_STG;
struct C8Addr { lds_cptr x0, x1, w0, w1; };
template <bool BKN> DI C8Addr c8_addr(char* lds, int lane, int wr, int nb0) {
  lds_cptr L = (lds_cptr)lds; const int fr = lane & 15, fq = lane >> 4, sw = (fr >> 1) & 7; C8Addr a;
  a.x0 = L + (32 * wr + fr) * 128 + ((fq ^ sw) << 4); a.x1 = L + (32 * wr + fr) * 128 + (((4 + fq) ^ sw) << 4);
  if (BKN) { const int q = fr >> 2, p4 = lane & 3, b = C8_W + (8 * fq + q) * 288 + nb0 * 32 + p4 * 8;
             a.w0 = L + b + (fq & 1) * 1152; a.w1 = L + b + ((fq & 1) ^ 1) * 1152; }
  else { a.w0 = L + C8_W + (16 * nb0 + fr) * 128 + ((fq ^ sw) << 4); a.w1 = L + C8_W + (16 * nb0 + fr) * 128 + (((4 + fq) ^ sw) << 4); }
  return a;
}
template <int NTL> struct C8Frag { bf16x8 x[2], w[NTL]; };
template <int NTL, bool BKN> DI void c8_read(C8Frag<NTL>& f, const C8Addr& a, int st, int ks) {
  f.x[0] = LDS_RD128((ks ? a.x1 : a.x0) + st); f.x[1] = LDS_RD128((ks ? a.x1 : a.x0) + st + 2048);
#pragma unroll
  for (int n = 0; n < NTL; ++n) {
    if (BKN) f.w[n] = lds_tr2(a.w0 + st + ks * 9216 + n * 32, a.w1 + st + ks * 9216 + n * 32);
    else f.w[n] = LDS_RD128((ks ? a.w1 : a.w0) + st + n * 2048);
  }
}
template <int NTL, bool WX> DI void c8_mma(f32x4 (&acc)[NTL][2], const C8Frag<NTL>& f) {
#pragma unroll
  for (int n = 0; n < NTL; ++n) {
#pragma unroll
    for (int m = 0; m < 2; ++m) acc[n][m] = WX ? MFMA16(f.w[n], f.x[m], acc[n][m]) : MFMA16(f.x[m], f.w[n], acc[n][m]);
  }
}
template <int NTL, bool BKN> DI void c8_sched() {
  constexpr int NR = 2 + (BKN ? 2 * NTL : NTL), NM = 2 * NTL;
  if (NR <= NM) {
#pragma unroll
    for (int i = 0; i < NR; ++i) { SGB(0x8, 1); SGB(0x100, 1); }
    SGB(0x8, NM - NR);
  } else {
#pragma unroll
    for (int i = 0; i < NM; ++i) { SGB(0x8, 1); SGB(0x100, 1); if (i < NR - NM) SGB(0x100, 1); }
  }
}

constexpr int K1_THREADS = 768;
constexpr int EP_PITCH = 304;
template <int WC, bool WX>
DI void k1_compute(char* lds, int lane, int w, f32x4 (&acc)[WC == 0 ? 3 : 2][4], float* tabs, char* ws) {
  constexpr int NTL = WC == 0 ? 3 : 2, NB0 = WC == 0 ? 0 : 2 * WC + 1, NST = WC == 0 ? 0 : (WC == 1 ? 2 : 1), M0 = WC == 1 ? 0 : WC - 1, M1 = 3;
  const int wr = w & 1, fr = lane & 15, fq = lane >> 4, sw = (fr >> 1) & 7;
  lds_cptr L = (lds_cptr)lds;
  lds_cptr x0 = L + (64 * wr + fr) * 128 + ((fq ^ sw) << 4), x1 = L + (64 * wr + fr) * 128 + (((4 + fq) ^ sw) << 4);
  const int wb = C8_W + (8 * fq + (fr >> 2)) * 288 + NB0 * 32 + (lane & 3) * 8;
  lds_cptr w0 = L + wb + (fq & 1) * 1152, w1 = L + wb + ((fq & 1) ^ 1) * 1152;
  const u32x4 onesv = {0x3f803f80u, 0x3f803f80u, 0x3f803f80u, 0x3f803f80u}; const bf16x8 ones = __builtin_bit_cast(bf16x8, onesv);
  bf16x8 xa[4], wa[NTL], xb[4], wb_[NTL];
  f32x4 cs[NTL] = {}, rsum[2] = {}, rsq[2] = {};
#define K1RD(X, Wf, st, ks) do { _Pragma("unroll") for (int n = 0; n < NTL; ++n) Wf[n] = lds_tr2(w0 + (st) + (ks) * 9216 + n * 32, w1 + (st) + (ks) * 9216 + n * 32); \
    _Pragma("unroll") for (int m = 0; m < 4; ++m) X[m] = LDS_RD128(((ks) ? x1 : x0) + (st) + m * 2048); } while (0)
#define K1MM(X, Wf) do { _Pragma("unroll") for (int n = 0; n < NTL; ++n) { _Pragma("unroll") for (int m = 0; m < 4; ++m) acc[n][m] = WX ? MFMA16(Wf[n], X[m], acc[n][m]) : MFMA16(X[m], Wf[n], acc[n][m]); \
    cs[n] = MFMA16(ones, Wf[n], cs[n]); } \
    if (NST >= 1) { rsum[0] = MFMA16(X[M0], ones, rsum[0]); rsq[0] = MFMA16(X[M0], X[M0], rsq[0]); } \
    if (NST >= 2) { rsum[1] = MFMA16(X[M1], ones, rsum[1]); rsq[1] = MFMA16(X[M1], X[M1], rsq[1]); } } while (0)
#define K1SCHED() do { _Pragma("unroll") for (int i_ = 0; i_ < 2 * NTL + 4; ++i_) { SGB(0x8, 1); SGB(0x100, 1); } SGB(0x8, 5 * NTL + 2 * NST - (2 * NTL + 4)); } while (0)
  WAITV(0); BAR();
  TS(1, 1);
  K1RD(xa, wa, 0, 0);
  int st = 0;
#pragma unroll 1
  for (int kt = 0; kt < 9; ++kt) {
    const int stn = st + C8_STG == C8_LDS ? 0 : st + C8_STG;
    K1RD(xb, wb_, st, 1); K1MM(xa, wa); K1SCHED();
    SB(); BAR(); SB();
    K1RD(xa, wa, stn, 0); K1MM(xb, wb_); K1SCHED();
    SB();
    st = stn;
  }
  TS(1, 2);
  const int l2 = lane_id(), r2 = l2 & 15, q2 = l2 >> 4;
  if (l2 < 16 && wr == 0) {
#pragma unroll
    for (int n = 0; n < NTL; ++n) tabs[256 + 16 * (NB0 + n) + l2] = cs[n][0];
  }
#pragma unroll
  for (int t = 0; t < NST; ++t) {
    const int mrow = 64 * wr + 16 * (t == 0 ? M0 : M1);
    if (r2 == 0) *(f32x4*)(tabs + mrow + 4 * q2) = rsum[t];
    if (q2 == (r2 >> 2)) tabs[128 + mrow + r2] = (r2 & 2) ? ((r2 & 1) ? rsq[t][3] : rsq[t][2]) : ((r2 & 1) ? rsq[t][1] : rsq[t][0]);
  }
}
template <int NTL>
DI void k1_epilogue(char* lds, int lane, int w, int type, int rt, int hg, const float* tabs, f32x4 (&acc)[NTL][4], char* kvimg) {
  asm volatile("" : "+v"(lane));
  const int wr = w & 1, wc = w >> 1, nb0 = wc == 0 ? 0 : 2 * wc + 1, fr = lane & 15, fq = lane >> 4;
  const float* rs = tabs; const float* mr = tabs + 128; const float* cs = tabs + 256;
  if (type == 2) {
#pragma unroll
    for (int n = 0; n < NTL; ++n) {
      const int col = 16 * (nb0 + n) + fr, hh = col / 18, d = col - hh * 18; const float c = cs[col];
      char* hb = kvimg + (size_t)(hg * 8 + hh) * KV_HEAD + (size_t)(rt * 4 + 2 * wr) * KV_TILE + KV_V + (fq & 1) * 304 + (fq >> 1) * 8 + d * 16;
#pragma unroll
      for (int m = 0; m < 4; ++m) {
        const int row = 64 * wr + 16 * m + 4 * fq;
        const f32x4 r4 = *(const f32x4*)(rs + row), m4 = *(const f32x4*)(mr + row);
        u32x2 o; o[0] = pk2(acc[n][m][0] * r4[0] - m4[0] * c, acc[n][m][1] * r4[1] - m4[1] * c); o[1] = pk2(acc[n][m][2] * r4[2] - m4[2] * c, acc[n][m][3] * r4[3] - m4[3] * c);
        *(u32x2*)(hb + (m >> 1) * KV_TILE + (m & 1) * KV_VS) = o;
      }
    }
  } else {
    const float qs = type == 0 ? 0.23570226039551584f * 1.4426950408889634f : 1.f;
#pragma unroll
    for (int m = 0; m < 4; ++m) {
      const int row = 64 * wr + 16 * m + fr; const float r = rs[row] * qs, mm = mr[row] * qs;
#pragma unroll
      for (int n = 0; n < NTL; ++n) {
        const int col = 16 * (nb0 + n) + 4 * fq; const f32x4 c4 = *(const f32x4*)(cs + col);
        u32x2 o; o[0] = pk2(acc[n][m][0] * r - mm * c4[0], acc[n][m][1] * r - mm * c4[1]); o[1] = pk2(acc[n][m][2] * r - mm * c4[2], acc[n][m][3] * r - mm * c4[3]);
        *(u32x2*)(lds + row * EP_PITCH + col * 2) = o;
      }
    }
  }
}
__global__ __launch_bounds__(K1_THREADS, 3) void k1_kernel(const u16* __restrict__ x, const u16* __restrict__ wqkv, char* __restrict__ ws) {
  extern __shared__ __attribute__((aligned(16))) char lds[];
  const int tid = threadIdx.x, lane = tid & 63; const int w = __builtin_amdgcn_readfirstlane(tid >> 6);
  const int b = blockIdx.x;
  TS(1, 0); TS(1, 6);
  if (b >= 192) {
    if (tid < 512) ((u32x4*)(ws + WS_XS))[(b - 192) * 512 + tid] = u32x4{0, 0, 0, 0};
    else if (tid < 528) ((u32x4*)(ws + WS_FLAG))[(b - 192) * 16 + tid - 512] = u32x4{0, 0, 0, 0};
    return;
  }
  const int xc = b & 7, idx = b >> 3;
  const int rt = xc * 2 + (idx & 1), ct = idx >> 1;
  const int type = ct >> 2, hg = ct & 3;
  float* tabs = (float*)(lds + C8_LDS);
  char* kvimg = ws + WS_KV; char* qimg = ws + WS_Q;
  const char* Xb = (const char*)(x + (size_t)rt * 128 * D); const char* Wb = (const char*)(wqkv + ct * 144);
  auto src = [&](int p) { return p < 16 ? Xb + tn_off(p * 8 + (lane >> 3), lane, D * 2) : Wb + kn_off<144>(p - 16, lane, 1728 * 2); };
  first_tile<34, 12>(lds, w, src);
  if (w >= 8) {
    __builtin_amdgcn_s_setprio(2);
    loader_loop<9, C8_NS, C8_STG, 34, 16, 4>(lds, w - 8, src, [&](int kt, long& ox, long& ow) { ox = (long)kt * 128; ow = (long)kt * 64 * 1728 * 2; }, NoPost());
    __syncthreads();
    const int row = (w - 8) * 64 + lane_id();
    if (row < 128) { const float mean = tabs[row] * (1.f / D), var = fmaxf(tabs[128 + row] * (1.f / D) - mean * mean, 0.f), rstd = rsqrtf(var + 1e-5f); tabs[row] = rstd; tabs[128 + row] = mean * rstd; }
    __syncthreads();
  } else {
#define K1_PATH(WC, WX) do { f32x4 acc[WC == 0 ? 3 : 2][4] = {}; \
      k1_compute<WC, WX>(lds, lane, w, acc, tabs, ws); \
      __syncthreads(); __syncthreads(); \
      k1_epilogue<WC == 0 ? 3 : 2>(lds, lane_id(), w, WX ? (type & 1) : 2, rt, hg, tabs, acc, kvimg); } while (0)
#define K1_SEL(WX) do { if (w < 2) K1_PATH(0, WX); else if (w < 4) K1_PATH(1, WX); else if (w < 6) K1_PATH(2, WX); else K1_PATH(3, WX); } while (0)
    if (type == 2) K1_SEL(false); else K1_SEL(true);
  }
  const int tid2 = w * 64 + lane_id();
  if (type == 2) {
    if (tid2 < 192) {
      const int tl = tid2 / 48, r = tid2 - tl * 48, hh = r / 6, c = r - hh * 6;
      char* tb = kvimg + (size_t)(hg * 8 + hh) * KV_HEAD + (size_t)(rt * 4 + tl) * KV_TILE;
      if (c < 4) *(u32x4*)(tb + KV_V + (c >> 1) * KV_VS + (c & 1) * 304 + 18 * 16) = u32x4{0x3f803f80u, 0x3f803f80u, 0x3f803f80u, 0x3f803f80u};
      else *(u32x4*)(tb + KV_Z + (c - 4) * KV_VS) = u32x4{0, 0, 0, 0};
    }
    TS(1, 3); TS(1, 7);
    return;
  }
  __syncthreads();
  for (int it = tid2; it < 1024; it += K1_THREADS) {
    const int tok = it & 127, hh = it >> 7;
    const unsigned* sp = (const unsigned*)(lds + tok * EP_PITCH + hh * 36);
    u32x4 c0 = {sp[0], sp[1], sp[2], sp[3]}, c1 = {sp[4], sp[5], sp[6], sp[7]}; const unsigned c2 = sp[8];
    char* tb = type == 0 ? qimg + (size_t)(hg * 8 + hh) * Q_HEAD + (size_t)(rt * 4 + (tok >> 5)) * Q_TILE
                         : kvimg + (size_t)(hg * 8 + hh) * KV_HEAD + (size_t)(rt * 4 + (tok >> 5)) * KV_TILE;
    const int key = tok & 31;
    *(u32x4*)(tb + key * 16) = c0; *(u32x4*)(tb + 512 + key * 16) = c1; *(unsigned*)(tb + 1024 + key * 4) = c2;
  }
  TS(1, 3); TS(1, 7);
}
static_assert(128 * EP_PITCH <= C8_LDS, "");
constexpr int K1_LDS = C8_LDS + 400 * 4;

constexpr int ATT_LDS = KV_HEAD + 8 * Q_TILE;
#define SWITCH 50
DI void p2_attention(const u16* __restrict__ wo, const u16* __restrict__ w_in, const u16* __restrict__ w_out, const u16* __restrict__ w_sp, char* __restrict__ ws, char* lds) {
  TS(2, 0); TS(2, 6);
  const char* kvimg = ws + WS_KV; const char* qimg = ws + WS_Q; u16* o = (u16*)(ws + WS_O);
  const int tid = threadIdx.x, lane = tid & 63; const int w = __builtin_amdgcn_readfirstlane(tid >> 6);
  const int b = blockIdx.x, xcd = b & 7, idx = b >> 3, h = xcd * 4 + (idx >> 3), qb = idx & 7;
  const char* kvg = kvimg + (size_t)h * KV_HEAD + lane * 16;
  if (w < 8) {
    const char* qg = qimg + (size_t)h * Q_HEAD + (qb * 8 + w) * Q_TILE + lane * 16;
    char* qtile = lds + KV_HEAD + w * Q_TILE;
    glds16(qg, qtile);
    if (lane < 8) glds16(qg + 1024, qtile + 1024);
  }
  glds16(kvg + w * 1024, lds + w * 1024); glds16(kvg + (w + 12) * 1024, lds + (w + 12) * 1024);
  WAITV(0);
  BAR();
  if (w >= 8) {
    const int lw = w - 8;
    __builtin_amdgcn_s_setprio(3);
#pragma unroll 1
    for (int p = 24 + lw; p < 150; p += 4) glds16(kvg + p * 1024, lds + p * 1024);
    WAITV(0);
    BAR();
    {
      const u32x4* wp = (const u32x4*)w_sp + (size_t)(b * 4 + lw) * 512 + lane;
      u32x4 acc = wp[0];
#pragma unroll
      for (int i = 1; i < 8; ++i) acc ^= wp[i * 64];
      if (acc[0] == 0x12345678u && acc[1] == 0x9abcdef0u && acc[2] == 0x0fedcba9u && acc[3] == 0x87654321u) ((unsigned*)(ws + WS_DBG))[0] = 1;
    }
    const int gw = b * 4 + lw, c = lane & 7, nb = lane >> 3;
    if (nb < 6) {
#pragma unroll 1
      for (int blk = gw; blk < 864 + 144 + 432; blk += 1024) {
        const u16* sp; int ld, kt, n0; size_t db;
        if (blk < 864) { kt = blk / 96; const int nbk = blk - kt * 96, z2 = nbk >= 48, nn = z2 ? nbk - 48 : nbk, t = nn / 3; n0 = nbk * 48; sp = w_in; ld = F;
          db = WS_WIN + ((size_t)((z2 ? 0 : 16) + t) * 9 + kt) * 18432 + (nn - t * 3) * 48 * 128; }
        else if (blk < 1008) { const int q = blk - 864; kt = q / 12; const int nbk = q - kt * 12; n0 = nbk * 48; sp = wo; ld = D; db = WS_WO + ((size_t)(nbk >> 1) * 12 + kt) * 12288 + (nbk & 1) * 48 * 128; }
        else { const int q = blk - 1008; kt = q / 12; const int nbk = q - kt * 12; n0 = nbk * 48; sp = w_out; ld = D; db = WS_WOUT + ((size_t)(nbk >> 1) * 36 + kt) * 12288 + (nbk & 1) * 48 * 128; }
        u32x4 r[8];
        if (blk >= 864 && blk < 1008) {
          const int kc = kt * 8 + c, hh = kc / 3, part = kc - hh * 3;
          const u16* sq = sp + (size_t)(hh * DH) * ld + n0 + nb * 8;
#pragma unroll
          for (int j = 0; j < 8; ++j) {
            const int d = part == 2 ? 16 + j : (j & 3) + 8 * (j >> 2) + 4 * part;
            r[j] = (part == 2 && j >= 2) ? u32x4{0, 0, 0, 0} : *(const u32x4*)(sq + (size_t)(d < DH ? d : 0) * ld);
          }
        } else {
          sp += (size_t)(kt * 64 + c * 8) * ld + n0 + nb * 8;
#pragma unroll
          for (int j = 0; j < 8; ++j) r[j] = *(const u32x4*)(sp + (size_t)j * ld);
        }
        const unsigned lo = (unsigned)db + nb * 1024;
#pragma unroll
        for (int e = 0; e < 4; ++e) {
          const unsigned sl = (unsigned)((c ^ ((4 * nb + e) & 7)) << 4);
#pragma unroll
          for (int hi = 0; hi < 2; ++hi) {
            const unsigned sel = hi ? 0x07060302u : 0x05040100u;
            u32x4 o;
#pragma unroll
            for (int q = 0; q < 4; ++q) o[q] = __builtin_amdgcn_perm(r[2 * q + 1][e], r[2 * q][e], sel);
            st16_wt(ws, lo + (2 * e + hi) * 128 + sl, o);
          }
        }
      }
    }
    WAITV(0);
    if (lane == 0) flag_add(ws, F_W);
    __builtin_amdgcn_s_setprio(0);
  } else {
  TS(2, 1); TR(2, 4);
  if (w >= 4) __builtin_amdgcn_s_setprio(1);
  const int r32 = lane & 31, hh = lane >> 5;
  lds_cptr L = (lds_cptr)lds;
  const bf16x8 qf0 = LDS_RD128(L + KV_HEAD + w * Q_TILE + lane * 16);
  u32x4 q1v = {0, 0, 0, 0}; { unsigned t = LDS_RD32(L + KV_HEAD + w * Q_TILE + 1024 + r32 * 4); q1v[0] = hh ? 0u : t; }
  const bf16x8 qf1 = __builtin_bit_cast(bf16x8, q1v);
  lds_cptr kp0 = L + lane * 16, kp1 = L + (hh ? KV_Z : KV_K1 + r32 * 4), vp = L + (r32 <= 18 ? KV_V + hh * 304 + r32 * 16 : KV_Z);
  f32x16 negm;
  {
    bf16x8 k0 = LDS_RD128(kp0); u32x4 k1 = {LDS_RD32(kp1), 0, 0, 0};
    f32x16 s = {}; s = MFMA32(k0, qf0, s); s = MFMA32(__builtin_bit_cast(bf16x8, k1), qf1, s);
    float m = s[0];
#pragma unroll
    for (int i = 1; i < 16; ++i) m = fmaxf(m, s[i]);
    auto rr = __builtin_amdgcn_permlane32_swap(__float_as_uint(m), __float_as_uint(m), false, false);
    m = fmaxf(__uint_as_float(rr[0]), __uint_as_float(rr[1]));
#pragma unroll
    for (int i = 0; i < 16; ++i) negm[i] = -m;
  }
  f32x16 O = {};
#define EX2(Sx, i) pk2(__builtin_amdgcn_exp2f(Sx[i]), __builtin_amdgcn_exp2f(Sx[(i) + 1]))
  bf16x8 k0, v0, v1; u32x4 k1 = {0, 0, 0, 0};
  f32x16 SA, SBc; u32x4 pA0, pA1, pB0, pB1;
#define STEP(Sc, Sn, Pc0, Pc1, Pp0, Pp1, koff, voff) do { \
    SB(); Sn = MFMA32(k0, qf0, negm); SB(); \
    Pc0[0] = EX2(Sc, 0); Pc0[1] = EX2(Sc, 2); PIN(Pc0); SB(); \
    Sn = MFMA32(__builtin_bit_cast(bf16x8, k1), qf1, Sn); SB(); \
    k0 = LDS_RD128(kp0 + (koff)); k1[0] = LDS_RD32(kp1 + (koff)); SB(); \
    Pc0[2] = EX2(Sc, 4); Pc0[3] = EX2(Sc, 6); PIN(Pc0); SB(); \
    O = MFMA32(v0, __builtin_bit_cast(bf16x8, Pp0), O); SB(); \
    Pc1[0] = EX2(Sc, 8); Pc1[1] = EX2(Sc, 10); PIN(Pc1); SB(); \
    O = MFMA32(v1, __builtin_bit_cast(bf16x8, Pp1), O); SB(); \
    v0 = LDS_RD128(vp + (voff)); v1 = LDS_RD128(vp + (voff) + KV_VS); SB(); \
    Pc1[2] = EX2(Sc, 12); Pc1[3] = EX2(Sc, 14); PIN(Pc1); SB(); \
  } while (0)
  k0 = LDS_RD128(kp0); k1[0] = LDS_RD32(kp1);
  SA = MFMA32(k0, qf0, negm); SA = MFMA32(__builtin_bit_cast(bf16x8, k1), qf1, SA);
  k0 = LDS_RD128(kp0 + KV_TILE); k1[0] = LDS_RD32(kp1 + KV_TILE);
  v0 = LDS_RD128(vp); v1 = LDS_RD128(vp + KV_VS);
  pB0 = u32x4{0, 0, 0, 0}; pB1 = u32x4{0, 0, 0, 0};
  int off = 0;
  for (int t = 0; t < 8; t += 2) {
    STEP(SA, SBc, pA0, pA1, pB0, pB1, off + 2 * KV_TILE, off);
    STEP(SBc, SA, pB0, pB1, pA0, pA1, off + 3 * KV_TILE, off + KV_TILE);
    off += 2 * KV_TILE;
  }
  BAR();
  for (int t = 8; t < 62; t += 2) {
    if (t == SWITCH) __builtin_amdgcn_s_setprio(0);
    STEP(SA, SBc, pA0, pA1, pB0, pB1, off + 2 * KV_TILE, off);
    STEP(SBc, SA, pB0, pB1, pA0, pA1, off + 3 * KV_TILE, off + KV_TILE);
    off += 2 * KV_TILE;
  }
  STEP(SA, SBc, pA0, pA1, pB0, pB1, off + KV_TILE, off);
  STEP(SBc, SA, pB0, pB1, pA0, pA1, off + KV_TILE, off + KV_TILE);
  O = MFMA32(v0, __builtin_bit_cast(bf16x8, pB0), O); O = MFMA32(v1, __builtin_bit_cast(bf16x8, pB1), O);
  TS(2, 2); TR(2, 5);
  {
    float mx = 0.f;
#pragma unroll
    for (int i = 0; i < 11; ++i) mx = fmaxf(mx, fabsf(O[i]));
    bool bad = !(mx < 1.2676506e30f);
#pragma unroll
    for (int i = 0; i < 11; ++i) bad |= (O[i] != O[i]);
    if (__any(bad)) {
      float m = -3.0e38f; O = f32x16{};
#pragma unroll 1
      for (int t = 0; t < 64; ++t) {
        const int of = t * KV_TILE;
        const bf16x8 a0 = LDS_RD128(kp0 + of); const u32x4 a1 = {LDS_RD32(kp1 + of), 0, 0, 0};
        f32x16 s = {}; s = MFMA32(a0, qf0, s); s = MFMA32(__builtin_bit_cast(bf16x8, a1), qf1, s);
        float tm = s[0];
#pragma unroll
        for (int i = 1; i < 16; ++i) tm = fmaxf(tm, s[i]);
        auto rr = __builtin_amdgcn_permlane32_swap(__float_as_uint(tm), __float_as_uint(tm), false, false);
        tm = fmaxf(__uint_as_float(rr[0]), __uint_as_float(rr[1]));
        const float mn = fmaxf(m, tm), al = __builtin_amdgcn_exp2f(m - mn); m = mn;
#pragma unroll
        for (int i = 0; i < 16; ++i) { O[i] *= al; s[i] -= mn; }
        u32x4 p0, p1;
#pragma unroll
        for (int i = 0; i < 4; ++i) { p0[i] = EX2(s, 2 * i); p1[i] = EX2(s, 8 + 2 * i); }
        O = MFMA32(LDS_RD128(vp + of), __builtin_bit_cast(bf16x8, p0), O); O = MFMA32(LDS_RD128(vp + of + KV_VS), __builtin_bit_cast(bf16x8, p1), O);
      }
    }
  }
  {
    float l = O[10];
    { auto rr = __builtin_amdgcn_permlane32_swap(__float_as_uint(l), __float_as_uint(l), false, false); l = __uint_as_float(rr[0]); }
    const float inv = 1.f / l;
    const size_t ob = WS_O + (size_t)(qb * 256 + w * 32 + r32) * 1536 + h * 48;
    const u32x4 c0 = {pk2(O[0] * inv, O[1] * inv), pk2(O[2] * inv, O[3] * inv), pk2(O[4] * inv, O[5] * inv), pk2(O[6] * inv, O[7] * inv)};
    st16_wt(ws, ob + hh * 16, c0);
    if (hh == 0) st16_wt(ws, ob + 32, u32x4{pk2(O[8] * inv, O[9] * inv), 0, 0, 0});
  }
  TS(2, 3);
  }
  WAITV(0); __syncthreads();
  if (tid == 0) flag_add(ws, F_O + qb);
  TS(2, 7);
}

constexpr int G_STG = 160 * 128, G_W = 8192, G_NS = 6, G_LDS = G_NS * G_STG;
template <int K, bool STATS, int KID>
DI void g_body(char* ws, char* lds, const u16* __restrict__ A, const char* __restrict__ W, const u16* __restrict__ res, u16* __restrict__ out, float* __restrict__ st2, int b) {
  constexpr int NT = K / 64;
  const int lane = lane_id(); const int w = __builtin_amdgcn_readfirstlane(threadIdx.x >> 6);
  const int xc = b & 7, idx = b >> 3, rt = xc * 4 + (idx & 3), ct = idx >> 2;
  const int m0 = rt * 64, n0 = ct * 96;
  TS(KID, 0); TS(KID, 6);
  const char* Xb = (const char*)(A + (size_t)m0 * K); const char* Wb = W + (size_t)ct * NT * 12288;
  auto src = [&](int p) { return p < 8 ? Xb + tn_off(p * 8 + (lane >> 3), lane, K * 2) : Wb + (p - 8) * 1024 + lane * 16; };
  if (STATS) { if (w == 11) flag_wait(ws, F_W, 1024); __syncthreads(); }
  glds16a<16>(src(8 + w), lds + (8 + w) * 1024);
  if (w == 11) { if (STATS) flag_wait(ws, F_O + (rt >> 2), 32); else flag_wait(ws, F_U + (rt >> 1), 16); }
  __syncthreads();
  if (w < 8) glds16a<16>(src(w), lds + w * 1024);
  if (w >= 4) {
    __builtin_amdgcn_s_setprio(2);
    loader_loop<NT, G_NS, G_STG, 20, 8, 8, 16, 16>(lds, w - 4, src, [&](int kt, long& ox, long& ow) { ox = (long)kt * 128; ow = (long)kt * 12288; }, NoPost());
    __builtin_amdgcn_s_setprio(0);
  } else {
  const int wr = w & 1, wc = w >> 1, fr = lane & 15, fq = lane >> 4, sw = (fr >> 1) & 7;
  lds_cptr L = (lds_cptr)lds;
  lds_cptr x0 = L + (32 * wr + fr) * 128 + ((fq ^ sw) << 4), x1 = L + (32 * wr + fr) * 128 + (((4 + fq) ^ sw) << 4);
  lds_cptr w0 = L + G_W + (48 * wc + fr) * 128 + ((fq ^ sw) << 4), w1 = L + G_W + (48 * wc + fr) * 128 + (((4 + fq) ^ sw) << 4);
  f32x4 acc[3][2] = {};
  bf16x8 xa[2], wa[3], xb[2], wb_[3];
#define GRD(X, Wf, st, ks) do { X[0] = LDS_RD128(((ks) ? x1 : x0) + (st)); X[1] = LDS_RD128(((ks) ? x1 : x0) + (st) + 2048); \
    _Pragma("unroll") for (int n = 0; n < 3; ++n) Wf[n] = LDS_RD128(((ks) ? w1 : w0) + (st) + n * 2048); } while (0)
#define GSCHED() do { _Pragma("unroll") for (int i_ = 0; i_ < 5; ++i_) { SGB(0x8, 1); SGB(0x100, 1); } SGB(0x8, 1); } while (0)
#define GMM(X, Wf) do { _Pragma("unroll") for (int n = 0; n < 3; ++n) { acc[n][0] = MFMA16(Wf[n], X[0], acc[n][0]); acc[n][1] = MFMA16(Wf[n], X[1], acc[n][1]); } } while (0)
  WAITV(0); BAR();
  TS(KID, 1);
  GRD(xa, wa, 0, 0);
  u32x2 rv[2][3];
#pragma unroll
  for (int m = 0; m < 2; ++m)
#pragma unroll
    for (int n = 0; n < 3; ++n) rv[m][n] = ld8_c(res + (size_t)(m0 + 32 * wr + 16 * m + fr) * D + n0 + 48 * wc + 16 * n + 4 * fq);
  int st = 0;
#pragma unroll 1
  for (int kt = 0; kt < NT; ++kt) {
    const int stn = st + G_STG == G_LDS ? 0 : st + G_STG;
    GRD(xb, wb_, st, 1);
    GMM(xa, wa);
    GSCHED();
    SB(); BAR(); SB();
    GRD(xa, wa, stn, 0);
    GMM(xb, wb_);
    GSCHED();
    SB();
    st = stn;
  }
  TS(KID, 2);
#pragma unroll
  for (int m = 0; m < 2; ++m) {
    const int row = m0 + 32 * wr + 16 * m + fr;
    float sum = 0.f, ssq = 0.f;
#pragma unroll
    for (int n = 0; n < 3; ++n) {
      const int col = n0 + 48 * wc + 16 * n + 4 * fq;
      const u32x2 r = rv[m][n];
      u32x2 o; o[0] = pk2(bflo(r[0]) + acc[n][m][0], bfhi(r[0]) + acc[n][m][1]); o[1] = pk2(bflo(r[1]) + acc[n][m][2], bfhi(r[1]) + acc[n][m][3]);
      if (STATS) st8_wt(out + (size_t)row * D + col, o); else *(u32x2*)(out + (size_t)row * D + col) = o;
      if (STATS) { const float a = bflo(o[0]), bq = bfhi(o[0]), c = bflo(o[1]), d = bfhi(o[1]); sum += (a + bq) + (c + d); ssq += (a * a + bq * bq) + (c * c + d * d); }
    }
    if (STATS) {
      sum += __shfl_xor(sum, 16); sum += __shfl_xor(sum, 32); ssq += __shfl_xor(ssq, 16); ssq += __shfl_xor(ssq, 32);
      if (fq == 0) st8_wt(st2 + ((size_t)row * 12 + ct * 2 + wc) * 2, u32x2{__float_as_uint(sum), __float_as_uint(ssq)});
    }
  }
  TS(KID, 3);
  }
  if (STATS) { WAITV(0); __syncthreads(); if (threadIdx.x == 0) flag_add(ws, F_H + (rt >> 1)); }
  TS(KID, 7);
}
DI void p3_colsum(const u16* __restrict__ w_in, char* __restrict__ ws, char* lds, int b) {
    const int tid = threadIdx.x, c = tid % 9, rg = tid / 9;
    float* red = (float*)lds;
    if (rg < 48) {
      const char* p = (const char*)(w_in + (b - 192) * 72 + c * 8) + (size_t)rg * (F * 2);
      u32x4 v[12];
#pragma unroll
      for (int i = 0; i < 12; ++i) v[i] = *(const u32x4*)(p + (size_t)i * 48 * F * 2);
      float a[8] = {};
#pragma unroll
      for (int i = 0; i < 12; ++i)
#pragma unroll
        for (int e = 0; e < 4; ++e) { a[2 * e] += bflo(v[i][e]); a[2 * e + 1] += bfhi(v[i][e]); }
#pragma unroll
      for (int e = 0; e < 8; ++e) red[rg * 72 + c * 8 + e] = a[e];
    }
    __syncthreads();
    if (tid < 72) { float t = 0.f; for (int g = 0; g < 48; ++g) t += red[g * 72 + tid]; st4_wt((float*)(ws + WS_CS) + (b - 192) * 72 + tid, __float_as_uint(t)); }
    WAITV(0); __syncthreads();
    if (tid == 0) flag_add(ws, F_CS);
}
constexpr unsigned XTAG = 0x9E3779B9u;
DI float dpp_sum16(float v) {
  v += __builtin_bit_cast(float, __builtin_amdgcn_mov_dpp(__builtin_bit_cast(int, v), 0xB1, 0xf, 0xf, true));
  v += __builtin_bit_cast(float, __builtin_amdgcn_mov_dpp(__builtin_bit_cast(int, v), 0x4E, 0xf, 0xf, true));
  v += __builtin_bit_cast(float, __builtin_amdgcn_mov_dpp(__builtin_bit_cast(int, v), 0x141, 0xf, 0xf, true));
  v += __builtin_bit_cast(float, __builtin_amdgcn_mov_dpp(__builtin_bit_cast(int, v), 0x140, 0xf, 0xf, true));
  return v;
}
constexpr int K4_STG = (128 + 288) * 128, K4_W2 = 16384, K4_W1 = 16384 + 18432, K4_NS = 3;
constexpr int K4_TAB = K4_NS * K4_STG;
constexpr int K4_PART = 40960, K4_ST2 = 45056;
constexpr int K4_LDS = K4_TAB + 544 * 4;
template <int NTL>
DI void k4_compute(char* lds, int lane, int w, int pi, int ft, char* ws, unsigned (&z1v)[4 * NTL]) {
  const int wr = w & 3, nb0 = NTL == 5 ? 0 : 5, wc = NTL == 5 ? 0 : 1, fr = lane & 15, fq = lane >> 4, sw = (fr >> 1) & 7;
  lds_cptr L = (lds_cptr)lds;
  lds_cptr x0 = L + (32 * wr + fr) * 128 + ((fq ^ sw) << 4), x1 = L + (32 * wr + fr) * 128 + (((4 + fq) ^ sw) << 4);
  lds_cptr w0 = L + K4_W2 + (16 * nb0 + fr) * 128 + ((fq ^ sw) << 4), w1 = L + K4_W2 + (16 * nb0 + fr) * 128 + (((4 + fq) ^ sw) << 4);
  f32x4 acc2[NTL][2] = {}, acc1[NTL][2] = {};
  bf16x8 xa[2], xb[2], wa[NTL], wb[NTL];
#define RDX(X, P, st) do { X[0] = LDS_RD128(P + (st)); X[1] = LDS_RD128(P + (st) + 2048); } while (0)
#define RDW(Wf, P, st) do { _Pragma("unroll") for (int n = 0; n < NTL; ++n) Wf[n] = LDS_RD128(P + (st) + n * 2048); } while (0)
#define MM2(X, Wf) do { _Pragma("unroll") for (int n = 0; n < NTL; ++n) { acc2[n][0] = MFMA16(X[0], Wf[n], acc2[n][0]); acc2[n][1] = MFMA16(X[1], Wf[n], acc2[n][1]); } } while (0)
#define MM1(X, Wf) do { _Pragma("unroll") for (int n = 0; n < NTL; ++n) { acc1[n][0] = MFMA16(Wf[n], X[0], acc1[n][0]); acc1[n][1] = MFMA16(Wf[n], X[1], acc1[n][1]); } } while (0)
#define SCH(NR) do { _Pragma("unroll") for (int i_ = 0; i_ < (NR); ++i_) { SGB(0x8, 1); SGB(0x100, 1); } SGB(0x8, 2 * NTL - (NR)); SB(); } while (0)
  WAITV(0); WAITL0();
  if (NTL == 4) BAR();
  int st = 0;
#pragma unroll 1
  for (int kt = 0; kt < 9; ++kt) {
    BAR(); SB();
    if (kt == 0) TS(4, 1);
    RDX(xa, x0, st); RDW(wa, w0, st); SB();
    RDW(wb, w0, st + 18432); MM2(xa, wa); SCH(NTL);
    RDX(xb, x1, st); RDW(wa, w1, st); MM1(xa, wb); SCH(NTL + 2);
    BAR(); SB();
    RDW(wb, w1, st + 18432); MM2(xb, wa); SCH(NTL);
    MM1(xb, wb); SB();
    st = st + K4_STG == K4_TAB ? 0 : st + K4_STG;
  }
  if (NTL == 5) BAR();
  TS(4, 4);
  int lz = lane_id();
  const float* tab = (const float*)(lds + K4_TAB); const float* rs = tab; const float* mr = tab + 128; const float* cs = tab + 256;
  float* part = (float*)(lds + K4_PART); const float* st2 = (const float*)(lds + K4_ST2);
  u32x2 z2p[NTL][2];
  {
    const int fr = lz & 15, fq = lz >> 4;
#pragma unroll
    for (int m = 0; m < 2; ++m) {
      const f32x4 r4 = *(const f32x4*)(rs + 32 * wr + 16 * m + 4 * fq), m4 = *(const f32x4*)(mr + 32 * wr + 16 * m + 4 * fq);
      f32x4 su = {0, 0, 0, 0}, sq = {0, 0, 0, 0};
#pragma unroll
      for (int n = 0; n < NTL; ++n) {
        const float c = cs[16 * (nb0 + n) + fr];
        f32x4 g;
#pragma unroll
        for (int r = 0; r < 4; ++r) { g[r] = gelu_t(acc2[n][m][r] * r4[r] - m4[r] * c); su[r] += g[r]; sq[r] = __builtin_fmaf(g[r], g[r], sq[r]); }
        z2p[n][m][0] = pk2(g[0], g[1]); z2p[n][m][1] = pk2(g[2], g[3]); PIN(z2p[n][m]);
      }
#pragma unroll
      for (int r = 0; r < 4; ++r) { su[r] = dpp_sum16(su[r]); sq[r] = dpp_sum16(sq[r]); }
      if (fr == 0) {
#pragma unroll
        for (int r = 0; r < 4; ++r) *(f32x2_t*)(part + (wc * 128 + 32 * wr + 16 * m + 4 * fq + r) * 2) = f32x2_t{su[r], sq[r]};
      }
    }
  }
  WAITL0(); BAR();
  if (wc == 0 && lz < 32) {
    const int row = 32 * wr + lz; const f32x2_t p0 = *(const f32x2_t*)(part + row * 2), p1 = *(const f32x2_t*)(part + (128 + row) * 2);
    const u32x4 g = {__float_as_uint(p0[0] + p1[0]), XTAG, __float_as_uint(p0[1] + p1[1]), XTAG};
    st16_wt(ws, WS_XS + (size_t)(((pi * 16 + ft) * 128 + row) * 16), g);
  }
  TS(4, 2);
  {
    const int fr = lz & 15, fq = lz >> 4;
    const float r0 = rs[32 * wr + fr], mm0 = mr[32 * wr + fr], r1 = rs[32 * wr + 16 + fr], mm1 = mr[32 * wr + 16 + fr];
#pragma unroll
    for (int n = 0; n < NTL; ++n) {
      const f32x4 c4 = *(const f32x4*)(cs + 144 + 16 * (nb0 + n) + 4 * fq);
      float g0[4], g1[4];
#pragma unroll
      for (int r = 0; r < 4; ++r) { g0[r] = gelu_t(acc1[n][0][r] * r0 - mm0 * c4[r]); g1[r] = gelu_t(acc1[n][1][r] * r1 - mm1 * c4[r]); }
      z1v[4 * n] = pk2(g0[0], g0[1]); z1v[4 * n + 1] = pk2(g0[2], g0[3]); z1v[4 * n + 2] = pk2(g1[0], g1[1]); z1v[4 * n + 3] = pk2(g1[2], g1[3]);
      PIN(z1v[4 * n]); PIN(z1v[4 * n + 1]); PIN(z1v[4 * n + 2]); PIN(z1v[4 * n + 3]);
    }
  }
  TS(4, 5);
  __syncthreads();
  asm volatile("" : "+v"(lz));
#pragma unroll
  for (int m = 0; m < 2; ++m) {
    const int fr = lz & 15, fq = lz >> 4;
    const int sl = 32 * wr + 16 * m + 4 * fq;
    const f32x4 sa = *(const f32x4*)(st2 + sl * 2), sb = *(const f32x4*)(st2 + sl * 2 + 4);
    const float mu[4] = {sa[0], sa[2], sb[0], sb[2]}, rr[4] = {sa[1], sa[3], sb[1], sb[3]};
#pragma unroll
    for (int n = 0; n < NTL; ++n) {
      const int f = 16 * (nb0 + n) + fr; const u32x2 p = z2p[n][m];
      u32x2 o; o[0] = pk2(bflo(p[0]) * rr[0] - mu[0], bfhi(p[0]) * rr[1] - mu[1]); o[1] = pk2(bflo(p[1]) * rr[2] - mu[2], bfhi(p[1]) * rr[3] - mu[3]);
      *(u32x2*)(lds + (sl >> 6) * 18432 + f * 128 + ((((sl & 63) >> 3) ^ ((f >> 1) & 7)) << 4) + (sl & 4) * 2) = o;
    }
  }
}
template <int NTL>
DI void k5_compute(char* lds, int lane, int w, int tt, int ft, const u16* __restrict__ bsp, char* ws, const unsigned (&z1v)[4 * NTL]) {
  const int wr = w & 3, nb0 = NTL == 5 ? 0 : 5, fr = lane & 15, fq = lane >> 4;
  const C8Addr a = c8_addr<false>(lds, lane, wr, nb0);
  f32x4 acc[NTL][2] = {};
  C8Frag<NTL> f0, f1;
  WAITV(0); BAR();
  TS(5, 1);
  c8_read<NTL, false>(f0, a, 0, 0);
  float bb[2];
#pragma unroll
  for (int m = 0; m < 2; ++m) bb[m] = bf2f(bsp[tt * 128 + 32 * wr + 16 * m + fr]);
  int st = 0;
#define C5_ITER(SIDE0, SIDE1) do { const int stn = st + C8_STG == C8_LDS ? 0 : st + C8_STG; \
    c8_read<NTL, false>(f1, a, st, 1); c8_mma<NTL, true>(acc, f0); c8_sched<NTL, false>(); SB(); SIDE0; SB(); BAR(); SB(); \
    c8_read<NTL, false>(f0, a, stn, 0); c8_mma<NTL, true>(acc, f1); c8_sched<NTL, false>(); SB(); SIDE1; SB(); st = stn; } while (0)
#pragma unroll 1
  for (int kt = 0; kt < 32; ++kt) C5_ITER((void)0, (void)0);
  TS(5, 2);
  __syncthreads();
#pragma unroll
  for (int m = 0; m < 2; ++m) {
    const int t = 32 * wr + 16 * m + fr;
#pragma unroll
    for (int n = 0; n < NTL; ++n) {
      const int f = 16 * (nb0 + n) + 4 * fq; const unsigned z0 = z1v[4 * n + 2 * m], z1 = z1v[4 * n + 2 * m + 1];
      u32x2 o; o[0] = pk2(bflo(z0) * (acc[n][m][0] + bb[m]), bfhi(z0) * (acc[n][m][1] + bb[m]));
      o[1] = pk2(bflo(z1) * (acc[n][m][2] + bb[m]), bfhi(z1) * (acc[n][m][3] + bb[m]));
      *(u32x2*)(lds + t * EP_PITCH + f * 2) = o;
    }
  }
}

DI void p4_copyout(char* ws, char* lds, int w, int pi, int ft) {
  __syncthreads();
  const size_t dst = WS_Z2T + ((size_t)ft * 32 + pi * 2) * 18432;
  for (int i = w * 64 + lane_id(); i < 2304; i += 768) st16_wt(ws, dst + i * 16, *(const u32x4*)(lds + i * 16));
  WAITV(0); __syncthreads();
  if (threadIdx.x == 0) flag_add(ws, F_Z + ft);
  TS(4, 3); TS(4, 7);
}
DI void p5_copyout(char* ws, char* lds, int w, int tt, int ft) {
  __syncthreads();
  const size_t ub = WS_U + ((size_t)tt * 128 * FH + ft * 144) * 2;
  for (int i = w * 64 + lane_id(); i < 128 * 18; i += 768) { const int t = i / 18, c = i - t * 18; st16_wt(ws, ub + (size_t)t * FH * 2 + c * 16, *(const u32x4*)(lds + t * EP_PITCH + c * 16)); }
  WAITV(0); __syncthreads();
  if (threadIdx.x == 0) flag_add(ws, F_U + tt);
  TS(5, 3); TS(5, 7);
}
DI void p45(const u16* __restrict__ wsp, const u16* __restrict__ bsp, char* __restrict__ ws, char* lds) {
  const int lane = lane_id(); const int w = __builtin_amdgcn_readfirstlane(threadIdx.x >> 6); const int tid = w * 64 + lane;
  const int b = blockIdx.x, xc = b & 7, idx = b >> 3;
  const int pi = (xc & 1) * 8 + (idx & 7), ft = (xc >> 1) * 4 + (idx >> 3);
  float* tab = (float*)(lds + K4_TAB);
  TS(4, 0); TS(4, 6);
  const char* Xb = ws + WS_H + (size_t)pi * 128 * D * 2; const char* W2b = ws + WS_WIN + (size_t)ft * 9 * 18432; const char* W1b = W2b + (size_t)16 * 9 * 18432;
  auto src = [&](int p) { return p < 16 ? Xb + tn_off(p * 8 + (lane >> 3), lane, D * 2) : (p < 34 ? W2b + (p - 16) * 1024 : W1b + (p - 34) * 1024) + lane * 16; };
  if (b >= 192) { if (w == 11) flag_wait(ws, F_W, 1024); __syncthreads(); }
#pragma unroll
  for (int j = 0; j < 3; ++j) glds16a<16>(src(16 + w + 12 * j), lds + (16 + w + 12 * j) * 1024);
  if (w == 11) { flag_wait(ws, F_H + pi, 12); flag_wait(ws, F_CS, 64); }
  __syncthreads();
  glds16a<16>(src(w), lds + w * 1024);
  if (w < 4) glds16a<16>(src(12 + w), lds + (12 + w) * 1024);
  const char* Xb5 = (const char*)(wsp + (size_t)pi * 128 * S); const char* Wb5 = ws + WS_Z2T + (size_t)ft * 32 * 18432;
  auto src5 = [&](int p) { return p < 16 ? Xb5 + tn_off(p * 8 + (lane >> 3), lane, S * 2) : Wb5 + (p - 16) * 1024 + lane * 16; };
  if (w >= 8) {
    const int lw = w - 8;
    __builtin_amdgcn_s_setprio(2);
    {
      const char* base[13];
#pragma unroll
      for (int j = 0; j < 13; ++j) base[j] = src(lw + 4 * j);
      auto issue = [&](int kt, int stage) {
#pragma unroll
        for (int j = 0; j < 13; ++j) glds16a<16>(base[j] + (j < 4 ? (long)kt * 128 : (long)kt * 18432), lds + stage * K4_STG + (lw + 4 * j) * 1024);
      };
      issue(1, 1);
      int stg = 2;
#pragma unroll 1
      for (int kt = 0; kt < 7; ++kt) { WAITV(13); BAR(); BAR(); issue(kt + 2, stg); stg = stg == 2 ? 0 : stg + 1; }
      WAITV(13); BAR(); BAR();
      WAITV(0); BAR(); BAR();
      BAR();
      BAR();
      const int row = 32 * lw + (lane >> 1);
      const __amdgpu_buffer_rsrc_t rs_ = __builtin_amdgcn_make_buffer_rsrc((void*)(ws + WS_XS), 0, 2048 * 16 * 16, 0x00020000);
      const unsigned vo = (unsigned)(((pi * 16 + (lane & 1) * 8) * 128 + row) * 16);
      float sum = 0.f, ssq = 0.f;
      for (int spin = 0; spin < (1 << 20); ++spin) {
        bool ok = true; sum = 0.f; ssq = 0.f;
        u32x4 g[8];
#pragma unroll
        for (int t = 0; t < 8; ++t) g[t] = __builtin_amdgcn_raw_buffer_load_b128(rs_, vo + t * 2048, 0, 16);
#pragma unroll
        for (int t = 0; t < 8; ++t) { ok &= g[t][1] == XTAG && g[t][3] == XTAG; sum += __uint_as_float(g[t][0]); ssq += __uint_as_float(g[t][2]); }
        if (__all(ok)) break;
        __builtin_amdgcn_s_sleep(8);
      }
      sum += __shfl_xor(sum, 1); ssq += __shfl_xor(ssq, 1);
      const float mean = sum * (1.f / FH), var = fmaxf(ssq * (1.f / FH) - mean * mean, 0.f), rstd = rsqrtf(var + 1e-5f);
      if ((lane & 1) == 0) *(f32x2_t*)(lds + K4_ST2 + row * 8) = f32x2_t{mean * rstd, rstd};
      __syncthreads();
    }
    p4_copyout(ws, lds, w, pi, ft);
    TS(5, 0); TS(5, 6);
    glds16(src5(w), lds + w * 1024); if (w < 4) glds16(src5(12 + w), lds + (12 + w) * 1024);
    if (w == 11) flag_wait(ws, F_Z + ft, 16);
    __syncthreads();
    glds16a<16>(src5(16 + w), lds + (16 + w) * 1024); if (w < 6) glds16a<16>(src5(28 + w), lds + (28 + w) * 1024);
    loader_loop<32, C8_NS, C8_STG, 34, 16, 4, 0, 16>(lds, lw, src5, [&](int kt, long& ox, long& ow) { ox = (long)kt * 128; ow = (long)kt * 18432; }, NoPost());
    __builtin_amdgcn_s_setprio(0);
    __syncthreads();
    p5_copyout(ws, lds, w, pi, ft);
  } else {
    if (tid < 128) {
      const char* p = ws + WS_ST2 + (size_t)(pi * 128 + tid) * 96;
      float sum = 0.f, ssq = 0.f;
#pragma unroll
      for (int t = 0; t < 12; ++t) { const u32x2 v = ld8_c(p + t * 8); sum += __uint_as_float(v[0]); ssq += __uint_as_float(v[1]); }
      const float mean = sum * (1.f / D), var = fmaxf(ssq * (1.f / D) - mean * mean, 0.f), rstd = rsqrtf(var + 1e-5f);
      tab[tid] = rstd; tab[128 + tid] = mean * rstd;
    } else if (tid < 416) {
      const int c = tid - 128;
      tab[256 + c] = ld4f_c((const float*)(ws + WS_CS) + (c < 144 ? FH + ft * 144 + c : ft * 144 + c - 144));
    }
#define P45_PATH(NTL) do { unsigned z1v[4 * NTL]; \
      k4_compute<NTL>(lds, lane, w, pi, ft, ws, z1v); \
      p4_copyout(ws, lds, w, pi, ft); \
      TS(5, 0); TS(5, 6); \
      glds16(src5(w), lds + w * 1024); if (w < 4) glds16(src5(12 + w), lds + (12 + w) * 1024); \
      __syncthreads(); \
      glds16a<16>(src5(16 + w), lds + (16 + w) * 1024); if (w < 6) glds16a<16>(src5(28 + w), lds + (28 + w) * 1024); \
      k5_compute<NTL>(lds, lane_id(), w, pi, ft, bsp, ws, z1v); \
      p5_copyout(ws, lds, w, pi, ft); } while (0)
    if (w < 4) P45_PATH(5); else P45_PATH(4);
  }
}

__global__ __launch_bounds__(768, 3) void mega_kernel(const u16* __restrict__ x, const u16* __restrict__ wo, const u16* __restrict__ w_in, const u16* __restrict__ w_sp,
                                                      const u16* __restrict__ b_sp, const u16* __restrict__ w_out, u16* __restrict__ out, char* __restrict__ ws) {
  extern __shared__ __attribute__((aligned(16))) char lds[];
  const int b = blockIdx.x;
  {
    unsigned long long pc; unsigned len;
    asm volatile("s_getpc_b64 %0\n\ts_mov_b32 %1, mega_code_end-.+4" : "=s"(pc), "=s"(len));
    if (b < 8 && (threadIdx.x >> 6) == 11) {
      unsigned acc = 0;
      for (unsigned o = (threadIdx.x & 63) * 128; o < len && o < (256u << 10); o += 64 * 128) acc ^= *(const volatile unsigned*)((const char*)pc + o);
      if (acc == 0x2468ace1u) ((unsigned*)(ws + WS_DBG))[2] = acc;
    }
  }
  p2_attention(wo, w_in, w_out, w_sp, ws, lds);
  if (b < 192) g_body<768, true, 3>(ws, lds, (const u16*)(ws + WS_O), ws + WS_WO, x, (u16*)(ws + WS_H), (float*)(ws + WS_ST2), b);
  else p3_colsum(w_in, ws, lds, b);
  __syncthreads();
  p45(w_sp, b_sp, ws, lds);
  if (b < 192) g_body<FH, false, 0>(ws, lds, (const u16*)(ws + WS_U), ws + WS_WOUT, (const u16*)(ws + WS_H), out, nullptr, b);
  asm volatile("mega_code_end:");
}

extern "C" void kernel_launch(void* const* d_in, const int* in_sizes, int n_in, void* d_out, int out_size, void* d_ws, size_t ws_size, hipStream_t stream) {
  const u16* x = (const u16*)d_in[0]; const u16* wqkv = (const u16*)d_in[1]; const u16* wo = (const u16*)d_in[2];
  const u16* w_in = (const u16*)d_in[4]; const u16* w_sp = (const u16*)d_in[5]; const u16* b_sp = (const u16*)d_in[6]; const u16* w_out = (const u16*)d_in[7];
  u16* out = (u16*)d_out; char* ws = (char*)d_ws;
  static bool attr = false;
  if (!attr) {
    (void)hipFuncSetAttribute((const void*)k1_kernel, hipFuncAttributeMaxDynamicSharedMemorySize, K1_LDS);
    (void)hipFuncSetAttribute((const void*)mega_kernel, hipFuncAttributeMaxDynamicSharedMemorySize, ATT_LDS);
    attr = true;
  }
  k1_kernel<<<256, K1_THREADS, K1_LDS, stream>>>(x, wqkv, ws);
  mega_kernel<<<256, 768, ATT_LDS, stream>>>(x, wo, w_in, w_sp, b_sp, w_out, out, ws);
}
```

```cpp
#include <hip/hip_runtime.h>
#include <hip/hip_bf16.h>
#include <stdint.h>
typedef unsigned short u16;
using bf16x8 = __attribute__((ext_vector_type(8))) short;
using s16x4 = __attribute__((ext_vector_type(4))) short;
using f32x4 = __attribute__((ext_vector_type(4))) float;
using f32x16 = __attribute__((ext_vector_type(16))) float;
using u32x4 = __attribute__((ext_vector_type(4))) unsigned;
using u32x2 = __attribute__((ext_vector_type(2))) unsigned;
using u32x32 = __attribute__((ext_vector_type(32))) unsigned;
typedef __bf16 bf16x2_t __attribute__((ext_vector_type(2)));
typedef float f32x2_t __attribute__((ext_vector_type(2)));
constexpr int S = 2048, D = 576, H = 32, DH = 18, F = 4608, FH = 2304;
#define DI __device__ __forceinline__
#define LAS __attribute__((address_space(3)))
#define GAS __attribute__((address_space(1)))
DI int lane_id() { int l = __builtin_amdgcn_mbcnt_hi(~0u, __builtin_amdgcn_mbcnt_lo(~0u, 0u)); asm volatile("" : "+v"(l)); return l; }
DI float bf2f(u16 x) { return __uint_as_float((unsigned)x << 16); }
DI float bflo(unsigned x) { return __uint_as_float(x << 16); }
DI float bfhi(unsigned x) { return __uint_as_float(x & 0xffff0000u); }
DI unsigned pk2(float lo, float hi) { f32x2_t v = {lo, hi}; return __builtin_bit_cast(unsigned, __builtin_convertvector(v, bf16x2_t)); }
#define MFMA32(a, b, c) __builtin_amdgcn_mfma_f32_32x32x16_bf16((a), (b), (c), 0, 0, 0)
#define MFMA16(a, b, c) __builtin_amdgcn_mfma_f32_16x16x32_bf16((a), (b), (c), 0, 0, 0)
#define SB() __builtin_amdgcn_sched_barrier(0)
#define SGB(mask, n) __builtin_amdgcn_sched_group_barrier(mask, n, 0)
#define PIN(x) asm volatile("" : "+v"(x))
#define BAR() __builtin_amdgcn_s_barrier()
#define WAITV(n) asm volatile("s_waitcnt vmcnt(%0)" :: "i"(n) : "memory")
#define WAITL0() asm volatile("s_waitcnt lgkmcnt(0)" ::: "memory")
DI void glds16(const void* g, void* l) { __builtin_amdgcn_global_load_lds((const GAS unsigned*)g, (LAS unsigned*)l, 16, 0, 0); }
template <int AUX> DI void glds16a(const void* g, void* l) { __builtin_amdgcn_global_load_lds((const GAS unsigned*)g, (LAS unsigned*)l, 16, 0, AUX); }
DI void st16_wt(char* ws, size_t off, u32x4 v) {
  const __amdgpu_buffer_rsrc_t r = __builtin_amdgcn_make_buffer_rsrc((void*)ws, 0, 128 << 20, 0x00020000);
  __builtin_amdgcn_raw_buffer_store_b128(v, r, (unsigned)off, 0, 16);
}
typedef LAS const char* lds_cptr;
typedef LAS char* lds_ptr;
#define LDS_RD128(p) (*(const LAS bf16x8*)(p))
#define LDS_RD32(p) (*(const LAS unsigned*)(p))
typedef short v4i16_t __attribute__((ext_vector_type(4)));
DI s16x4 lds_tr(lds_cptr p) { return __builtin_bit_cast(s16x4, __builtin_amdgcn_ds_read_tr16_b64_v4i16((LAS v4i16_t*)p)); }
DI bf16x8 lds_tr2(lds_cptr p0, lds_cptr p1) { const s16x4 a = lds_tr(p0), b = lds_tr(p1); return __builtin_shufflevector(a, b, 0, 1, 2, 3, 4, 5, 6, 7); }

#define TIMING 0
#define TS(k, i)
#define TR(k, i)
#define TSTAMP(v)
#define RSTAMP(v)

constexpr int KV_TILE = 2400, KV_K1 = 1024, KV_V = 1152, KV_VS = 624, KV_Z = KV_V + 608, KV_HEAD = 64 * KV_TILE;
constexpr int Q_TILE = 1152, Q_HEAD = 64 * Q_TILE;
constexpr size_t WS_KV = 0;
constexpr size_t WS_Q = 5u << 20;
constexpr size_t WS_O = 8u << 20;
constexpr size_t WS_H = 11u << 20;
constexpr size_t WS_ST2 = 14u << 20;
constexpr size_t WS_CS = 15u << 20;
constexpr size_t WS_XS = 16u << 20;
constexpr size_t WS_FLAG = WS_XS + (512u << 10);
constexpr size_t WS_Z2T = 17u << 20;
constexpr size_t WS_Z1 = 27u << 20;
constexpr size_t WS_U = 37u << 20;
constexpr size_t WS_WIN = 47u << 20;
constexpr size_t WS_WO = 53u << 20;
constexpr size_t WS_WOUT = 54u << 20;
constexpr size_t WS_DBG = 64u << 20;

constexpr int F_O = 0, F_W = 8, F_H = 9, F_CS = 25, F_Z = 26, F_U = 42;
typedef GAS unsigned gu32;
DI gu32* flagp(char* ws, int i) { return (gu32*)(ws + WS_FLAG + (size_t)i * 256); }
DI void flag_add(char* ws, int i) { __hip_atomic_fetch_add(flagp(ws, i), 1u, __ATOMIC_RELAXED, __HIP_MEMORY_SCOPE_AGENT); }
DI void flag_wait(char* ws, int i, unsigned target) {
  gu32* p = flagp(ws, i);
  for (int spin = 0; spin < (1 << 22); ++spin) {
    if ((unsigned)__builtin_amdgcn_readfirstlane(__hip_atomic_load(p, __ATOMIC_RELAXED, __HIP_MEMORY_SCOPE_AGENT)) >= target) break;
    __builtin_amdgcn_s_sleep(2);
  }
}
DI void st4_wt(void* p, unsigned v) { __hip_atomic_store((gu32*)p, v, __ATOMIC_RELAXED, __HIP_MEMORY_SCOPE_AGENT); }
DI void st8_wt(void* p, u32x2 v) { __hip_atomic_store((GAS unsigned long long*)p, ((unsigned long long)v[1] << 32) | v[0], __ATOMIC_RELAXED, __HIP_MEMORY_SCOPE_AGENT); }
DI u32x2 ld8_c(const void* p) { const unsigned long long v = __hip_atomic_load((GAS unsigned long long*)p, __ATOMIC_RELAXED, __HIP_MEMORY_SCOPE_AGENT); return u32x2{(unsigned)v, (unsigned)(v >> 32)}; }
DI float ld4f_c(const void* p) { return __uint_as_float(__hip_atomic_load((gu32*)p, __ATOMIC_RELAXED, __HIP_MEMORY_SCOPE_AGENT)); }
template <int NP, int NW, int NPX = 0, int AX = 0, int AW = 0, class SRC>
DI void first_tile(char* lds, int w, SRC src) {
#pragma unroll
  for (int j = 0; j < (NP + NW - 1) / NW; ++j) { const int p = w + NW * j; if (p < NP) { if (p < NPX) glds16a<AX>(src(p), lds + p * 1024); else glds16a<AW>(src(p), lds + p * 1024); } }
}
template <int NT, int NS, int STG, int NP, int NPX, int NLD, int AX = 0, int AW = 0, class SRC, class OFF, class POST>
DI void loader_loop(char* lds, int lw, SRC src, OFF off, POST post) {
  constexpr int PPW = (NP + NLD - 1) / NLD, PMIN = NP / NLD;
  static_assert((NS - 1) * PPW + 4 <= 63, "vmcnt");
  static_assert(NPX % NLD == 0, "X pieces");
  const char* base[PPW];
#pragma unroll
  for (int j = 0; j < PPW; ++j) { const int p = lw + NLD * j; base[j] = src(p < NP ? p : NP - 1); }
  auto issue = [&](int kt, int stage) {
    long ox, ow; off(kt, ox, ow);
#pragma unroll
    for (int j = 0; j < PPW; ++j) if (j < PMIN || lw + NLD * j < NP) {
      if (j < NPX / NLD) glds16a<AX>(base[j] + ox, lds + stage * STG + (lw + NLD * j) * 1024); else glds16a<AW>(base[j] + ow, lds + stage * STG + (lw + NLD * j) * 1024); }
  };
  issue(1, 1);
  WAITV(PMIN);
  BAR();
#pragma unroll
  for (int t = 2; t < NS - 1; ++t) issue(t, t);
  post(0);
  int stg = NS - 1, pst = 1;
#pragma unroll 1
  for (int kt = 0; kt < NT - (NS - 1); ++kt) {
    WAITV((NS - 3) * PMIN);
    BAR();
    issue(kt + NS - 1, stg); stg = stg + 1 == NS ? 0 : stg + 1;
    post(pst); pst = pst + 1 == NS ? 0 : pst + 1;
  }
#pragma unroll
  for (int r = NS - 2; r >= 0; --r) {
    WAITV((r - 1 > 0 ? r - 1 : 0) * PMIN); BAR();
    if (r > 0) { post(pst); pst = pst + 1 == NS ? 0 : pst + 1; }
  }
}
struct NoPost { DI void operator()(int) const {} };
DI unsigned tn_off(int row, int lane, int ld_bytes) { return (unsigned)row * ld_bytes + ((((lane & 7) ^ ((row >> 1) & 7))) << 4); }
template <int BN> DI unsigned kn_off(int p, int lane, int ld_bytes) {
  constexpr int CPR = BN / 8;
  const int c = 64 * p + lane, kp = c / CPR, cc = c - kp * CPR, odd = (kp >> 3) & 1, k = kp ^ (odd << 2);
  int cs = cc;
  if (BN == 96) { int blk = (cc >> 1) - odd; if (blk < 0) blk += 6; cs = blk * 2 + (cc & 1); }
  return (unsigned)k * ld_bytes + (unsigned)cs * 16;
}

DI float gelu_t(float x) {
  const float c1 = -2.f * 0.7978845608028654f * 1.4426950408889634f, c2 = c1 * 0.044715f;
  const float e = __builtin_amdgcn_exp2f(x * __builtin_fmaf(x * x, c2, c1));
  return x * __builtin_amdgcn_rcpf(1.f + e);
}

constexpr int C8_STG = 272 * 128, C8_W = 16384, C8_NS = 4, C8_LDS = C8_NS * C8_STG;
struct C8Addr { lds_cptr x0, x1, w0, w1; };
template <bool BKN> DI C8Addr c8_addr(char* lds, int lane, int wr, int nb0) {
  lds_cptr L = (lds_cptr)lds; const int fr = lane & 15, fq = lane >> 4, sw = (fr >> 1) & 7; C8Addr a;
  a.x0 = L + (32 * wr + fr) * 128 + ((fq ^ sw) << 4); a.x1 = L + (32 * wr + fr) * 128 + (((4 + fq) ^ sw) << 4);
  if (BKN) { const int q = fr >> 2, p4 = lane & 3, b = C8_W + (8 * fq + q) * 288 + nb0 * 32 + p4 * 8;
             a.w0 = L + b + (fq & 1) * 1152; a.w1 = L + b + ((fq & 1) ^ 1) * 1152; }
  else { a.w0 = L + C8_W + (16 * nb0 + fr) * 128 + ((fq ^ sw) << 4); a.w1 = L + C8_W + (16 * nb0 + fr) * 128 + (((4 + fq) ^ sw) << 4); }
  return a;
}
template <int NTL> struct C8Frag { bf16x8 x[2], w[NTL]; };
template <int NTL, bool BKN> DI void c8_read(C8Frag<NTL>& f, const C8Addr& a, int st, int ks) {
  f.x[0] = LDS_RD128((ks ? a.x1 : a.x0) + st); f.x[1] = LDS_RD128((ks ? a.x1 : a.x0) + st + 2048);
#pragma unroll
  for (int n = 0; n < NTL; ++n) {
    if (BKN) f.w[n] = lds_tr2(a.w0 + st + ks * 9216 + n * 32, a.w1 + st + ks * 9216 + n * 32);
    else f.w[n] = LDS_RD128((ks ? a.w1 : a.w0) + st + n * 2048);
  }
}
template <int NTL, bool WX> DI void c8_mma(f32x4 (&acc)[NTL][2], const C8Frag<NTL>& f) {
#pragma unroll
  for (int n = 0; n < NTL; ++n) {
#pragma unroll
    for (int m = 0; m < 2; ++m) acc[n][m] = WX ? MFMA16(f.w[n], f.x[m], acc[n][m]) : MFMA16(f.x[m], f.w[n], acc[n][m]);
  }
}
template <int NTL, bool BKN> DI void c8_sched() {
  constexpr int NR = 2 + (BKN ? 2 * NTL : NTL), NM = 2 * NTL;
  if (NR <= NM) {
#pragma unroll
    for (int i = 0; i < NR; ++i) { SGB(0x8, 1); SGB(0x100, 1); }
    SGB(0x8, NM - NR);
  } else {
#pragma unroll
    for (int i = 0; i < NM; ++i) { SGB(0x8, 1); SGB(0x100, 1); if (i < NR - NM) SGB(0x100, 1); }
  }
}

constexpr int K1_THREADS = 768;
constexpr int EP_PITCH = 304;
template <int WC, bool WX>
DI void k1_compute(char* lds, int lane, int w, f32x4 (&acc)[WC == 0 ? 3 : 2][4], float* tabs, char* ws) {
  constexpr int NTL = WC == 0 ? 3 : 2, NB0 = WC == 0 ? 0 : 2 * WC + 1, NST = WC == 0 ? 0 : (WC == 1 ? 2 : 1), M0 = WC == 1 ? 0 : WC - 1, M1 = 3;
  const int wr = w & 1, fr = lane & 15, fq = lane >> 4, sw = (fr >> 1) & 7;
  lds_cptr L = (lds_cptr)lds;
  lds_cptr x0 = L + (64 * wr + fr) * 128 + ((fq ^ sw) << 4), x1 = L + (64 * wr + fr) * 128 + (((4 + fq) ^ sw) << 4);
  const int wb = C8_W + (8 * fq + (fr >> 2)) * 288 + NB0 * 32 + (lane & 3) * 8;
  lds_cptr w0 = L + wb + (fq & 1) * 1152, w1 = L + wb + ((fq & 1) ^ 1) * 1152;
  const u32x4 onesv = {0x3f803f80u, 0x3f803f80u, 0x3f803f80u, 0x3f803f80u}; const bf16x8 ones = __builtin_bit_cast(bf16x8, onesv);
  bf16x8 xa[4], wa[NTL], xb[4], wb_[NTL];
  f32x4 cs[NTL] = {}, rsum[2] = {}, rsq[2] = {};
#define K1RD(X, Wf, st, ks) do { _Pragma("unroll") for (int n = 0; n < NTL; ++n) Wf[n] = lds_tr2(w0 + (st) + (ks) * 9216 + n * 32, w1 + (st) + (ks) * 9216 + n * 32); \
    _Pragma("unroll") for (int m = 0; m < 4; ++m) X[m] = LDS_RD128(((ks) ? x1 : x0) + (st) + m * 2048); } while (0)
#define K1MM(X, Wf) do { _Pragma("unroll") for (int n = 0; n < NTL; ++n) { _Pragma("unroll") for (int m = 0; m < 4; ++m) acc[n][m] = WX ? MFMA16(Wf[n], X[m], acc[n][m]) : MFMA16(X[m], Wf[n], acc[n][m]); \
    cs[n] = MFMA16(ones, Wf[n], cs[n]); } \
    if (NST >= 1) { rsum[0] = MFMA16(X[M0], ones, rsum[0]); rsq[0] = MFMA16(X[M0], X[M0], rsq[0]); } \
    if (NST >= 2) { rsum[1] = MFMA16(X[M1], ones, rsum[1]); rsq[1] = MFMA16(X[M1], X[M1], rsq[1]); } } while (0)
#define K1SCHED() do { _Pragma("unroll") for (int i_ = 0; i_ < 2 * NTL + 4; ++i_) { SGB(0x8, 1); SGB(0x100, 1); } SGB(0x8, 5 * NTL + 2 * NST - (2 * NTL + 4)); } while (0)
  WAITV(0); BAR();
  TS(1, 1);
  K1RD(xa, wa, 0, 0);
  int st = 0;
#pragma unroll 1
  for (int kt = 0; kt < 9; ++kt) {
    const int stn = st + C8_STG == C8_LDS ? 0 : st + C8_STG;
    K1RD(xb, wb_, st, 1); K1MM(xa, wa); K1SCHED();
    SB(); BAR(); SB();
    K1RD(xa, wa, stn, 0); K1MM(xb, wb_); K1SCHED();
    SB();
    st = stn;
  }
  TS(1, 2);
  const int l2 = lane_id(), r2 = l2 & 15, q2 = l2 >> 4;
  if (l2 < 16 && wr == 0) {
#pragma unroll
    for (int n = 0; n < NTL; ++n) tabs[256 + 16 * (NB0 + n) + l2] = cs[n][0];
  }
#pragma unroll
  for (int t = 0; t < NST; ++t) {
    const int mrow = 64 * wr + 16 * (t == 0 ? M0 : M1);
    if (r2 == 0) *(f32x4*)(tabs + mrow + 4 * q2) = rsum[t];
    if (q2 == (r2 >> 2)) tabs[128 + mrow + r2] = (r2 & 2) ? ((r2 & 1) ? rsq[t][3] : rsq[t][2]) : ((r2 & 1) ? rsq[t][1] : rsq[t][0]);
  }
}
template <int NTL>
DI void k1_epilogue(char* lds, int lane, int w, int type, int rt, int hg, const float* tabs, f32x4 (&acc)[NTL][4], char* kvimg) {
  asm volatile("" : "+v"(lane));
  const int wr = w & 1, wc = w >> 1, nb0 = wc == 0 ? 0 : 2 * wc + 1, fr = lane & 15, fq = lane >> 4;
  const float* rs = tabs; const float* mr = tabs + 128; const float* cs = tabs + 256;
  if (type == 2) {
#pragma unroll
    for (int n = 0; n < NTL; ++n) {
      const int col = 16 * (nb0 + n) + fr, hh = col / 18, d = col - hh * 18; const float c = cs[col];
      char* hb = kvimg + (size_t)(hg * 8 + hh) * KV_HEAD + (size_t)(rt * 4 + 2 * wr) * KV_TILE + KV_V + (fq & 1) * 304 + (fq >> 1) * 8 + d * 16;
#pragma unroll
      for (int m = 0; m < 4; ++m) {
        const int row = 64 * wr + 16 * m + 4 * fq;
        const f32x4 r4 = *(const f32x4*)(rs + row), m4 = *(const f32x4*)(mr + row);
        u32x2 o; o[0] = pk2(acc[n][m][0] * r4[0] - m4[0] * c, acc[n][m][1] * r4[1] - m4[1] * c); o[1] = pk2(acc[n][m][2] * r4[2] - m4[2] * c, acc[n][m][3] * r4[3] - m4[3] * c);
        *(u32x2*)(hb + (m >> 1) * KV_TILE + (m & 1) * KV_VS) = o;
      }
    }
  } else {
    const float qs = type == 0 ? 0.23570226039551584f * 1.4426950408889634f : 1.f;
#pragma unroll
    for (int m = 0; m < 4; ++m) {
      const int row = 64 * wr + 16 * m + fr; const float r = rs[row] * qs, mm = mr[row] * qs;
#pragma unroll
      for (int n = 0; n < NTL; ++n) {
        const int col = 16 * (nb0 + n) + 4 * fq; const f32x4 c4 = *(const f32x4*)(cs + col);
        u32x2 o; o[0] = pk2(acc[n][m][0] * r - mm * c4[0], acc[n][m][1] * r - mm * c4[1]); o[1] = pk2(acc[n][m][2] * r - mm * c4[2], acc[n][m][3] * r - mm * c4[3]);
        *(u32x2*)(lds + row * EP_PITCH + col * 2) = o;
      }
    }
  }
}
__global__ __launch_bounds__(K1_THREADS, 3) void k1_kernel(const u16* __restrict__ x, const u16* __restrict__ wqkv, char* __restrict__ ws) {
  extern __shared__ __attribute__((aligned(16))) char lds[];
  const int tid = threadIdx.x, lane = tid & 63; const int w = __builtin_amdgcn_readfirstlane(tid >> 6);
  const int b = blockIdx.x;
  if (blockIdx.x == 0 && threadIdx.x == 0) ((volatile long long*)(ws + WS_DBG + 4096))[0] = __builtin_amdgcn_s_memrealtime();
  TS(1, 0); TS(1, 6);
  if (b >= 192) {
    if (tid < 512) ((u32x4*)(ws + WS_XS))[(b - 192) * 512 + tid] = u32x4{0, 0, 0, 0};
    else if (tid < 528) ((u32x4*)(ws + WS_FLAG))[(b - 192) * 16 + tid - 512] = u32x4{0, 0, 0, 0};
    return;
  }
  const int xc = b & 7, idx = b >> 3;
  const int rt = xc * 2 + (idx & 1), ct = idx >> 1;
  const int type = ct >> 2, hg = ct & 3;
  float* tabs = (float*)(lds + C8_LDS);
  char* kvimg = ws + WS_KV; char* qimg = ws + WS_Q;
  const char* Xb = (const char*)(x + (size_t)rt * 128 * D); const char* Wb = (const char*)(wqkv + ct * 144);
  auto src = [&](int p) { return p < 16 ? Xb + tn_off(p * 8 + (lane >> 3), lane, D * 2) : Wb + kn_off<144>(p - 16, lane, 1728 * 2); };
  first_tile<34, 12>(lds, w, src);
  if (w >= 8) {
    __builtin_amdgcn_s_setprio(2);
    loader_loop<9, C8_NS, C8_STG, 34, 16, 4>(lds, w - 8, src, [&](int kt, long& ox, long& ow) { ox = (long)kt * 128; ow = (long)kt * 64 * 1728 * 2; }, NoPost());
    __syncthreads();
    const int row = (w - 8) * 64 + lane_id();
    if (row < 128) { const float mean = tabs[row] * (1.f / D), var = fmaxf(tabs[128 + row] * (1.f / D) - mean * mean, 0.f), rstd = rsqrtf(var + 1e-5f); tabs[row] = rstd; tabs[128 + row] = mean * rstd; }
    __syncthreads();
  } else {
#define K1_PATH(WC, WX) do { f32x4 acc[WC == 0 ? 3 : 2][4] = {}; \
      k1_compute<WC, WX>(lds, lane, w, acc, tabs, ws); \
      __syncthreads(); __syncthreads(); \
      k1_epilogue<WC == 0 ? 3 : 2>(lds, lane_id(), w, WX ? (type & 1) : 2, rt, hg, tabs, acc, kvimg); } while (0)
#define K1_SEL(WX) do { if (w < 2) K1_PATH(0, WX); else if (w < 4) K1_PATH(1, WX); else if (w < 6) K1_PATH(2, WX); else K1_PATH(3, WX); } while (0)
    if (type == 2) K1_SEL(false); else K1_SEL(true);
  }
  const int tid2 = w * 64 + lane_id();
  if (type == 2) {
    if (tid2 < 192) {
      const int tl = tid2 / 48, r = tid2 - tl * 48, hh = r / 6, c = r - hh * 6;
      char* tb = kvimg + (size_t)(hg * 8 + hh) * KV_HEAD + (size_t)(rt * 4 + tl) * KV_TILE;
      if (c < 4) *(u32x4*)(tb + KV_V + (c >> 1) * KV_VS + (c & 1) * 304 + 18 * 16) = u32x4{0x3f803f80u, 0x3f803f80u, 0x3f803f80u, 0x3f803f80u};
      else *(u32x4*)(tb + KV_Z + (c - 4) * KV_VS) = u32x4{0, 0, 0, 0};
    }
    TS(1, 3); TS(1, 7);
    return;
  }
  __syncthreads();
  for (int it = tid2; it < 1024; it += K1_THREADS) {
    const int tok = it & 127, hh = it >> 7;
    const unsigned* sp = (const unsigned*)(lds + tok * EP_PITCH + hh * 36);
    u32x4 c0 = {sp[0], sp[1], sp[2], sp[3]}, c1 = {sp[4], sp[5], sp[6], sp[7]}; const unsigned c2 = sp[8];
    char* tb = type == 0 ? qimg + (size_t)(hg * 8 + hh) * Q_HEAD + (size_t)(rt * 4 + (tok >> 5)) * Q_TILE
                         : kvimg + (size_t)(hg * 8 + hh) * KV_HEAD + (size_t)(rt * 4 + (tok >> 5)) * KV_TILE;
    const int key = tok & 31;
    *(u32x4*)(tb + key * 16) = c0; *(u32x4*)(tb + 512 + key * 16) = c1; *(unsigned*)(tb + 1024 + key * 4) = c2;
  }
  TS(1, 3); TS(1, 7);
}
static_assert(128 * EP_PITCH <= C8_LDS, "");
constexpr int K1_LDS = C8_LDS + 400 * 4;

constexpr int ATT_LDS = KV_HEAD + 8 * Q_TILE;
#define SWITCH 50
DI void p2_attention(const u16* __restrict__ wo, const u16* __restrict__ w_in, const u16* __restrict__ w_out, const u16* __restrict__ w_sp, char* __restrict__ ws, char* lds) {
  TS(2, 0); TS(2, 6);
  const char* kvimg = ws + WS_KV; const char* qimg = ws + WS_Q; u16* o = (u16*)(ws + WS_O);
  const int tid = threadIdx.x, lane = tid & 63; const int w = __builtin_amdgcn_readfirstlane(tid >> 6);
  const int b = blockIdx.x, xcd = b & 7, idx = b >> 3, h = xcd * 4 + (idx >> 3), qb = idx & 7;
  const char* kvg = kvimg + (size_t)h * KV_HEAD + lane * 16;
  if (w < 8) {
    const char* qg = qimg + (size_t)h * Q_HEAD + (qb * 8 + w) * Q_TILE + lane * 16;
    char* qtile = lds + KV_HEAD + w * Q_TILE;
    glds16(qg, qtile);
    if (lane < 8) glds16(qg + 1024, qtile + 1024);
  }
  glds16(kvg + w * 1024, lds + w * 1024); glds16(kvg + (w + 12) * 1024, lds + (w + 12) * 1024);
  WAITV(0);
  BAR();
  if (w >= 8) {
    const int lw = w - 8;
    __builtin_amdgcn_s_setprio(3);
#pragma unroll 1
    for (int p = 24 + lw; p < 150; p += 4) glds16(kvg + p * 1024, lds + p * 1024);
    WAITV(0);
    BAR();
    {
      const u32x4* wp = (const u32x4*)w_sp + (size_t)(b * 4 + lw) * 512 + lane;
      u32x4 acc = wp[0];
#pragma unroll
      for (int i = 1; i < 8; ++i) acc ^= wp[i * 64];
      if (acc[0] == 0x12345678u && acc[1] == 0x9abcdef0u && acc[2] == 0x0fedcba9u && acc[3] == 0x87654321u) ((unsigned*)(ws + WS_DBG))[0] = 1;
    }
    const int gw = b * 4 + lw, c = lane & 7, nb = lane >> 3;
    if (nb < 6) {
#pragma unroll 1
      for (int blk = gw; blk < 864 + 144 + 432; blk += 1024) {
        const u16* sp; int ld, kt, n0; size_t db;
        if (blk < 864) { kt = blk / 96; const int nbk = blk - kt * 96, z2 = nbk >= 48, nn = z2 ? nbk - 48 : nbk, t = nn / 3; n0 = nbk * 48; sp = w_in; ld = F;
          db = WS_WIN + ((size_t)((z2 ? 0 : 16) + t) * 9 + kt) * 18432 + (nn - t * 3) * 48 * 128; }
        else if (blk < 1008) { const int q = blk - 864; kt = q / 12; const int nbk = q - kt * 12; n0 = nbk * 48; sp = wo; ld = D; db = WS_WO + ((size_t)(nbk >> 1) * 12 + kt) * 12288 + (nbk & 1) * 48 * 128; }
        else { const int q = blk - 1008; kt = q / 12; const int nbk = q - kt * 12; n0 = nbk * 48; sp = w_out; ld = D; db = WS_WOUT + ((size_t)(nbk >> 1) * 36 + kt) * 12288 + (nbk & 1) * 48 * 128; }
        u32x4 r[8];
        if (blk >= 864 && blk < 1008) {
          const int kc = kt * 8 + c, hh = kc / 3, part = kc - hh * 3;
          const u16* sq = sp + (size_t)(hh * DH) * ld + n0 + nb * 8;
#pragma unroll
          for (int j = 0; j < 8; ++j) {
            const int d = part == 2 ? 16 + j : (j & 3) + 8 * (j >> 2) + 4 * part;
            r[j] = (part == 2 && j >= 2) ? u32x4{0, 0, 0, 0} : *(const u32x4*)(sq + (size_t)(d < DH ? d : 0) * ld);
          }
        } else {
          sp += (size_t)(kt * 64 + c * 8) * ld + n0 + nb * 8;
#pragma unroll
          for (int j = 0; j < 8; ++j) r[j] = *(const u32x4*)(sp + (size_t)j * ld);
        }
        const unsigned lo = (unsigned)db + nb * 1024;
#pragma unroll
        for (int e = 0; e < 4; ++e) {
          const unsigned sl = (unsigned)((c ^ ((4 * nb + e) & 7)) << 4);
#pragma unroll
          for (int hi = 0; hi < 2; ++hi) {
            const unsigned sel = hi ? 0x07060302u : 0x05040100u;
            u32x4 o;
#pragma unroll
            for (int q = 0; q < 4; ++q) o[q] = __builtin_amdgcn_perm(r[2 * q + 1][e], r[2 * q][e], sel);
            st16_wt(ws, lo + (2 * e + hi) * 128 + sl, o);
          }
        }
      }
    }
    WAITV(0);
    if (lane == 0) flag_add(ws, F_W);
    __builtin_amdgcn_s_setprio(0);
  } else {
  TS(2, 1); TR(2, 4);
  if (w >= 4) __builtin_amdgcn_s_setprio(1);
  const int r32 = lane & 31, hh = lane >> 5;
  lds_cptr L = (lds_cptr)lds;
  const bf16x8 qf0 = LDS_RD128(L + KV_HEAD + w * Q_TILE + lane * 16);
  u32x4 q1v = {0, 0, 0, 0}; { unsigned t = LDS_RD32(L + KV_HEAD + w * Q_TILE + 1024 + r32 * 4); q1v[0] = hh ? 0u : t; }
  const bf16x8 qf1 = __builtin_bit_cast(bf16x8, q1v);
  lds_cptr kp0 = L + lane * 16, kp1 = L + (hh ? KV_Z : KV_K1 + r32 * 4), vp = L + (r32 <= 18 ? KV_V + hh * 304 + r32 * 16 : KV_Z);
  f32x16 negm;
  {
    bf16x8 k0 = LDS_RD128(kp0); u32x4 k1 = {LDS_RD32(kp1), 0, 0, 0};
    f32x16 s = {}; s = MFMA32(k0, qf0, s); s = MFMA32(__builtin_bit_cast(bf16x8, k1), qf1, s);
    float m = s[0];
#pragma unroll
    for (int i = 1; i < 16; ++i) m = fmaxf(m, s[i]);
    auto rr = __builtin_amdgcn_permlane32_swap(__float_as_uint(m), __float_as_uint(m), false, false);
    m = fmaxf(__uint_as_float(rr[0]), __uint_as_float(rr[1]));
#pragma unroll
    for (int i = 0; i < 16; ++i) negm[i] = -m;
  }
  f32x16 O = {};
#define EX2(Sx, i) pk2(__builtin_amdgcn_exp2f(Sx[i]), __builtin_amdgcn_exp2f(Sx[(i) + 1]))
  bf16x8 k0, v0, v1; u32x4 k1 = {0, 0, 0, 0};
  f32x16 SA, SBc; u32x4 pA0, pA1, pB0, pB1;
#define STEP(Sc, Sn, Pc0, Pc1, Pp0, Pp1, koff, voff) do { \
    SB(); Sn = MFMA32(k0, qf0, negm); SB(); \
    Pc0[0] = EX2(Sc, 0); Pc0[1] = EX2(Sc, 2); PIN(Pc0); SB(); \
    Sn = MFMA32(__builtin_bit_cast(bf16x8, k1), qf1, Sn); SB(); \
    k0 = LDS_RD128(kp0 + (koff)); k1[0] = LDS_RD32(kp1 + (koff)); SB(); \
    Pc0[2] = EX2(Sc, 4); Pc0[3] = EX2(Sc, 6); PIN(Pc0); SB(); \
    O = MFMA32(v0, __builtin_bit_cast(bf16x8, Pp0), O); SB(); \
    Pc1[0] = EX2(Sc, 8); Pc1[1] = EX2(Sc, 10); PIN(Pc1); SB(); \
    O = MFMA32(v1, __builtin_bit_cast(bf16x8, Pp1), O); SB(); \
    v0 = LDS_RD128(vp + (voff)); v1 = LDS_RD128(vp + (voff) + KV_VS); SB(); \
    Pc1[2] = EX2(Sc, 12); Pc1[3] = EX2(Sc, 14); PIN(Pc1); SB(); \
  } while (0)
  k0 = LDS_RD128(kp0); k1[0] = LDS_RD32(kp1);
  SA = MFMA32(k0, qf0, negm); SA = MFMA32(__builtin_bit_cast(bf16x8, k1), qf1, SA);
  k0 = LDS_RD128(kp0 + KV_TILE); k1[0] = LDS_RD32(kp1 + KV_TILE);
  v0 = LDS_RD128(vp); v1 = LDS_RD128(vp + KV_VS);
  pB0 = u32x4{0, 0, 0, 0}; pB1 = u32x4{0, 0, 0, 0};
  int off = 0;
  for (int t = 0; t < 8; t += 2) {
    STEP(SA, SBc, pA0, pA1, pB0, pB1, off + 2 * KV_TILE, off);
    STEP(SBc, SA, pB0, pB1, pA0, pA1, off + 3 * KV_TILE, off + KV_TILE);
    off += 2 * KV_TILE;
  }
  BAR();
  for (int t = 8; t < 62; t += 2) {
    if (t == SWITCH) __builtin_amdgcn_s_setprio(0);
    STEP(SA, SBc, pA0, pA1, pB0, pB1, off + 2 * KV_TILE, off);
    STEP(SBc, SA, pB0, pB1, pA0, pA1, off + 3 * KV_TILE, off + KV_TILE);
    off += 2 * KV_TILE;
  }
  STEP(SA, SBc, pA0, pA1, pB0, pB1, off + KV_TILE, off);
  STEP(SBc, SA, pB0, pB1, pA0, pA1, off + KV_TILE, off + KV_TILE);
  O = MFMA32(v0, __builtin_bit_cast(bf16x8, pB0), O); O = MFMA32(v1, __builtin_bit_cast(bf16x8, pB1), O);
  TS(2, 2); TR(2, 5);
  {
    float mx = 0.f;
#pragma unroll
    for (int i = 0; i < 11; ++i) mx = fmaxf(mx, fabsf(O[i]));
    bool bad = !(mx < 1.2676506e30f);
#pragma unroll
    for (int i = 0; i < 11; ++i) bad |= (O[i] != O[i]);
    if (__any(bad)) {
      float m = -3.0e38f; O = f32x16{};
#pragma unroll 1
      for (int t = 0; t < 64; ++t) {
        const int of = t * KV_TILE;
        const bf16x8 a0 = LDS_RD128(kp0 + of); const u32x4 a1 = {LDS_RD32(kp1 + of), 0, 0, 0};
        f32x16 s = {}; s = MFMA32(a0, qf0, s); s = MFMA32(__builtin_bit_cast(bf16x8, a1), qf1, s);
        float tm = s[0];
#pragma unroll
        for (int i = 1; i < 16; ++i) tm = fmaxf(tm, s[i]);
        auto rr = __builtin_amdgcn_permlane32_swap(__float_as_uint(tm), __float_as_uint(tm), false, false);
        tm = fmaxf(__uint_as_float(rr[0]), __uint_as_float(rr[1]));
        const float mn = fmaxf(m, tm), al = __builtin_amdgcn_exp2f(m - mn); m = mn;
#pragma unroll
        for (int i = 0; i < 16; ++i) { O[i] *= al; s[i] -= mn; }
        u32x4 p0, p1;
#pragma unroll
        for (int i = 0; i < 4; ++i) { p0[i] = EX2(s, 2 * i); p1[i] = EX2(s, 8 + 2 * i); }
        O = MFMA32(LDS_RD128(vp + of), __builtin_bit_cast(bf16x8, p0), O); O = MFMA32(LDS_RD128(vp + of + KV_VS), __builtin_bit_cast(bf16x8, p1), O);
      }
    }
  }
  {
    float l = O[10];
    { auto rr = __builtin_amdgcn_permlane32_swap(__float_as_uint(l), __float_as_uint(l), false, false); l = __uint_as_float(rr[0]); }
    const float inv = 1.f / l;
    const size_t ob = WS_O + (size_t)(qb * 256 + w * 32 + r32) * 1536 + h * 48;
    const u32x4 c0 = {pk2(O[0] * inv, O[1] * inv), pk2(O[2] * inv, O[3] * inv), pk2(O[4] * inv, O[5] * inv), pk2(O[6] * inv, O[7] * inv)};
    st16_wt(ws, ob + hh * 16, c0);
    if (hh == 0) st16_wt(ws, ob + 32, u32x4{pk2(O[8] * inv, O[9] * inv), 0, 0, 0});
  }
  TS(2, 3);
  }
  WAITV(0); __syncthreads();
  if (tid == 0) flag_add(ws, F_O + qb);
  TS(2, 7);
}

constexpr int G_STG = 160 * 128, G_W = 8192, G_NS = 6, G_LDS = G_NS * G_STG;
template <int K, bool STATS, int KID>
DI void g_body(char* ws, char* lds, const u16* __restrict__ A, const char* __restrict__ W, const u16* __restrict__ res, u16* __restrict__ out, float* __restrict__ st2, int b) {
  constexpr int NT = K / 64;
  const int lane = lane_id(); const int w = __builtin_amdgcn_readfirstlane(threadIdx.x >> 6);
  const int xc = b & 7, idx = b >> 3, rt = xc * 4 + (idx & 3), ct = idx >> 2;
  const int m0 = rt * 64, n0 = ct * 96;
  TS(KID, 0); TS(KID, 6);
  const char* Xb = (const char*)(A + (size_t)m0 * K); const char* Wb = W + (size_t)ct * NT * 12288;
  auto src = [&](int p) { return p < 8 ? Xb + tn_off(p * 8 + (lane >> 3), lane, K * 2) : Wb + (p - 8) * 1024 + lane * 16; };
  if (STATS) { if (w == 11) flag_wait(ws, F_W, 1024); __syncthreads(); }
  glds16a<16>(src(8 + w), lds + (8 + w) * 1024);
  if (w == 11) { if (STATS) flag_wait(ws, F_O + (rt >> 2), 32); else flag_wait(ws, F_U + (rt >> 1), 16); }
  __syncthreads();
  if (w < 8) glds16a<16>(src(w), lds + w * 1024);
  if (w >= 4) {
    __builtin_amdgcn_s_setprio(2);
    loader_loop<NT, G_NS, G_STG, 20, 8, 8, 16, 16>(lds, w - 4, src, [&](int kt, long& ox, long& ow) { ox = (long)kt * 128; ow = (long)kt * 12288; }, NoPost());
    __builtin_amdgcn_s_setprio(0);
  } else {
  const int wr = w & 1, wc = w >> 1, fr = lane & 15, fq = lane >> 4, sw = (fr >> 1) & 7;
  lds_cptr L = (lds_cptr)lds;
  lds_cptr x0 = L + (32 * wr + fr) * 128 + ((fq ^ sw) << 4), x1 = L + (32 * wr + fr) * 128 + (((4 + fq) ^ sw) << 4);
  lds_cptr w0 = L + G_W + (48 * wc + fr) * 128 + ((fq ^ sw) << 4), w1 = L + G_W + (48 * wc + fr) * 128 + (((4 + fq) ^ sw) << 4);
  f32x4 acc[3][2] = {};
  bf16x8 xa[2], wa[3], xb[2], wb_[3];
#define GRD(X, Wf, st, ks) do { X[0] = LDS_RD128(((ks) ? x1 : x0) + (st)); X[1] = LDS_RD128(((ks) ? x1 : x0) + (st) + 2048); \
    _Pragma("unroll") for (int n = 0; n < 3; ++n) Wf[n] = LDS_RD128(((ks) ? w1 : w0) + (st) + n * 2048); } while (0)
#define GSCHED() do { _Pragma("unroll") for (int i_ = 0; i_ < 5; ++i_) { SGB(0x8, 1); SGB(0x100, 1); } SGB(0x8, 1); } while (0)
#define GMM(X, Wf) do { _Pragma("unroll") for (int n = 0; n < 3; ++n) { acc[n][0] = MFMA16(Wf[n], X[0], acc[n][0]); acc[n][1] = MFMA16(Wf[n], X[1], acc[n][1]); } } while (0)
  WAITV(0); BAR();
  TS(KID, 1);
  GRD(xa, wa, 0, 0);
  u32x2 rv[2][3];
#pragma unroll
  for (int m = 0; m < 2; ++m)
#pragma unroll
    for (int n = 0; n < 3; ++n) rv[m][n] = ld8_c(res + (size_t)(m0 + 32 * wr + 16 * m + fr) * D + n0 + 48 * wc + 16 * n + 4 * fq);
  int st = 0;
#pragma unroll 1
  for (int kt = 0; kt < NT; ++kt) {
    const int stn = st + G_STG == G_LDS ? 0 : st + G_STG;
    GRD(xb, wb_, st, 1);
    GMM(xa, wa);
    GSCHED();
    SB(); BAR(); SB();
    GRD(xa, wa, stn, 0);
    GMM(xb, wb_);
    GSCHED();
    SB();
    st = stn;
  }
  TS(KID, 2);
#pragma unroll
  for (int m = 0; m < 2; ++m) {
    const int row = m0 + 32 * wr + 16 * m + fr;
    float sum = 0.f, ssq = 0.f;
#pragma unroll
    for (int n = 0; n < 3; ++n) {
      const int col = n0 + 48 * wc + 16 * n + 4 * fq;
      const u32x2 r = rv[m][n];
      u32x2 o; o[0] = pk2(bflo(r[0]) + acc[n][m][0], bfhi(r[0]) + acc[n][m][1]); o[1] = pk2(bflo(r[1]) + acc[n][m][2], bfhi(r[1]) + acc[n][m][3]);
      if (STATS) st8_wt(out + (size_t)row * D + col, o); else *(u32x2*)(out + (size_t)row * D + col) = o;
      if (STATS) { const float a = bflo(o[0]), bq = bfhi(o[0]), c = bflo(o[1]), d = bfhi(o[1]); sum += (a + bq) + (c + d); ssq += (a * a + bq * bq) + (c * c + d * d); }
    }
    if (STATS) {
      sum += __shfl_xor(sum, 16); sum += __shfl_xor(sum, 32); ssq += __shfl_xor(ssq, 16); ssq += __shfl_xor(ssq, 32);
      if (fq == 0) st8_wt(st2 + ((size_t)row * 12 + ct * 2 + wc) * 2, u32x2{__float_as_uint(sum), __float_as_uint(ssq)});
    }
  }
  TS(KID, 3);
  }
  if (STATS) { WAITV(0); __syncthreads(); if (threadIdx.x == 0) flag_add(ws, F_H + (rt >> 1)); }
  TS(KID, 7);
}
DI void p3_colsum(const u16* __restrict__ w_in, char* __restrict__ ws, char* lds, int b) {
    const int tid = threadIdx.x, c = tid % 9, rg = tid / 9;
    float* red = (float*)lds;
    if (rg < 48) {
      const char* p = (const char*)(w_in + (b - 192) * 72 + c * 8) + (size_t)rg * (F * 2);
      u32x4 v[12];
#pragma unroll
      for (int i = 0; i < 12; ++i) v[i] = *(const u32x4*)(p + (size_t)i * 48 * F * 2);
      float a[8] = {};
#pragma unroll
      for (int i = 0; i < 12; ++i)
#pragma unroll
        for (int e = 0; e < 4; ++e) { a[2 * e] += bflo(v[i][e]); a[2 * e + 1] += bfhi(v[i][e]); }
#pragma unroll
      for (int e = 0; e < 8; ++e) red[rg * 72 + c * 8 + e] = a[e];
    }
    __syncthreads();
    if (tid < 72) { float t = 0.f; for (int g = 0; g < 48; ++g) t += red[g * 72 + tid]; st4_wt((float*)(ws + WS_CS) + (b - 192) * 72 + tid, __float_as_uint(t)); }
    WAITV(0); __syncthreads();
    if (tid == 0) flag_add(ws, F_CS);
}
constexpr unsigned XTAG = 0x9E3779B9u;
DI float dpp_sum16(float v) {
  v += __builtin_bit_cast(float, __builtin_amdgcn_mov_dpp(__builtin_bit_cast(int, v), 0xB1, 0xf, 0xf, true));
  v += __builtin_bit_cast(float, __builtin_amdgcn_mov_dpp(__builtin_bit_cast(int, v), 0x4E, 0xf, 0xf, true));
  v += __builtin_bit_cast(float, __builtin_amdgcn_mov_dpp(__builtin_bit_cast(int, v), 0x141, 0xf, 0xf, true));
  v += __builtin_bit_cast(float, __builtin_amdgcn_mov_dpp(__builtin_bit_cast(int, v), 0x140, 0xf, 0xf, true));
  return v;
}
constexpr int K4_STG = (128 + 288) * 128, K4_W2 = 16384, K4_W1 = 16384 + 18432, K4_NS = 3;
constexpr int K4_TAB = K4_NS * K4_STG;
constexpr int K4_PART = 40960, K4_ST2 = 45056;
constexpr int K4_LDS = K4_TAB + 544 * 4;
template <int NTL>
DI void k4_compute(char* lds, int lane, int w, int pi, int ft, char* ws, unsigned (&z1v)[4 * NTL]) {
  const int wr = w & 3, nb0 = NTL == 5 ? 0 : 5, wc = NTL == 5 ? 0 : 1, fr = lane & 15, fq = lane >> 4, sw = (fr >> 1) & 7;
  lds_cptr L = (lds_cptr)lds;
  lds_cptr x0 = L + (32 * wr + fr) * 128 + ((fq ^ sw) << 4), x1 = L + (32 * wr + fr) * 128 + (((4 + fq) ^ sw) << 4);
  lds_cptr w0 = L + K4_W2 + (16 * nb0 + fr) * 128 + ((fq ^ sw) << 4), w1 = L + K4_W2 + (16 * nb0 + fr) * 128 + (((4 + fq) ^ sw) << 4);
  f32x4 acc2[NTL][2] = {}, acc1[NTL][2] = {};
  bf16x8 xa[2], xb[2], wa[NTL], wb[NTL];
#define RDX(X, P, st) do { X[0] = LDS_RD128(P + (st)); X[1] = LDS_RD128(P + (st) + 2048); } while (0)
#define RDW(Wf, P, st) do { _Pragma("unroll") for (int n = 0; n < NTL; ++n) Wf[n] = LDS_RD128(P + (st) + n * 2048); } while (0)
#define MM2(X, Wf) do { _Pragma("unroll") for (int n = 0; n < NTL; ++n) { acc2[n][0] = MFMA16(X[0], Wf[n], acc2[n][0]); acc2[n][1] = MFMA16(X[1], Wf[n], acc2[n][1]); } } while (0)
#define MM1(X, Wf) do { _Pragma("unroll") for (int n = 0; n < NTL; ++n) { acc1[n][0] = MFMA16(Wf[n], X[0], acc1[n][0]); acc1[n][1] = MFMA16(Wf[n], X[1], acc1[n][1]); } } while (0)
#define SCH(NR) do { _Pragma("unroll") for (int i_ = 0; i_ < (NR); ++i_) { SGB(0x8, 1); SGB(0x100, 1); } SGB(0x8, 2 * NTL - (NR)); SB(); } while (0)
  WAITV(0); WAITL0();
  if (NTL == 4) BAR();
  int st = 0;
#pragma unroll 1
  for (int kt = 0; kt < 9; ++kt) {
    BAR(); SB();
    if (kt == 0) TS(4, 1);
    RDX(xa, x0, st); RDW(wa, w0, st); SB();
    RDW(wb, w0, st + 18432); MM2(xa, wa); SCH(NTL);
    RDX(xb, x1, st); RDW(wa, w1, st); MM1(xa, wb); SCH(NTL + 2);
    BAR(); SB();
    RDW(wb, w1, st + 18432); MM2(xb, wa); SCH(NTL);
    MM1(xb, wb); SB();
    st = st + K4_STG == K4_TAB ? 0 : st + K4_STG;
  }
  if (NTL == 5) BAR();
  TS(4, 4);
  int lz = lane_id();
  const float* tab = (const float*)(lds + K4_TAB); const float* rs = tab; const float* mr = tab + 128; const float* cs = tab + 256;
  float* part = (float*)(lds + K4_PART); const float* st2 = (const float*)(lds + K4_ST2);
  u32x2 z2p[NTL][2];
  {
    const int fr = lz & 15, fq = lz >> 4;
#pragma unroll
    for (int m = 0; m < 2; ++m) {
      const f32x4 r4 = *(const f32x4*)(rs + 32 * wr + 16 * m + 4 * fq), m4 = *(const f32x4*)(mr + 32 * wr + 16 * m + 4 * fq);
      f32x4 su = {0, 0, 0, 0}, sq = {0, 0, 0, 0};
#pragma unroll
      for (int n = 0; n < NTL; ++n) {
        const float c = cs[16 * (nb0 + n) + fr];
        f32x4 g;
#pragma unroll
        for (int r = 0; r < 4; ++r) { g[r] = gelu_t(acc2[n][m][r] * r4[r] - m4[r] * c); su[r] += g[r]; sq[r] = __builtin_fmaf(g[r], g[r], sq[r]); }
        z2p[n][m][0] = pk2(g[0], g[1]); z2p[n][m][1] = pk2(g[2], g[3]); PIN(z2p[n][m]);
      }
#pragma unroll
      for (int r = 0; r < 4; ++r) { su[r] = dpp_sum16(su[r]); sq[r] = dpp_sum16(sq[r]); }
      if (fr == 0) {
#pragma unroll
        for (int r = 0; r < 4; ++r) *(f32x2_t*)(part + (wc * 128 + 32 * wr + 16 * m + 4 * fq + r) * 2) = f32x2_t{su[r], sq[r]};
      }
    }
  }
  WAITL0(); BAR();
  if (wc == 0 && lz < 32) {
    const int row = 32 * wr + lz; const f32x2_t p0 = *(const f32x2_t*)(part + row * 2), p1 = *(const f32x2_t*)(part + (128 + row) * 2);
    const u32x4 g = {__float_as_uint(p0[0] + p1[0]), XTAG, __float_as_uint(p0[1] + p1[1]), XTAG};
    st16_wt(ws, WS_XS + (size_t)(((pi * 16 + ft) * 128 + row) * 16), g);
  }
  TS(4, 2);
  {
    const int fr = lz & 15, fq = lz >> 4;
    const float r0 = rs[32 * wr + fr], mm0 = mr[32 * wr + fr], r1 = rs[32 * wr + 16 + fr], mm1 = mr[32 * wr + 16 + fr];
#pragma unroll
    for (int n = 0; n < NTL; ++n) {
      const f32x4 c4 = *(const f32x4*)(cs + 144 + 16 * (nb0 + n) + 4 * fq);
      float g0[4], g1[4];
#pragma unroll
      for (int r = 0; r < 4; ++r) { g0[r] = gelu_t(acc1[n][0][r] * r0 - mm0 * c4[r]); g1[r] = gelu_t(acc1[n][1][r] * r1 - mm1 * c4[r]); }
      z1v[4 * n] = pk2(g0[0], g0[1]); z1v[4 * n + 1] = pk2(g0[2], g0[3]); z1v[4 * n + 2] = pk2(g1[0], g1[1]); z1v[4 * n + 3] = pk2(g1[2], g1[3]);
      PIN(z1v[4 * n]); PIN(z1v[4 * n + 1]); PIN(z1v[4 * n + 2]); PIN(z1v[4 * n + 3]);
    }
  }
  TS(4, 5);
  __syncthreads();
  asm volatile("" : "+v"(lz));
#pragma unroll
  for (int m = 0; m < 2; ++m) {
    const int fr = lz & 15, fq = lz >> 4;
    const int sl = 32 * wr + 16 * m + 4 * fq;
    const f32x4 sa = *(const f32x4*)(st2 + sl * 2), sb = *(const f32x4*)(st2 + sl * 2 + 4);
    const float mu[4] = {sa[0], sa[2], sb[0], sb[2]}, rr[4] = {sa[1], sa[3], sb[1], sb[3]};
#pragma unroll
    for (int n = 0; n < NTL; ++n) {
      const int f = 16 * (nb0 + n) + fr; const u32x2 p = z2p[n][m];
      u32x2 o; o[0] = pk2(bflo(p[0]) * rr[0] - mu[0], bfhi(p[0]) * rr[1] - mu[1]); o[1] = pk2(bflo(p[1]) * rr[2] - mu[2], bfhi(p[1]) * rr[3] - mu[3]);
      *(u32x2*)(lds + (sl >> 6) * 18432 + f * 128 + ((((sl & 63) >> 3) ^ ((f >> 1) & 7)) << 4) + (sl & 4) * 2) = o;
    }
  }
}
template <int NTL>
DI void k5_compute(char* lds, int lane, int w, int tt, int ft, const u16* __restrict__ bsp, char* ws, const unsigned (&z1v)[4 * NTL]) {
  const int wr = w & 3, nb0 = NTL == 5 ? 0 : 5, fr = lane & 15, fq = lane >> 4;
  const C8Addr a = c8_addr<false>(lds, lane, wr, nb0);
  f32x4 acc[NTL][2] = {};
  C8Frag<NTL> f0, f1;
  WAITV(0); BAR();
  TS(5, 1);
  c8_read<NTL, false>(f0, a, 0, 0);
  float bb[2];
#pragma unroll
  for (int m = 0; m < 2; ++m) bb[m] = bf2f(bsp[tt * 128 + 32 * wr + 16 * m + fr]);
  int st = 0;
#define C5_ITER(SIDE0, SIDE1) do { const int stn = st + C8_STG == C8_LDS ? 0 : st + C8_STG; \
    c8_read<NTL, false>(f1, a, st, 1); c8_mma<NTL, true>(acc, f0); c8_sched<NTL, false>(); SB(); SIDE0; SB(); BAR(); SB(); \
    c8_read<NTL, false>(f0, a, stn, 0); c8_mma<NTL, true>(acc, f1); c8_sched<NTL, false>(); SB(); SIDE1; SB(); st = stn; } while (0)
#pragma unroll 1
  for (int kt = 0; kt < 32; ++kt) C5_ITER((void)0, (void)0);
  TS(5, 2);
  __syncthreads();
#pragma unroll
  for (int m = 0; m < 2; ++m) {
    const int t = 32 * wr + 16 * m + fr;
#pragma unroll
    for (int n = 0; n < NTL; ++n) {
      const int f = 16 * (nb0 + n) + 4 * fq; const unsigned z0 = z1v[4 * n + 2 * m], z1 = z1v[4 * n + 2 * m + 1];
      u32x2 o; o[0] = pk2(bflo(z0) * (acc[n][m][0] + bb[m]), bfhi(z0) * (acc[n][m][1] + bb[m]));
      o[1] = pk2(bflo(z1) * (acc[n][m][2] + bb[m]), bfhi(z1) * (acc[n][m][3] + bb[m]));
      *(u32x2*)(lds + t * EP_PITCH + f * 2) = o;
    }
  }
}

DI void p4_copyout(char* ws, char* lds, int w, int pi, int ft) {
  __syncthreads();
  const size_t dst = WS_Z2T + ((size_t)ft * 32 + pi * 2) * 18432;
  for (int i = w * 64 + lane_id(); i < 2304; i += 768) st16_wt(ws, dst + i * 16, *(const u32x4*)(lds + i * 16));
  WAITV(0); __syncthreads();
  if (threadIdx.x == 0) flag_add(ws, F_Z + ft);
  TS(4, 3); TS(4, 7);
}
DI void p5_copyout(char* ws, char* lds, int w, int tt, int ft) {
  __syncthreads();
  const size_t ub = WS_U + ((size_t)tt * 128 * FH + ft * 144) * 2;
  for (int i = w * 64 + lane_id(); i < 128 * 18; i += 768) { const int t = i / 18, c = i - t * 18; st16_wt(ws, ub + (size_t)t * FH * 2 + c * 16, *(const u32x4*)(lds + t * EP_PITCH + c * 16)); }
  WAITV(0); __syncthreads();
  if (threadIdx.x == 0) flag_add(ws, F_U + tt);
  TS(5, 3); TS(5, 7);
}
DI void p45(const u16* __restrict__ wsp, const u16* __restrict__ bsp, char* __restrict__ ws, char* lds) {
  const int lane = lane_id(); const int w = __builtin_amdgcn_readfirstlane(threadIdx.x >> 6); const int tid = w * 64 + lane;
  const int b = blockIdx.x, xc = b & 7, idx = b >> 3;
  const int pi = (xc & 1) * 8 + (idx & 7), ft = (xc >> 1) * 4 + (idx >> 3);
  float* tab = (float*)(lds + K4_TAB);
  TS(4, 0); TS(4, 6);
  const char* Xb = ws + WS_H + (size_t)pi * 128 * D * 2; const char* W2b = ws + WS_WIN + (size_t)ft * 9 * 18432; const char* W1b = W2b + (size_t)16 * 9 * 18432;
  auto src = [&](int p) { return p < 16 ? Xb + tn_off(p * 8 + (lane >> 3), lane, D * 2) : (p < 34 ? W2b + (p - 16) * 1024 : W1b + (p - 34) * 1024) + lane * 16; };
  if (b >= 192) { if (w == 11) flag_wait(ws, F_W, 1024); __syncthreads(); }
#pragma unroll
  for (int j = 0; j < 3; ++j) glds16a<16>(src(16 + w + 12 * j), lds + (16 + w + 12 * j) * 1024);
  if (w == 11) { flag_wait(ws, F_H + pi, 12); flag_wait(ws, F_CS, 64); }
  __syncthreads();
  glds16a<16>(src(w), lds + w * 1024);
  if (w < 4) glds16a<16>(src(12 + w), lds + (12 + w) * 1024);
  const char* Xb5 = (const char*)(wsp + (size_t)pi * 128 * S); const char* Wb5 = ws + WS_Z2T + (size_t)ft * 32 * 18432;
  auto src5 = [&](int p) { return p < 16 ? Xb5 + tn_off(p * 8 + (lane >> 3), lane, S * 2) : Wb5 + (p - 16) * 1024 + lane * 16; };
  if (w >= 8) {
    const int lw = w - 8;
    __builtin_amdgcn_s_setprio(2);
    {
      const char* base[13];
#pragma unroll
      for (int j = 0; j < 13; ++j) base[j] = src(lw + 4 * j);
      auto issue = [&](int kt, int stage) {
#pragma unroll
        for (int j = 0; j < 13; ++j) glds16a<16>(base[j] + (j < 4 ? (long)kt * 128 : (long)kt * 18432), lds + stage * K4_STG + (lw + 4 * j) * 1024);
      };
      issue(1, 1);
      int stg = 2;
#pragma unroll 1
      for (int kt = 0; kt < 7; ++kt) { WAITV(13); BAR(); BAR(); issue(kt + 2, stg); stg = stg == 2 ? 0 : stg + 1; }
      WAITV(13); BAR(); BAR();
      WAITV(0); BAR(); BAR();
      BAR();
      BAR();
      const int row = 32 * lw + (lane >> 1);
      const __amdgpu_buffer_rsrc_t rs_ = __builtin_amdgcn_make_buffer_rsrc((void*)(ws + WS_XS), 0, 2048 * 16 * 16, 0x00020000);
      const unsigned vo = (unsigned)(((pi * 16 + (lane & 1) * 8) * 128 + row) * 16);
      float sum = 0.f, ssq = 0.f;
      for (int spin = 0; spin < (1 << 20); ++spin) {
        bool ok = true; sum = 0.f; ssq = 0.f;
        u32x4 g[8];
#pragma unroll
        for (int t = 0; t < 8; ++t) g[t] = __builtin_amdgcn_raw_buffer_load_b128(rs_, vo + t * 2048, 0, 16);
#pragma unroll
        for (int t = 0; t < 8; ++t) { ok &= g[t][1] == XTAG && g[t][3] == XTAG; sum += __uint_as_float(g[t][0]); ssq += __uint_as_float(g[t][2]); }
        if (__all(ok)) break;
        __builtin_amdgcn_s_sleep(8);
      }
      sum += __shfl_xor(sum, 1); ssq += __shfl_xor(ssq, 1);
      const float mean = sum * (1.f / FH), var = fmaxf(ssq * (1.f / FH) - mean * mean, 0.f), rstd = rsqrtf(var + 1e-5f);
      if ((lane & 1) == 0) *(f32x2_t*)(lds + K4_ST2 + row * 8) = f32x2_t{mean * rstd, rstd};
      __syncthreads();
    }
    p4_copyout(ws, lds, w, pi, ft);
    TS(5, 0); TS(5, 6);
    glds16(src5(w), lds + w * 1024); if (w < 4) glds16(src5(12 + w), lds + (12 + w) * 1024);
    if (w == 11) flag_wait(ws, F_Z + ft, 16);
    __syncthreads();
    glds16a<16>(src5(16 + w), lds + (16 + w) * 1024); if (w < 6) glds16a<16>(src5(28 + w), lds + (28 + w) * 1024);
    loader_loop<32, C8_NS, C8_STG, 34, 16, 4, 0, 16>(lds, lw, src5, [&](int kt, long& ox, long& ow) { ox = (long)kt * 128; ow = (long)kt * 18432; }, NoPost());
    __builtin_amdgcn_s_setprio(0);
    __syncthreads();
    p5_copyout(ws, lds, w, pi, ft);
  } else {
    if (tid < 128) {
      const char* p = ws + WS_ST2 + (size_t)(pi * 128 + tid) * 96;
      float sum = 0.f, ssq = 0.f;
#pragma unroll
      for (int t = 0; t < 12; ++t) { const u32x2 v = ld8_c(p + t * 8); sum += __uint_as_float(v[0]); ssq += __uint_as_float(v[1]); }
      const float mean = sum * (1.f / D), var = fmaxf(ssq * (1.f / D) - mean * mean, 0.f), rstd = rsqrtf(var + 1e-5f);
      tab[tid] = rstd; tab[128 + tid] = mean * rstd;
    } else if (tid < 416) {
      const int c = tid - 128;
      tab[256 + c] = ld4f_c((const float*)(ws + WS_CS) + (c < 144 ? FH + ft * 144 + c : ft * 144 + c - 144));
    }
#define P45_PATH(NTL) do { unsigned z1v[4 * NTL]; \
      k4_compute<NTL>(lds, lane, w, pi, ft, ws, z1v); \
      p4_copyout(ws, lds, w, pi, ft); \
      if (blockIdx.x == 0 && threadIdx.x == 0) ((volatile long long*)(ws + WS_DBG + 4096))[4] = __builtin_amdgcn_s_memrealtime(); \
      TS(5, 0); TS(5, 6); \
      glds16(src5(w), lds + w * 1024); if (w < 4) glds16(src5(12 + w), lds + (12 + w) * 1024); \
      __syncthreads(); \
      glds16a<16>(src5(16 + w), lds + (16 + w) * 1024); if (w < 6) glds16a<16>(src5(28 + w), lds + (28 + w) * 1024); \
      k5_compute<NTL>(lds, lane_id(), w, pi, ft, bsp, ws, z1v); \
      p5_copyout(ws, lds, w, pi, ft); } while (0)
    if (w < 4) P45_PATH(5); else P45_PATH(4);
  }
}

__global__ __launch_bounds__(768, 3) void mega_kernel(const u16* __restrict__ x, const u16* __restrict__ wo, const u16* __restrict__ w_in, const u16* __restrict__ w_sp,
                                                      const u16* __restrict__ b_sp, const u16* __restrict__ w_out, u16* __restrict__ out, char* __restrict__ ws) {
  extern __shared__ __attribute__((aligned(16))) char lds[];
  const int b = blockIdx.x;
  if (blockIdx.x == 0 && threadIdx.x == 0) ((volatile long long*)(ws + WS_DBG + 4096))[1] = __builtin_amdgcn_s_memrealtime();
  p2_attention(wo, w_in, w_out, w_sp, ws, lds);
  if (blockIdx.x == 0 && threadIdx.x == 0) ((volatile long long*)(ws + WS_DBG + 4096))[2] = __builtin_amdgcn_s_memrealtime();
  if (b < 192) g_body<768, true, 3>(ws, lds, (const u16*)(ws + WS_O), ws + WS_WO, x, (u16*)(ws + WS_H), (float*)(ws + WS_ST2), b);
  else p3_colsum(w_in, ws, lds, b);
  __syncthreads();
  if (blockIdx.x == 0 && threadIdx.x == 0) ((volatile long long*)(ws + WS_DBG + 4096))[3] = __builtin_amdgcn_s_memrealtime();
  p45(w_sp, b_sp, ws, lds);
  if (blockIdx.x == 0 && threadIdx.x == 0) ((volatile long long*)(ws + WS_DBG + 4096))[5] = __builtin_amdgcn_s_memrealtime();
  if (b < 192) g_body<FH, false, 0>(ws, lds, (const u16*)(ws + WS_U), ws + WS_WOUT, (const u16*)(ws + WS_H), out, nullptr, b);
  if (blockIdx.x == 0 && threadIdx.x == 0) ((volatile long long*)(ws + WS_DBG + 4096))[6] = __builtin_amdgcn_s_memrealtime();
}

__global__ void probe_kernel(char* ws, int i0, int i1) {
  const volatile long long* s = (const volatile long long*)(ws + WS_DBG + 4096);
  const long long d = (s[i1] - s[i0]) * 10;
  const long long t0 = __builtin_amdgcn_s_memrealtime();
  while (__builtin_amdgcn_s_memrealtime() - t0 < d && __builtin_amdgcn_s_memrealtime() - t0 < 100000) __builtin_amdgcn_s_sleep(1);
}
extern "C" void kernel_launch(void* const* d_in, const int* in_sizes, int n_in, void* d_out, int out_size, void* d_ws, size_t ws_size, hipStream_t stream) {
  const u16* x = (const u16*)d_in[0]; const u16* wqkv = (const u16*)d_in[1]; const u16* wo = (const u16*)d_in[2];
  const u16* w_in = (const u16*)d_in[4]; const u16* w_sp = (const u16*)d_in[5]; const u16* b_sp = (const u16*)d_in[6]; const u16* w_out = (const u16*)d_in[7];
  u16* out = (u16*)d_out; char* ws = (char*)d_ws;
  static bool attr = false;
  if (!attr) {
    (void)hipFuncSetAttribute((const void*)k1_kernel, hipFuncAttributeMaxDynamicSharedMemorySize, K1_LDS);
    (void)hipFuncSetAttribute((const void*)mega_kernel, hipFuncAttributeMaxDynamicSharedMemorySize, ATT_LDS);
    attr = true;
  }
  k1_kernel<<<256, K1_THREADS, K1_LDS, stream>>>(x, wqkv, ws);
  mega_kernel<<<256, 768, ATT_LDS, stream>>>(x, wo, w_in, w_sp, b_sp, w_out, out, ws);
  probe_kernel<<<1, 64, 0, stream>>>(ws, 2, 3);
}
```

```cpp
#include <hip/hip_runtime.h>
#include <hip/hip_bf16.h>
#include <stdint.h>
typedef unsigned short u16;
using bf16x8 = __attribute__((ext_vector_type(8))) short;
using s16x4 = __attribute__((ext_vector_type(4))) short;
using f32x4 = __attribute__((ext_vector_type(4))) float;
using f32x16 = __attribute__((ext_vector_type(16))) float;
using u32x4 = __attribute__((ext_vector_type(4))) unsigned;
using u32x2 = __attribute__((ext_vector_type(2))) unsigned;
using u32x32 = __attribute__((ext_vector_type(32))) unsigned;
typedef __bf16 bf16x2_t __attribute__((ext_vector_type(2)));
typedef float f32x2_t __attribute__((ext_vector_type(2)));
constexpr int S = 2048, D = 576, H = 32, DH = 18, F = 4608, FH = 2304;
#define DI __device__ __forceinline__
#define LAS __attribute__((address_space(3)))
#define GAS __attribute__((address_space(1)))
DI int lane_id() { int l = __builtin_amdgcn_mbcnt_hi(~0u, __builtin_amdgcn_mbcnt_lo(~0u, 0u)); asm volatile("" : "+v"(l)); return l; }
DI float bf2f(u16 x) { return __uint_as_float((unsigned)x << 16); }
DI float bflo(unsigned x) { return __uint_as_float(x << 16); }
DI float bfhi(unsigned x) { return __uint_as_float(x & 0xffff0000u); }
DI unsigned pk2(float lo, float hi) { f32x2_t v = {lo, hi}; return __builtin_bit_cast(unsigned, __builtin_convertvector(v, bf16x2_t)); }
#define MFMA32(a, b, c) __builtin_amdgcn_mfma_f32_32x32x16_bf16((a), (b), (c), 0, 0, 0)
#define MFMA16(a, b, c) __builtin_amdgcn_mfma_f32_16x16x32_bf16((a), (b), (c), 0, 0, 0)
#define SB() __builtin_amdgcn_sched_barrier(0)
#define SGB(mask, n) __builtin_amdgcn_sched_group_barrier(mask, n, 0)
#define PIN(x) asm volatile("" : "+v"(x))
#define BAR() __builtin_amdgcn_s_barrier()
#define WAITV(n) asm volatile("s_waitcnt vmcnt(%0)" :: "i"(n) : "memory")
#define WAITL0() asm volatile("s_waitcnt lgkmcnt(0)" ::: "memory")
DI void glds16(const void* g, void* l) { __builtin_amdgcn_global_load_lds((const GAS unsigned*)g, (LAS unsigned*)l, 16, 0, 0); }
template <int AUX> DI void glds16a(const void* g, void* l) { __builtin_amdgcn_global_load_lds((const GAS unsigned*)g, (LAS unsigned*)l, 16, 0, AUX); }
DI void st16_wt(char* ws, size_t off, u32x4 v) {
  const __amdgpu_buffer_rsrc_t r = __builtin_amdgcn_make_buffer_rsrc((void*)ws, 0, 128 << 20, 0x00020000);
  __builtin_amdgcn_raw_buffer_store_b128(v, r, (unsigned)off, 0, 16);
}
typedef LAS const char* lds_cptr;
typedef LAS char* lds_ptr;
#define LDS_RD128(p) (*(const LAS bf16x8*)(p))
#define LDS_RD32(p) (*(const LAS unsigned*)(p))
typedef short v4i16_t __attribute__((ext_vector_type(4)));
DI s16x4 lds_tr(lds_cptr p) { return __builtin_bit_cast(s16x4, __builtin_amdgcn_ds_read_tr16_b64_v4i16((LAS v4i16_t*)p)); }
DI bf16x8 lds_tr2(lds_cptr p0, lds_cptr p1) { const s16x4 a = lds_tr(p0), b = lds_tr(p1); return __builtin_shufflevector(a, b, 0, 1, 2, 3, 4, 5, 6, 7); }

#define TIMING 0
#define TS(k, i)
#define TR(k, i)
#define TSTAMP(v)
#define RSTAMP(v)

constexpr int KV_TILE = 2400, KV_K1 = 1024, KV_V = 1152, KV_VS = 624, KV_Z = KV_V + 608, KV_HEAD = 64 * KV_TILE;
constexpr int Q_TILE = 1152, Q_HEAD = 64 * Q_TILE;
constexpr size_t WS_KV = 0;
constexpr size_t WS_Q = 5u << 20;
constexpr size_t WS_O = 8u << 20;
constexpr size_t WS_H = 11u << 20;
constexpr size_t WS_ST2 = 14u << 20;
constexpr size_t WS_CS = 15u << 20;
constexpr size_t WS_XS = 16u << 20;
constexpr size_t WS_FLAG = WS_XS + (512u << 10);
constexpr size_t WS_Z2T = 17u << 20;
constexpr size_t WS_Z1 = 27u << 20;
constexpr size_t WS_U = 37u << 20;
constexpr size_t WS_WIN = 47u << 20;
constexpr size_t WS_WO = 53u << 20;
constexpr size_t WS_WOUT = 54u << 20;
constexpr size_t WS_DBG = 64u << 20;

constexpr int F_O = 0, F_W = 8, F_H = 9, F_CS = 25, F_Z = 26, F_U = 42;
typedef GAS unsigned gu32;
DI gu32* flagp(char* ws, int i) { return (gu32*)(ws + WS_FLAG + (size_t)i * 256); }
DI void flag_add(char* ws, int i) { __hip_atomic_fetch_add(flagp(ws, i), 1u, __ATOMIC_RELAXED, __HIP_MEMORY_SCOPE_AGENT); }
DI void flag_wait(char* ws, int i, unsigned target) {
  gu32* p = flagp(ws, i);
  for (int spin = 0; spin < (1 << 22); ++spin) {
    if ((unsigned)__builtin_amdgcn_readfirstlane(__hip_atomic_load(p, __ATOMIC_RELAXED, __HIP_MEMORY_SCOPE_AGENT)) >= target) break;
    __builtin_amdgcn_s_sleep(2);
  }
}
DI void st4_wt(void* p, unsigned v) { __hip_atomic_store((gu32*)p, v, __ATOMIC_RELAXED, __HIP_MEMORY_SCOPE_AGENT); }
DI void st8_wt(void* p, u32x2 v) { __hip_atomic_store((GAS unsigned long long*)p, ((unsigned long long)v[1] << 32) | v[0], __ATOMIC_RELAXED, __HIP_MEMORY_SCOPE_AGENT); }
DI u32x2 ld8_c(const void* p) { const unsigned long long v = __hip_atomic_load((GAS unsigned long long*)p, __ATOMIC_RELAXED, __HIP_MEMORY_SCOPE_AGENT); return u32x2{(unsigned)v, (unsigned)(v >> 32)}; }
DI float ld4f_c(const void* p) { return __uint_as_float(__hip_atomic_load((gu32*)p, __ATOMIC_RELAXED, __HIP_MEMORY_SCOPE_AGENT)); }
template <int NP, int NW, int NPX = 0, int AX = 0, int AW = 0, class SRC>
DI void first_tile(char* lds, int w, SRC src) {
#pragma unroll
  for (int j = 0; j < (NP + NW - 1) / NW; ++j) { const int p = w + NW * j; if (p < NP) { if (p < NPX) glds16a<AX>(src(p), lds + p * 1024); else glds16a<AW>(src(p), lds + p * 1024); } }
}
template <int NT, int NS, int STG, int NP, int NPX, int NLD, int AX = 0, int AW = 0, class SRC, class OFF, class POST>
DI void loader_loop(char* lds, int lw, SRC src, OFF off, POST post) {
  constexpr int PPW = (NP + NLD - 1) / NLD, PMIN = NP / NLD;
  static_assert((NS - 1) * PPW + 4 <= 63, "vmcnt");
  static_assert(NPX % NLD == 0, "X pieces");
  const char* base[PPW];
#pragma unroll
  for (int j = 0; j < PPW; ++j) { const int p = lw + NLD * j; base[j] = src(p < NP ? p : NP - 1); }
  auto issue = [&](int kt, int stage) {
    long ox, ow; off(kt, ox, ow);
#pragma unroll
    for (int j = 0; j < PPW; ++j) if (j < PMIN || lw + NLD * j < NP) {
      if (j < NPX / NLD) glds16a<AX>(base[j] + ox, lds + stage * STG + (lw + NLD * j) * 1024); else glds16a<AW>(base[j] + ow, lds + stage * STG + (lw + NLD * j) * 1024); }
  };
  issue(1, 1);
  WAITV(PMIN);
  BAR();
#pragma unroll
  for (int t = 2; t < NS - 1; ++t) issue(t, t);
  post(0);
  int stg = NS - 1, pst = 1;
#pragma unroll 1
  for (int kt = 0; kt < NT - (NS - 1); ++kt) {
    WAITV((NS - 3) * PMIN);
    BAR();
    issue(kt + NS - 1, stg); stg = stg + 1 == NS ? 0 : stg + 1;
    post(pst); pst = pst + 1 == NS ? 0 : pst + 1;
  }
#pragma unroll
  for (int r = NS - 2; r >= 0; --r) {
    WAITV((r - 1 > 0 ? r - 1 : 0) * PMIN); BAR();
    if (r > 0) { post(pst); pst = pst + 1 == NS ? 0 : pst + 1; }
  }
}
struct NoPost { DI void operator()(int) const {} };
DI unsigned tn_off(int row, int lane, int ld_bytes) { return (unsigned)row * ld_bytes + ((((lane & 7) ^ ((row >> 1) & 7))) << 4); }
template <int BN> DI unsigned kn_off(int p, int lane, int ld_bytes) {
  constexpr int CPR = BN / 8;
  const int c = 64 * p + lane, kp = c / CPR, cc = c - kp * CPR, odd = (kp >> 3) & 1, k = kp ^ (odd << 2);
  int cs = cc;
  if (BN == 96) { int blk = (cc >> 1) - odd; if (blk < 0) blk += 6; cs = blk * 2 + (cc & 1); }
  return (unsigned)k * ld_bytes + (unsigned)cs * 16;
}

DI float gelu_t(float x) {
  const float c1 = -2.f * 0.7978845608028654f * 1.4426950408889634f, c2 = c1 * 0.044715f;
  const float e = __builtin_amdgcn_exp2f(x * __builtin_fmaf(x * x, c2, c1));
  return x * __builtin_amdgcn_rcpf(1.f + e);
}

constexpr int C8_STG = 272 * 128, C8_W = 16384, C8_NS = 4, C8_LDS = C8_NS * C8_STG;
struct C8Addr { lds_cptr x0, x1, w0, w1; };
template <bool BKN> DI C8Addr c8_addr(char* lds, int lane, int wr, int nb0) {
  lds_cptr L = (lds_cptr)lds; const int fr = lane & 15, fq = lane >> 4, sw = (fr >> 1) & 7; C8Addr a;
  a.x0 = L + (32 * wr + fr) * 128 + ((fq ^ sw) << 4); a.x1 = L + (32 * wr + fr) * 128 + (((4 + fq) ^ sw) << 4);
  if (BKN) { const int q = fr >> 2, p4 = lane & 3, b = C8_W + (8 * fq + q) * 288 + nb0 * 32 + p4 * 8;
             a.w0 = L + b + (fq & 1) * 1152; a.w1 = L + b + ((fq & 1) ^ 1) * 1152; }
  else { a.w0 = L + C8_W + (16 * nb0 + fr) * 128 + ((fq ^ sw) << 4); a.w1 = L + C8_W + (16 * nb0 + fr) * 128 + (((4 + fq) ^ sw) << 4); }
  return a;
}
template <int NTL> struct C8Frag { bf16x8 x[2], w[NTL]; };
template <int NTL, bool BKN> DI void c8_read(C8Frag<NTL>& f, const C8Addr& a, int st, int ks) {
  f.x[0] = LDS_RD128((ks ? a.x1 : a.x0) + st); f.x[1] = LDS_RD128((ks ? a.x1 : a.x0) + st + 2048);
#pragma unroll
  for (int n = 0; n < NTL; ++n) {
    if (BKN) f.w[n] = lds_tr2(a.w0 + st + ks * 9216 + n * 32, a.w1 + st + ks * 9216 + n * 32);
    else f.w[n] = LDS_RD128((ks ? a.w1 : a.w0) + st + n * 2048);
  }
}
template <int NTL, bool WX> DI void c8_mma(f32x4 (&acc)[NTL][2], const C8Frag<NTL>& f) {
#pragma unroll
  for (int n = 0; n < NTL; ++n) {
#pragma unroll
    for (int m = 0; m < 2; ++m) acc[n][m] = WX ? MFMA16(f.w[n], f.x[m], acc[n][m]) : MFMA16(f.x[m], f.w[n], acc[n][m]);
  }
}
template <int NTL, bool BKN> DI void c8_sched() {
  constexpr int NR = 2 + (BKN ? 2 * NTL : NTL), NM = 2 * NTL;
  if (NR <= NM) {
#pragma unroll
    for (int i = 0; i < NR; ++i) { SGB(0x8, 1); SGB(0x100, 1); }
    SGB(0x8, NM - NR);
  } else {
#pragma unroll
    for (int i = 0; i < NM; ++i) { SGB(0x8, 1); SGB(0x100, 1); if (i < NR - NM) SGB(0x100, 1); }
  }
}

constexpr int K1_THREADS = 768;
constexpr int EP_PITCH = 304;
template <int WC, bool WX>
DI void k1_compute(char* lds, int lane, int w, f32x4 (&acc)[WC == 0 ? 3 : 2][4], float* tabs, char* ws) {
  constexpr int NTL = WC == 0 ? 3 : 2, NB0 = WC == 0 ? 0 : 2 * WC + 1, NST = WC == 0 ? 0 : (WC == 1 ? 2 : 1), M0 = WC == 1 ? 0 : WC - 1, M1 = 3;
  const int wr = w & 1, fr = lane & 15, fq = lane >> 4, sw = (fr >> 1) & 7;
  lds_cptr L = (lds_cptr)lds;
  lds_cptr x0 = L + (64 * wr + fr) * 128 + ((fq ^ sw) << 4), x1 = L + (64 * wr + fr) * 128 + (((4 + fq) ^ sw) << 4);
  const int wb = C8_W + (8 * fq + (fr >> 2)) * 288 + NB0 * 32 + (lane & 3) * 8;
  lds_cptr w0 = L + wb + (fq & 1) * 1152, w1 = L + wb + ((fq & 1) ^ 1) * 1152;
  const u32x4 onesv = {0x3f803f80u, 0x3f803f80u, 0x3f803f80u, 0x3f803f80u}; const bf16x8 ones = __builtin_bit_cast(bf16x8, onesv);
  bf16x8 xa[4], wa[NTL], xb[4], wb_[NTL];
  f32x4 cs[NTL] = {}, rsum[2] = {}, rsq[2] = {};
#define K1RD(X, Wf, st, ks) do { _Pragma("unroll") for (int n = 0; n < NTL; ++n) Wf[n] = lds_tr2(w0 + (st) + (ks) * 9216 + n * 32, w1 + (st) + (ks) * 9216 + n * 32); \
    _Pragma("unroll") for (int m = 0; m < 4; ++m) X[m] = LDS_RD128(((ks) ? x1 : x0) + (st) + m * 2048); } while (0)
#define K1MM(X, Wf) do { _Pragma("unroll") for (int n = 0; n < NTL; ++n) { _Pragma("unroll") for (int m = 0; m < 4; ++m) acc[n][m] = WX ? MFMA16(Wf[n], X[m], acc[n][m]) : MFMA16(X[m], Wf[n], acc[n][m]); \
    cs[n] = MFMA16(ones, Wf[n], cs[n]); } \
    if (NST >= 1) { rsum[0] = MFMA16(X[M0], ones, rsum[0]); rsq[0] = MFMA16(X[M0], X[M0], rsq[0]); } \
    if (NST >= 2) { rsum[1] = MFMA16(X[M1], ones, rsum[1]); rsq[1] = MFMA16(X[M1], X[M1], rsq[1]); } } while (0)
#define K1SCHED() do { _Pragma("unroll") for (int i_ = 0; i_ < 2 * NTL + 4; ++i_) { SGB(0x8, 1); SGB(0x100, 1); } SGB(0x8, 5 * NTL + 2 * NST - (2 * NTL + 4)); } while (0)
  WAITV(0); BAR();
  TS(1, 1);
  K1RD(xa, wa, 0, 0);
  int st = 0;
#pragma unroll 1
  for (int kt = 0; kt < 9; ++kt) {
    const int stn = st + C8_STG == C8_LDS ? 0 : st + C8_STG;
    K1RD(xb, wb_, st, 1); K1MM(xa, wa); K1SCHED();
    SB(); BAR(); SB();
    K1RD(xa, wa, stn, 0); K1MM(xb, wb_); K1SCHED();
    SB();
    st = stn;
  }
  TS(1, 2);
  const int l2 = lane_id(), r2 = l2 & 15, q2 = l2 >> 4;
  if (l2 < 16 && wr == 0) {
#pragma unroll
    for (int n = 0; n < NTL; ++n) tabs[256 + 16 * (NB0 + n) + l2] = cs[n][0];
  }
#pragma unroll
  for (int t = 0; t < NST; ++t) {
    const int mrow = 64 * wr + 16 * (t == 0 ? M0 : M1);
    if (r2 == 0) *(f32x4*)(tabs + mrow + 4 * q2) = rsum[t];
    if (q2 == (r2 >> 2)) tabs[128 + mrow + r2] = (r2 & 2) ? ((r2 & 1) ? rsq[t][3] : rsq[t][2]) : ((r2 & 1) ? rsq[t][1] : rsq[t][0]);
  }
}
template <int NTL>
DI void k1_epilogue(char* lds, int lane, int w, int type, int rt, int hg, const float* tabs, f32x4 (&acc)[NTL][4], char* kvimg) {
  asm volatile("" : "+v"(lane));
  const int wr = w & 1, wc = w >> 1, nb0 = wc == 0 ? 0 : 2 * wc + 1, fr = lane & 15, fq = lane >> 4;
  const float* rs = tabs; const float* mr = tabs + 128; const float* cs = tabs + 256;
  if (type == 2) {
#pragma unroll
    for (int n = 0; n < NTL; ++n) {
      const int col = 16 * (nb0 + n) + fr, hh = col / 18, d = col - hh * 18; const float c = cs[col];
      char* hb = kvimg + (size_t)(hg * 8 + hh) * KV_HEAD + (size_t)(rt * 4 + 2 * wr) * KV_TILE + KV_V + (fq & 1) * 304 + (fq >> 1) * 8 + d * 16;
#pragma unroll
      for (int m = 0; m < 4; ++m) {
        const int row = 64 * wr + 16 * m + 4 * fq;
        const f32x4 r4 = *(const f32x4*)(rs + row), m4 = *(const f32x4*)(mr + row);
        u32x2 o; o[0] = pk2(acc[n][m][0] * r4[0] - m4[0] * c, acc[n][m][1] * r4[1] - m4[1] * c); o[1] = pk2(acc[n][m][2] * r4[2] - m4[2] * c, acc[n][m][3] * r4[3] - m4[3] * c);
        *(u32x2*)(hb + (m >> 1) * KV_TILE + (m & 1) * KV_VS) = o;
      }
    }
  } else {
    const float qs = type == 0 ? 0.23570226039551584f * 1.4426950408889634f : 1.f;
#pragma unroll
    for (int m = 0; m < 4; ++m) {
      const int row = 64 * wr + 16 * m + fr; const float r = rs[row] * qs, mm = mr[row] * qs;
#pragma unroll
      for (int n = 0; n < NTL; ++n) {
        const int col = 16 * (nb0 + n) + 4 * fq; const f32x4 c4 = *(const f32x4*)(cs + col);
        u32x2 o; o[0] = pk2(acc[n][m][0] * r - mm * c4[0], acc[n][m][1] * r - mm * c4[1]); o[1] = pk2(acc[n][m][2] * r - mm * c4[2], acc[n][m][3] * r - mm * c4[3]);
        *(u32x2*)(lds + row * EP_PITCH + col * 2) = o;
      }
    }
  }
}
__global__ __launch_bounds__(K1_THREADS, 3) void k1_kernel(const u16* __restrict__ x, const u16* __restrict__ wqkv, char* __restrict__ ws) {
  extern __shared__ __attribute__((aligned(16))) char lds[];
  const int tid = threadIdx.x, lane = tid & 63; const int w = __builtin_amdgcn_readfirstlane(tid >> 6);
  const int b = blockIdx.x;
  TS(1, 0); TS(1, 6);
  if (b >= 192) {
    if (tid < 512) ((u32x4*)(ws + WS_XS))[(b - 192) * 512 + tid] = u32x4{0, 0, 0, 0};
    else if (tid < 528) ((u32x4*)(ws + WS_FLAG))[(b - 192) * 16 + tid - 512] = u32x4{0, 0, 0, 0};
    return;
  }
  const int xc = b & 7, idx = b >> 3;
  const int rt = xc * 2 + (idx & 1), ct = idx >> 1;
  const int type = ct >> 2, hg = ct & 3;
  float* tabs = (float*)(lds + C8_LDS);
  char* kvimg = ws + WS_KV; char* qimg = ws + WS_Q;
  const char* Xb = (const char*)(x + (size_t)rt * 128 * D); const char* Wb = (const char*)(wqkv + ct * 144);
  auto src = [&](int p) { return p < 16 ? Xb + tn_off(p * 8 + (lane >> 3), lane, D * 2) : Wb + kn_off<144>(p - 16, lane, 1728 * 2); };
  first_tile<34, 12>(lds, w, src);
  if (w >= 8) {
    __builtin_amdgcn_s_setprio(2);
    loader_loop<9, C8_NS, C8_STG, 34, 16, 4>(lds, w - 8, src, [&](int kt, long& ox, long& ow) { ox = (long)kt * 128; ow = (long)kt * 64 * 1728 * 2; }, NoPost());
    __syncthreads();
    const int row = (w - 8) * 64 + lane_id();
    if (row < 128) { const float mean = tabs[row] * (1.f / D), var = fmaxf(tabs[128 + row] * (1.f / D) - mean * mean, 0.f), rstd = rsqrtf(var + 1e-5f); tabs[row] = rstd; tabs[128 + row] = mean * rstd; }
    __syncthreads();
  } else {
#define K1_PATH(WC, WX) do { f32x4 acc[WC == 0 ? 3 : 2][4] = {}; \
      k1_compute<WC, WX>(lds, lane, w, acc, tabs, ws); \
      __syncthreads(); __syncthreads(); \
      k1_epilogue<WC == 0 ? 3 : 2>(lds, lane_id(), w, WX ? (type & 1) : 2, rt, hg, tabs, acc, kvimg); } while (0)
#define K1_SEL(WX) do { if (w < 2) K1_PATH(0, WX); else if (w < 4) K1_PATH(1, WX); else if (w < 6) K1_PATH(2, WX); else K1_PATH(3, WX); } while (0)
    if (type == 2) K1_SEL(false); else K1_SEL(true);
  }
  const int tid2 = w * 64 + lane_id();
  if (type == 2) {
    if (tid2 < 192) {
      const int tl = tid2 / 48, r = tid2 - tl * 48, hh = r / 6, c = r - hh * 6;
      char* tb = kvimg + (size_t)(hg * 8 + hh) * KV_HEAD + (size_t)(rt * 4 + tl) * KV_TILE;
      if (c < 4) *(u32x4*)(tb + KV_V + (c >> 1) * KV_VS + (c & 1) * 304 + 18 * 16) = u32x4{0x3f803f80u, 0x3f803f80u, 0x3f803f80u, 0x3f803f80u};
      else *(u32x4*)(tb + KV_Z + (c - 4) * KV_VS) = u32x4{0, 0, 0, 0};
    }
    TS(1, 3); TS(1, 7);
    return;
  }
  __syncthreads();
  for (int it = tid2; it < 1024; it += K1_THREADS) {
    const int tok = it & 127, hh = it >> 7;
    const unsigned* sp = (const unsigned*)(lds + tok * EP_PITCH + hh * 36);
    u32x4 c0 = {sp[0], sp[1], sp[2], sp[3]}, c1 = {sp[4], sp[5], sp[6], sp[7]}; const unsigned c2 = sp[8];
    char* tb = type == 0 ? qimg + (size_t)(hg * 8 + hh) * Q_HEAD + (size_t)(rt * 4 + (tok >> 5)) * Q_TILE
                         : kvimg + (size_t)(hg * 8 + hh) * KV_HEAD + (size_t)(rt * 4 + (tok >> 5)) * KV_TILE;
    const int key = tok & 31;
    *(u32x4*)(tb + key * 16) = c0; *(u32x4*)(tb + 512 + key * 16) = c1; *(unsigned*)(tb + 1024 + key * 4) = c2;
  }
  TS(1, 3); TS(1, 7);
}
static_assert(128 * EP_PITCH <= C8_LDS, "");
constexpr int K1_LDS = C8_LDS + 400 * 4;

constexpr int ATT_LDS = KV_HEAD + 8 * Q_TILE;
#define SWITCH 50
DI void p2_attention(const u16* __restrict__ wo, const u16* __restrict__ w_in, const u16* __restrict__ w_out, const u16* __restrict__ w_sp, char* __restrict__ ws, char* lds) {
  TS(2, 0); TS(2, 6);
  const char* kvimg = ws + WS_KV; const char* qimg = ws + WS_Q; u16* o = (u16*)(ws + WS_O);
  const int tid = threadIdx.x, lane = tid & 63; const int w = __builtin_amdgcn_readfirstlane(tid >> 6);
  const int b = blockIdx.x, xcd = b & 7, idx = b >> 3, h = xcd * 4 + (idx >> 3), qb = idx & 7;
  const char* kvg = kvimg + (size_t)h * KV_HEAD + lane * 16;
  if (w < 8) {
    const char* qg = qimg + (size_t)h * Q_HEAD + (qb * 8 + w) * Q_TILE + lane * 16;
    char* qtile = lds + KV_HEAD + w * Q_TILE;
    glds16(qg, qtile);
    if (lane < 8) glds16(qg + 1024, qtile + 1024);
  }
  glds16(kvg + w * 1024, lds + w * 1024); glds16(kvg + (w + 12) * 1024, lds + (w + 12) * 1024);
  WAITV(0);
  BAR();
  if (w >= 8) {
    const int lw = w - 8;
    __builtin_amdgcn_s_setprio(3);
#pragma unroll 1
    for (int p = 24 + lw; p < 150; p += 4) glds16(kvg + p * 1024, lds + p * 1024);
    WAITV(0);
    BAR();
    {
      const u32x4* wp = (const u32x4*)w_sp + (size_t)(b * 4 + lw) * 512 + lane;
      u32x4 acc = wp[0];
#pragma unroll
      for (int i = 1; i < 8; ++i) acc ^= wp[i * 64];
      if (acc[0] == 0x12345678u && acc[1] == 0x9abcdef0u && acc[2] == 0x0fedcba9u && acc[3] == 0x87654321u) ((unsigned*)(ws + WS_DBG))[0] = 1;
    }
    const int gw = b * 4 + lw, c = lane & 7, nb = lane >> 3;
    if (nb < 6) {
#pragma unroll 1
      for (int blk = gw; blk < 864 + 144 + 432; blk += 1024) {
        const u16* sp; int ld, kt, n0; size_t db;
        if (blk < 864) { kt = blk / 96; const int nbk = blk - kt * 96, z2 = nbk >= 48, nn = z2 ? nbk - 48 : nbk, t = nn / 3; n0 = nbk * 48; sp = w_in; ld = F;
          db = WS_WIN + ((size_t)((z2 ? 0 : 16) + t) * 9 + kt) * 18432 + (nn - t * 3) * 48 * 128; }
        else if (blk < 1008) { const int q = blk - 864; kt = q / 12; const int nbk = q - kt * 12; n0 = nbk * 48; sp = wo; ld = D; db = WS_WO + ((size_t)(nbk >> 1) * 12 + kt) * 12288 + (nbk & 1) * 48 * 128; }
        else { const int q = blk - 1008; kt = q / 12; const int nbk = q - kt * 12; n0 = nbk * 48; sp = w_out; ld = D; db = WS_WOUT + ((size_t)(nbk >> 1) * 36 + kt) * 12288 + (nbk & 1) * 48 * 128; }
        u32x4 r[8];
        if (blk >= 864 && blk < 1008) {
          const int kc = kt * 8 + c, hh = kc / 3, part = kc - hh * 3;
          const u16* sq = sp + (size_t)(hh * DH) * ld + n0 + nb * 8;
#pragma unroll
          for (int j = 0; j < 8; ++j) {
            const int d = part == 2 ? 16 + j : (j & 3) + 8 * (j >> 2) + 4 * part;
            r[j] = (part == 2 && j >= 2) ? u32x4{0, 0, 0, 0} : *(const u32x4*)(sq + (size_t)(d < DH ? d : 0) * ld);
          }
        } else {
          sp += (size_t)(kt * 64 + c * 8) * ld + n0 + nb * 8;
#pragma unroll
          for (int j = 0; j < 8; ++j) r[j] = *(const u32x4*)(sp + (size_t)j * ld);
        }
        const unsigned lo = (unsigned)db + nb * 1024;
#pragma unroll
        for (int e = 0; e < 4; ++e) {
          const unsigned sl = (unsigned)((c ^ ((4 * nb + e) & 7)) << 4);
#pragma unroll
          for (int hi = 0; hi < 2; ++hi) {
            const unsigned sel = hi ? 0x07060302u : 0x05040100u;
            u32x4 o;
#pragma unroll
            for (int q = 0; q < 4; ++q) o[q] = __builtin_amdgcn_perm(r[2 * q + 1][e], r[2 * q][e], sel);
            st16_wt(ws, lo + (2 * e + hi) * 128 + sl, o);
          }
        }
      }
    }
    WAITV(0);
    if (lane == 0) flag_add(ws, F_W);
    __builtin_amdgcn_s_setprio(0);
  } else {
  TS(2, 1); TR(2, 4);
  if (w >= 4) __builtin_amdgcn_s_setprio(1);
  const int r32 = lane & 31, hh = lane >> 5;
  lds_cptr L = (lds_cptr)lds;
  const bf16x8 qf0 = LDS_RD128(L + KV_HEAD + w * Q_TILE + lane * 16);
  u32x4 q1v = {0, 0, 0, 0}; { unsigned t = LDS_RD32(L + KV_HEAD + w * Q_TILE + 1024 + r32 * 4); q1v[0] = hh ? 0u : t; }
  const bf16x8 qf1 = __builtin_bit_cast(bf16x8, q1v);
  lds_cptr kp0 = L + lane * 16, kp1 = L + (hh ? KV_Z : KV_K1 + r32 * 4), vp = L + (r32 <= 18 ? KV_V + hh * 304 + r32 * 16 : KV_Z);
  f32x16 negm;
  {
    bf16x8 k0 = LDS_RD128(kp0); u32x4 k1 = {LDS_RD32(kp1), 0, 0, 0};
    f32x16 s = {}; s = MFMA32(k0, qf0, s); s = MFMA32(__builtin_bit_cast(bf16x8, k1), qf1, s);
    float m = s[0];
#pragma unroll
    for (int i = 1; i < 16; ++i) m = fmaxf(m, s[i]);
    auto rr = __builtin_amdgcn_permlane32_swap(__float_as_uint(m), __float_as_uint(m), false, false);
    m = fmaxf(__uint_as_float(rr[0]), __uint_as_float(rr[1]));
#pragma unroll
    for (int i = 0; i < 16; ++i) negm[i] = -m;
  }
  f32x16 O = {};
#define EX2(Sx, i) pk2(__builtin_amdgcn_exp2f(Sx[i]), __builtin_amdgcn_exp2f(Sx[(i) + 1]))
  bf16x8 k0, v0, v1; u32x4 k1 = {0, 0, 0, 0};
  f32x16 SA, SBc; u32x4 pA0, pA1, pB0, pB1;
#define STEP(Sc, Sn, Pc0, Pc1, Pp0, Pp1, koff, voff) do { \
    SB(); Sn = MFMA32(k0, qf0, negm); SB(); \
    Pc0[0] = EX2(Sc, 0); Pc0[1] = EX2(Sc, 2); PIN(Pc0); SB(); \
    Sn = MFMA32(__builtin_bit_cast(bf16x8, k1), qf1, Sn); SB(); \
    k0 = LDS_RD128(kp0 + (koff)); k1[0] = LDS_RD32(kp1 + (koff)); SB(); \
    Pc0[2] = EX2(Sc, 4); Pc0[3] = EX2(Sc, 6); PIN(Pc0); SB(); \
    O = MFMA32(v0, __builtin_bit_cast(bf16x8, Pp0), O); SB(); \
    Pc1[0] = EX2(Sc, 8); Pc1[1] = EX2(Sc, 10); PIN(Pc1); SB(); \
    O = MFMA32(v1, __builtin_bit_cast(bf16x8, Pp1), O); SB(); \
    v0 = LDS_RD128(vp + (voff)); v1 = LDS_RD128(vp + (voff) + KV_VS); SB(); \
    Pc1[2] = EX2(Sc, 12); Pc1[3] = EX2(Sc, 14); PIN(Pc1); SB(); \
  } while (0)
  k0 = LDS_RD128(kp0); k1[0] = LDS_RD32(kp1);
  SA = MFMA32(k0, qf0, negm); SA = MFMA32(__builtin_bit_cast(bf16x8, k1), qf1, SA);
  k0 = LDS_RD128(kp0 + KV_TILE); k1[0] = LDS_RD32(kp1 + KV_TILE);
  v0 = LDS_RD128(vp); v1 = LDS_RD128(vp + KV_VS);
  pB0 = u32x4{0, 0, 0, 0}; pB1 = u32x4{0, 0, 0, 0};
  int off = 0;
  for (int t = 0; t < 8; t += 2) {
    STEP(SA, SBc, pA0, pA1, pB0, pB1, off + 2 * KV_TILE, off);
    STEP(SBc, SA, pB0, pB1, pA0, pA1, off + 3 * KV_TILE, off + KV_TILE);
    off += 2 * KV_TILE;
  }
  BAR();
  for (int t = 8; t < 62; t += 2) {
    if (t == SWITCH) __builtin_amdgcn_s_setprio(0);
    STEP(SA, SBc, pA0, pA1, pB0, pB1, off + 2 * KV_TILE, off);
    STEP(SBc, SA, pB0, pB1, pA0, pA1, off + 3 * KV_TILE, off + KV_TILE);
    off += 2 * KV_TILE;
  }
  STEP(SA, SBc, pA0, pA1, pB0, pB1, off + KV_TILE, off);
  STEP(SBc, SA, pB0, pB1, pA0, pA1, off + KV_TILE, off + KV_TILE);
  O = MFMA32(v0, __builtin_bit_cast(bf16x8, pB0), O); O = MFMA32(v1, __builtin_bit_cast(bf16x8, pB1), O);
  TS(2, 2); TR(2, 5);
  {
    float mx = 0.f;
#pragma unroll
    for (int i = 0; i < 11; ++i) mx = fmaxf(mx, fabsf(O[i]));
    bool bad = !(mx < 1.2676506e30f);
#pragma unroll
    for (int i = 0; i < 11; ++i) bad |= (O[i] != O[i]);
    if (__any(bad)) {
      float m = -3.0e38f; O = f32x16{};
#pragma unroll 1
      for (int t = 0; t < 64; ++t) {
        const int of = t * KV_TILE;
        const bf16x8 a0 = LDS_RD128(kp0 + of); const u32x4 a1 = {LDS_RD32(kp1 + of), 0, 0, 0};
        f32x16 s = {}; s = MFMA32(a0, qf0, s); s = MFMA32(__builtin_bit_cast(bf16x8, a1), qf1, s);
        float tm = s[0];
#pragma unroll
        for (int i = 1; i < 16; ++i) tm = fmaxf(tm, s[i]);
        auto rr = __builtin_amdgcn_permlane32_swap(__float_as_uint(tm), __float_as_uint(tm), false, false);
        tm = fmaxf(__uint_as_float(rr[0]), __uint_as_float(rr[1]));
        const float mn = fmaxf(m, tm), al = __builtin_amdgcn_exp2f(m - mn); m = mn;
#pragma unroll
        for (int i = 0; i < 16; ++i) { O[i] *= al; s[i] -= mn; }
        u32x4 p0, p1;
#pragma unroll
        for (int i = 0; i < 4; ++i) { p0[i] = EX2(s, 2 * i); p1[i] = EX2(s, 8 + 2 * i); }
        O = MFMA32(LDS_RD128(vp + of), __builtin_bit_cast(bf16x8, p0), O); O = MFMA32(LDS_RD128(vp + of + KV_VS), __builtin_bit_cast(bf16x8, p1), O);
      }
    }
  }
  {
    float l = O[10];
    { auto rr = __builtin_amdgcn_permlane32_swap(__float_as_uint(l), __float_as_uint(l), false, false); l = __uint_as_float(rr[0]); }
    const float inv = 1.f / l;
    const size_t ob = WS_O + (size_t)(qb * 256 + w * 32 + r32) * 1536 + h * 48;
    const u32x4 c0 = {pk2(O[0] * inv, O[1] * inv), pk2(O[2] * inv, O[3] * inv), pk2(O[4] * inv, O[5] * inv), pk2(O[6] * inv, O[7] * inv)};
    st16_wt(ws, ob + hh * 16, c0);
    if (hh == 0) st16_wt(ws, ob + 32, u32x4{pk2(O[8] * inv, O[9] * inv), 0, 0, 0});
  }
  TS(2, 3);
  }
  WAITV(0); __syncthreads();
  if (tid == 0) flag_add(ws, F_O + qb);
  TS(2, 7);
}

constexpr int G_STG = 160 * 128, G_W = 8192, G_NS = 8, G_LDS = G_NS * G_STG;
template <int K, bool STATS, int KID>
DI void g_body(char* ws, char* lds, const u16* __restrict__ A, const char* __restrict__ W, const u16* __restrict__ res, u16* __restrict__ out, float* __restrict__ st2, int b) {
  constexpr int NT = K / 64;
  const int lane = lane_id(); const int w = __builtin_amdgcn_readfirstlane(threadIdx.x >> 6);
  const int xc = b & 7, idx = b >> 3, rt = xc * 4 + (idx & 3), ct = idx >> 2;
  const int m0 = rt * 64, n0 = ct * 96;
  TS(KID, 0); TS(KID, 6);
  const char* Xb = (const char*)(A + (size_t)m0 * K); const char* Wb = W + (size_t)ct * NT * 12288;
  const int lw = w - 4;
  const char* xs = Xb + tn_off(lw * 8 + (lane >> 3), lane, K * 2); const char* wsrc = Wb + lw * 1024 + lane * 16;
  auto issW = [&](int kt, int stage) { glds16a<16>(wsrc + (long)kt * 12288, lds + stage * G_STG + (8 + lw) * 1024); if (lw < 4) glds16a<16>(wsrc + (long)kt * 12288 + 8192, lds + stage * G_STG + (16 + lw) * 1024); };
  auto issX = [&](int kt, int stage) { glds16a<16>(xs + (long)kt * 128, lds + stage * G_STG + lw * 1024); };
  if (STATS) { if (w == 11) flag_wait(ws, F_W, 1024); __syncthreads(); }
  if (w >= 4) {
#pragma unroll
    for (int t = 0; t < 7; ++t) issW(t, t);
  }
  if (w == 11) { if (STATS) flag_wait(ws, F_O + (rt >> 2), 32); else flag_wait(ws, F_U + (rt >> 1), 16); }
  __syncthreads();
  if (w >= 4) {
    __builtin_amdgcn_s_setprio(2);
#pragma unroll
    for (int t = 0; t < 7; ++t) issX(t, t);
    WAITV(6); BAR();
#pragma unroll
    for (int kt = 0; kt < 6; ++kt) {
      constexpr int dummy = 0; (void)dummy;
      const int imax = kt + 6 < NT - 1 ? kt + 6 : NT - 1;
      switch ((5 - kt) + 2 * (imax - 6)) { case 5: WAITV(5); break; case 6: WAITV(6); break; case 7: WAITV(7); break; case 8: WAITV(8); break; case 9: WAITV(9); break; default: WAITV(10); break; }
      BAR();
      if (kt + 7 < NT) { issX(kt + 7, (kt + 7) & 7); issW(kt + 7, (kt + 7) & 7); }
    }
#pragma unroll 1
    for (int kt = 6; kt + 7 < NT; ++kt) { WAITV(10); BAR(); issX(kt + 7, (kt + 7) & 7); issW(kt + 7, (kt + 7) & 7); }
    {
      constexpr int K0 = NT - 7 > 6 ? NT - 7 : 6;
#pragma unroll
      for (int kt = K0; kt < NT; ++kt) {
        const int later = 2 * (NT - 2 - kt);
        if (later >= 10) WAITV(10); else if (later >= 8) WAITV(8); else if (later >= 6) WAITV(6); else if (later >= 4) WAITV(4); else if (later >= 2) WAITV(2); else WAITV(0);
        BAR();
      }
    }
    __builtin_amdgcn_s_setprio(0);
  } else {
  const int wr = w & 1, wc = w >> 1, fr = lane & 15, fq = lane >> 4, sw = (fr >> 1) & 7;
  lds_cptr L = (lds_cptr)lds;
  lds_cptr x0 = L + (32 * wr + fr) * 128 + ((fq ^ sw) << 4), x1 = L + (32 * wr + fr) * 128 + (((4 + fq) ^ sw) << 4);
  lds_cptr w0 = L + G_W + (48 * wc + fr) * 128 + ((fq ^ sw) << 4), w1 = L + G_W + (48 * wc + fr) * 128 + (((4 + fq) ^ sw) << 4);
  f32x4 acc[3][2] = {};
  bf16x8 xa[2], wa[3], xb[2], wb_[3];
#define GRD(X, Wf, st, ks) do { X[0] = LDS_RD128(((ks) ? x1 : x0) + (st)); X[1] = LDS_RD128(((ks) ? x1 : x0) + (st) + 2048); \
    _Pragma("unroll") for (int n = 0; n < 3; ++n) Wf[n] = LDS_RD128(((ks) ? w1 : w0) + (st) + n * 2048); } while (0)
#define GSCHED() do { _Pragma("unroll") for (int i_ = 0; i_ < 5; ++i_) { SGB(0x8, 1); SGB(0x100, 1); } SGB(0x8, 1); } while (0)
#define GMM(X, Wf) do { _Pragma("unroll") for (int n = 0; n < 3; ++n) { acc[n][0] = MFMA16(Wf[n], X[0], acc[n][0]); acc[n][1] = MFMA16(Wf[n], X[1], acc[n][1]); } } while (0)
  BAR();
  TS(KID, 1);
  GRD(xa, wa, 0, 0);
  u32x2 rv[2][3];
#pragma unroll
  for (int m = 0; m < 2; ++m)
#pragma unroll
    for (int n = 0; n < 3; ++n) rv[m][n] = ld8_c(res + (size_t)(m0 + 32 * wr + 16 * m + fr) * D + n0 + 48 * wc + 16 * n + 4 * fq);
  int st = 0;
#pragma unroll 1
  for (int kt = 0; kt < NT; ++kt) {
    const int stn = st + G_STG == G_LDS ? 0 : st + G_STG;
    GRD(xb, wb_, st, 1);
    GMM(xa, wa);
    GSCHED();
    SB(); BAR(); SB();
    GRD(xa, wa, stn, 0);
    GMM(xb, wb_);
    GSCHED();
    SB();
    st = stn;
  }
  TS(KID, 2);
#pragma unroll
  for (int m = 0; m < 2; ++m) {
    const int row = m0 + 32 * wr + 16 * m + fr;
    float sum = 0.f, ssq = 0.f;
#pragma unroll
    for (int n = 0; n < 3; ++n) {
      const int col = n0 + 48 * wc + 16 * n + 4 * fq;
      const u32x2 r = rv[m][n];
      u32x2 o; o[0] = pk2(bflo(r[0]) + acc[n][m][0], bfhi(r[0]) + acc[n][m][1]); o[1] = pk2(bflo(r[1]) + acc[n][m][2], bfhi(r[1]) + acc[n][m][3]);
      if (STATS) st8_wt(out + (size_t)row * D + col, o); else *(u32x2*)(out + (size_t)row * D + col) = o;
      if (STATS) { const float a = bflo(o[0]), bq = bfhi(o[0]), c = bflo(o[1]), d = bfhi(o[1]); sum += (a + bq) + (c + d); ssq += (a * a + bq * bq) + (c * c + d * d); }
    }
    if (STATS) {
      sum += __shfl_xor(sum, 16); sum += __shfl_xor(sum, 32); ssq += __shfl_xor(ssq, 16); ssq += __shfl_xor(ssq, 32);
      if (fq == 0) st8_wt(st2 + ((size_t)row * 12 + ct * 2 + wc) * 2, u32x2{__float_as_uint(sum), __float_as_uint(ssq)});
    }
  }
  TS(KID, 3);
  }
  if (STATS) { WAITV(0); __syncthreads(); if (threadIdx.x == 0) flag_add(ws, F_H + (rt >> 1)); }
  TS(KID, 7);
}
DI void p3_colsum(const u16* __restrict__ w_in, char* __restrict__ ws, char* lds, int b) {
    const int tid = threadIdx.x, c = tid % 9, rg = tid / 9;
    float* red = (float*)lds;
    if (rg < 48) {
      const char* p = (const char*)(w_in + (b - 192) * 72 + c * 8) + (size_t)rg * (F * 2);
      u32x4 v[12];
#pragma unroll
      for (int i = 0; i < 12; ++i) v[i] = *(const u32x4*)(p + (size_t)i * 48 * F * 2);
      float a[8] = {};
#pragma unroll
      for (int i = 0; i < 12; ++i)
#pragma unroll
        for (int e = 0; e < 4; ++e) { a[2 * e] += bflo(v[i][e]); a[2 * e + 1] += bfhi(v[i][e]); }
#pragma unroll
      for (int e = 0; e < 8; ++e) red[rg * 72 + c * 8 + e] = a[e];
    }
    __syncthreads();
    if (tid < 72) { float t = 0.f; for (int g = 0; g < 48; ++g) t += red[g * 72 + tid]; st4_wt((float*)(ws + WS_CS) + (b - 192) * 72 + tid, __float_as_uint(t)); }
    WAITV(0); __syncthreads();
    if (tid == 0) flag_add(ws, F_CS);
}
constexpr unsigned XTAG = 0x9E3779B9u;
DI float dpp_sum16(float v) {
  v += __builtin_bit_cast(float, __builtin_amdgcn_mov_dpp(__builtin_bit_cast(int, v), 0xB1, 0xf, 0xf, true));
  v += __builtin_bit_cast(float, __builtin_amdgcn_mov_dpp(__builtin_bit_cast(int, v), 0x4E, 0xf, 0xf, true));
  v += __builtin_bit_cast(float, __builtin_amdgcn_mov_dpp(__builtin_bit_cast(int, v), 0x141, 0xf, 0xf, true));
  v += __builtin_bit_cast(float, __builtin_amdgcn_mov_dpp(__builtin_bit_cast(int, v), 0x140, 0xf, 0xf, true));
  return v;
}
constexpr int K4_STG = (128 + 288) * 128, K4_W2 = 16384, K4_W1 = 16384 + 18432, K4_NS = 3;
constexpr int K4_TAB = K4_NS * K4_STG;
constexpr int K4_PART = 40960, K4_ST2 = 45056;
constexpr int K4_LDS = K4_TAB + 544 * 4;
template <int NTL>
DI void k4_compute(char* lds, int lane, int w, int pi, int ft, char* ws, unsigned (&z1v)[4 * NTL]) {
  const int wr = w & 3, nb0 = NTL == 5 ? 0 : 5, wc = NTL == 5 ? 0 : 1, fr = lane & 15, fq = lane >> 4, sw = (fr >> 1) & 7;
  lds_cptr L = (lds_cptr)lds;
  lds_cptr x0 = L + (32 * wr + fr) * 128 + ((fq ^ sw) << 4), x1 = L + (32 * wr + fr) * 128 + (((4 + fq) ^ sw) << 4);
  lds_cptr w0 = L + K4_W2 + (16 * nb0 + fr) * 128 + ((fq ^ sw) << 4), w1 = L + K4_W2 + (16 * nb0 + fr) * 128 + (((4 + fq) ^ sw) << 4);
  f32x4 acc2[NTL][2] = {}, acc1[NTL][2] = {};
  bf16x8 xa[2], xb[2], wa[NTL], wb[NTL];
#define RDX(X, P, st) do { X[0] = LDS_RD128(P + (st)); X[1] = LDS_RD128(P + (st) + 2048); } while (0)
#define RDW(Wf, P, st) do { _Pragma("unroll") for (int n = 0; n < NTL; ++n) Wf[n] = LDS_RD128(P + (st) + n * 2048); } while (0)
#define MM2(X, Wf) do { _Pragma("unroll") for (int n = 0; n < NTL; ++n) { acc2[n][0] = MFMA16(X[0], Wf[n], acc2[n][0]); acc2[n][1] = MFMA16(X[1], Wf[n], acc2[n][1]); } } while (0)
#define MM1(X, Wf) do { _Pragma("unroll") for (int n = 0; n < NTL; ++n) { acc1[n][0] = MFMA16(Wf[n], X[0], acc1[n][0]); acc1[n][1] = MFMA16(Wf[n], X[1], acc1[n][1]); } } while (0)
#define SCH(NR) do { _Pragma("unroll") for (int i_ = 0; i_ < (NR); ++i_) { SGB(0x8, 1); SGB(0x100, 1); } SGB(0x8, 2 * NTL - (NR)); SB(); } while (0)
  WAITV(0); WAITL0();
  if (NTL == 4) BAR();
  int st = 0;
#pragma unroll 1
  for (int kt = 0; kt < 9; ++kt) {
    BAR(); SB();
    if (kt == 0) TS(4, 1);
    RDX(xa, x0, st); RDW(wa, w0, st); SB();
    RDW(wb, w0, st + 18432); MM2(xa, wa); SCH(NTL);
    RDX(xb, x1, st); RDW(wa, w1, st); MM1(xa, wb); SCH(NTL + 2);
    BAR(); SB();
    RDW(wb, w1, st + 18432); MM2(xb, wa); SCH(NTL);
    MM1(xb, wb); SB();
    st = st + K4_STG == K4_TAB ? 0 : st + K4_STG;
  }
  if (NTL == 5) BAR();
  TS(4, 4);
  int lz = lane_id();
  const float* tab = (const float*)(lds + K4_TAB); const float* rs = tab; const float* mr = tab + 128; const float* cs = tab + 256;
  float* part = (float*)(lds + K4_PART); const float* st2 = (const float*)(lds + K4_ST2);
  u32x2 z2p[NTL][2];
  {
    const int fr = lz & 15, fq = lz >> 4;
#pragma unroll
    for (int m = 0; m < 2; ++m) {
      const f32x4 r4 = *(const f32x4*)(rs + 32 * wr + 16 * m + 4 * fq), m4 = *(const f32x4*)(mr + 32 * wr + 16 * m + 4 * fq);
      f32x4 su = {0, 0, 0, 0}, sq = {0, 0, 0, 0};
#pragma unroll
      for (int n = 0; n < NTL; ++n) {
        const float c = cs[16 * (nb0 + n) + fr];
        f32x4 g;
#pragma unroll
        for (int r = 0; r < 4; ++r) { g[r] = gelu_t(acc2[n][m][r] * r4[r] - m4[r] * c); su[r] += g[r]; sq[r] = __builtin_fmaf(g[r], g[r], sq[r]); }
        z2p[n][m][0] = pk2(g[0], g[1]); z2p[n][m][1] = pk2(g[2], g[3]); PIN(z2p[n][m]);
      }
#pragma unroll
      for (int r = 0; r < 4; ++r) { su[r] = dpp_sum16(su[r]); sq[r] = dpp_sum16(sq[r]); }
      if (fr == 0) {
#pragma unroll
        for (int r = 0; r < 4; ++r) *(f32x2_t*)(part + (wc * 128 + 32 * wr + 16 * m + 4 * fq + r) * 2) = f32x2_t{su[r], sq[r]};
      }
    }
  }
  WAITL0(); BAR();
  if (wc == 0 && lz < 32) {
    const int row = 32 * wr + lz; const f32x2_t p0 = *(const f32x2_t*)(part + row * 2), p1 = *(const f32x2_t*)(part + (128 + row) * 2);
    const u32x4 g = {__float_as_uint(p0[0] + p1[0]), XTAG, __float_as_uint(p0[1] + p1[1]), XTAG};
    st16_wt(ws, WS_XS + (size_t)(((pi * 16 + ft) * 128 + row) * 16), g);
  }
  TS(4, 2);
  {
    const int fr = lz & 15, fq = lz >> 4;
    const float r0 = rs[32 * wr + fr], mm0 = mr[32 * wr + fr], r1 = rs[32 * wr + 16 + fr], mm1 = mr[32 * wr + 16 + fr];
#pragma unroll
    for (int n = 0; n < NTL; ++n) {
      const f32x4 c4 = *(const f32x4*)(cs + 144 + 16 * (nb0 + n) + 4 * fq);
      float g0[4], g1[4];
#pragma unroll
      for (int r = 0; r < 4; ++r) { g0[r] = gelu_t(acc1[n][0][r] * r0 - mm0 * c4[r]); g1[r] = gelu_t(acc1[n][1][r] * r1 - mm1 * c4[r]); }
      z1v[4 * n] = pk2(g0[0], g0[1]); z1v[4 * n + 1] = pk2(g0[2], g0[3]); z1v[4 * n + 2] = pk2(g1[0], g1[1]); z1v[4 * n + 3] = pk2(g1[2], g1[3]);
      PIN(z1v[4 * n]); PIN(z1v[4 * n + 1]); PIN(z1v[4 * n + 2]); PIN(z1v[4 * n + 3]);
    }
  }
  TS(4, 5);
  __syncthreads();
  asm volatile("" : "+v"(lz));
#pragma unroll
  for (int m = 0; m < 2; ++m) {
    const int fr = lz & 15, fq = lz >> 4;
    const int sl = 32 * wr + 16 * m + 4 * fq;
    const f32x4 sa = *(const f32x4*)(st2 + sl * 2), sb = *(const f32x4*)(st2 + sl * 2 + 4);
    const float mu[4] = {sa[0], sa[2], sb[0], sb[2]}, rr[4] = {sa[1], sa[3], sb[1], sb[3]};
#pragma unroll
    for (int n = 0; n < NTL; ++n) {
      const int f = 16 * (nb0 + n) + fr; const u32x2 p = z2p[n][m];
      u32x2 o; o[0] = pk2(bflo(p[0]) * rr[0] - mu[0], bfhi(p[0]) * rr[1] - mu[1]); o[1] = pk2(bflo(p[1]) * rr[2] - mu[2], bfhi(p[1]) * rr[3] - mu[3]);
      *(u32x2*)(lds + (sl >> 6) * 18432 + f * 128 + ((((sl & 63) >> 3) ^ ((f >> 1) & 7)) << 4) + (sl & 4) * 2) = o;
    }
  }
}
template <int NTL>
DI void k5_compute(char* lds, int lane, int w, int tt, int ft, const u16* __restrict__ bsp, char* ws, const unsigned (&z1v)[4 * NTL]) {
  const int wr = w & 3, nb0 = NTL == 5 ? 0 : 5, fr = lane & 15, fq = lane >> 4;
  const C8Addr a = c8_addr<false>(lds, lane, wr, nb0);
  f32x4 acc[NTL][2] = {};
  C8Frag<NTL> f0, f1;
  WAITV(0); BAR();
  TS(5, 1);
  c8_read<NTL, false>(f0, a, 0, 0);
  float bb[2];
#pragma unroll
  for (int m = 0; m < 2; ++m) bb[m] = bf2f(bsp[tt * 128 + 32 * wr + 16 * m + fr]);
  int st = 0;
#define C5_ITER(SIDE0, SIDE1) do { const int stn = st + C8_STG == C8_LDS ? 0 : st + C8_STG; \
    c8_read<NTL, false>(f1, a, st, 1); c8_mma<NTL, true>(acc, f0); c8_sched<NTL, false>(); SB(); SIDE0; SB(); BAR(); SB(); \
    c8_read<NTL, false>(f0, a, stn, 0); c8_mma<NTL, true>(acc, f1); c8_sched<NTL, false>(); SB(); SIDE1; SB(); st = stn; } while (0)
#pragma unroll 1
  for (int kt = 0; kt < 32; ++kt) C5_ITER((void)0, (void)0);
  TS(5, 2);
  __syncthreads();
#pragma unroll
  for (int m = 0; m < 2; ++m) {
    const int t = 32 * wr + 16 * m + fr;
#pragma unroll
    for (int n = 0; n < NTL; ++n) {
      const int f = 16 * (nb0 + n) + 4 * fq; const unsigned z0 = z1v[4 * n + 2 * m], z1 = z1v[4 * n + 2 * m + 1];
      u32x2 o; o[0] = pk2(bflo(z0) * (acc[n][m][0] + bb[m]), bfhi(z0) * (acc[n][m][1] + bb[m]));
      o[1] = pk2(bflo(z1) * (acc[n][m][2] + bb[m]), bfhi(z1) * (acc[n][m][3] + bb[m]));
      *(u32x2*)(lds + t * EP_PITCH + f * 2) = o;
    }
  }
}

DI void p4_copyout(char* ws, char* lds, int w, int pi, int ft) {
  __syncthreads();
  const size_t dst = WS_Z2T + ((size_t)ft * 32 + pi * 2) * 18432;
  for (int i = w * 64 + lane_id(); i < 2304; i += 768) st16_wt(ws, dst + i * 16, *(const u32x4*)(lds + i * 16));
  WAITV(0); __syncthreads();
  if (threadIdx.x == 0) flag_add(ws, F_Z + ft);
  TS(4, 3); TS(4, 7);
}
DI void p5_copyout(char* ws, char* lds, int w, int tt, int ft) {
  __syncthreads();
  const size_t ub = WS_U + ((size_t)tt * 128 * FH + ft * 144) * 2;
  for (int i = w * 64 + lane_id(); i < 128 * 18; i += 768) { const int t = i / 18, c = i - t * 18; st16_wt(ws, ub + (size_t)t * FH * 2 + c * 16, *(const u32x4*)(lds + t * EP_PITCH + c * 16)); }
  WAITV(0); __syncthreads();
  if (threadIdx.x == 0) flag_add(ws, F_U + tt);
  TS(5, 3); TS(5, 7);
}
DI void p45(const u16* __restrict__ wsp, const u16* __restrict__ bsp, char* __restrict__ ws, char* lds) {
  const int lane = lane_id(); const int w = __builtin_amdgcn_readfirstlane(threadIdx.x >> 6); const int tid = w * 64 + lane;
  const int b = blockIdx.x, xc = b & 7, idx = b >> 3;
  const int pi = (xc & 1) * 8 + (idx & 7), ft = (xc >> 1) * 4 + (idx >> 3);
  float* tab = (float*)(lds + K4_TAB);
  TS(4, 0); TS(4, 6);
  const char* Xb = ws + WS_H + (size_t)pi * 128 * D * 2; const char* W2b = ws + WS_WIN + (size_t)ft * 9 * 18432; const char* W1b = W2b + (size_t)16 * 9 * 18432;
  auto src = [&](int p) { return p < 16 ? Xb + tn_off(p * 8 + (lane >> 3), lane, D * 2) : (p < 34 ? W2b + (p - 16) * 1024 : W1b + (p - 34) * 1024) + lane * 16; };
  if (b >= 192) { if (w == 11) flag_wait(ws, F_W, 1024); __syncthreads(); }
#pragma unroll
  for (int j = 0; j < 3; ++j) glds16a<16>(src(16 + w + 12 * j), lds + (16 + w + 12 * j) * 1024);
  if (w == 11) { flag_wait(ws, F_H + pi, 12); flag_wait(ws, F_CS, 64); }
  __syncthreads();
  glds16a<16>(src(w), lds + w * 1024);
  if (w < 4) glds16a<16>(src(12 + w), lds + (12 + w) * 1024);
  const char* Xb5 = (const char*)(wsp + (size_t)pi * 128 * S); const char* Wb5 = ws + WS_Z2T + (size_t)ft * 32 * 18432;
  auto src5 = [&](int p) { return p < 16 ? Xb5 + tn_off(p * 8 + (lane >> 3), lane, S * 2) : Wb5 + (p - 16) * 1024 + lane * 16; };
  if (w >= 8) {
    const int lw = w - 8;
    __builtin_amdgcn_s_setprio(2);
    {
      const char* base[13];
#pragma unroll
      for (int j = 0; j < 13; ++j) base[j] = src(lw + 4 * j);
      auto issue = [&](int kt, int stage) {
#pragma unroll
        for (int j = 0; j < 13; ++j) glds16a<16>(base[j] + (j < 4 ? (long)kt * 128 : (long)kt * 18432), lds + stage * K4_STG + (lw + 4 * j) * 1024);
      };
      issue(1, 1);
      int stg = 2;
#pragma unroll 1
      for (int kt = 0; kt < 7; ++kt) { WAITV(13); BAR(); BAR(); issue(kt + 2, stg); stg = stg == 2 ? 0 : stg + 1; }
      WAITV(13); BAR(); BAR();
      WAITV(0); BAR(); BAR();
      BAR();
      BAR();
      const int row = 32 * lw + (lane >> 1);
      const __amdgpu_buffer_rsrc_t rs_ = __builtin_amdgcn_make_buffer_rsrc((void*)(ws + WS_XS), 0, 2048 * 16 * 16, 0x00020000);
      const unsigned vo = (unsigned)(((pi * 16 + (lane & 1) * 8) * 128 + row) * 16);
      float sum = 0.f, ssq = 0.f;
      for (int spin = 0; spin < (1 << 20); ++spin) {
        bool ok = true; sum = 0.f; ssq = 0.f;
        u32x4 g[8];
#pragma unroll
        for (int t = 0; t < 8; ++t) g[t] = __builtin_amdgcn_raw_buffer_load_b128(rs_, vo + t * 2048, 0, 16);
#pragma unroll
        for (int t = 0; t < 8; ++t) { ok &= g[t][1] == XTAG && g[t][3] == XTAG; sum += __uint_as_float(g[t][0]); ssq += __uint_as_float(g[t][2]); }
        if (__all(ok)) break;
        __builtin_amdgcn_s_sleep(8);
      }
      sum += __shfl_xor(sum, 1); ssq += __shfl_xor(ssq, 1);
      const float mean = sum * (1.f / FH), var = fmaxf(ssq * (1.f / FH) - mean * mean, 0.f), rstd = rsqrtf(var + 1e-5f);
      if ((lane & 1) == 0) *(f32x2_t*)(lds + K4_ST2 + row * 8) = f32x2_t{mean * rstd, rstd};
      __syncthreads();
    }
    p4_copyout(ws, lds, w, pi, ft);
    TS(5, 0); TS(5, 6);
    glds16(src5(w), lds + w * 1024); if (w < 4) glds16(src5(12 + w), lds + (12 + w) * 1024);
    if (w == 11) flag_wait(ws, F_Z + ft, 16);
    __syncthreads();
    glds16a<16>(src5(16 + w), lds + (16 + w) * 1024); if (w < 6) glds16a<16>(src5(28 + w), lds + (28 + w) * 1024);
    loader_loop<32, C8_NS, C8_STG, 34, 16, 4, 0, 16>(lds, lw, src5, [&](int kt, long& ox, long& ow) { ox = (long)kt * 128; ow = (long)kt * 18432; }, NoPost());
    __builtin_amdgcn_s_setprio(0);
    __syncthreads();
    p5_copyout(ws, lds, w, pi, ft);
  } else {
    if (tid < 128) {
      const char* p = ws + WS_ST2 + (size_t)(pi * 128 + tid) * 96;
      float sum = 0.f, ssq = 0.f;
#pragma unroll
      for (int t = 0; t < 12; ++t) { const u32x2 v = ld8_c(p + t * 8); sum += __uint_as_float(v[0]); ssq += __uint_as_float(v[1]); }
      const float mean = sum * (1.f / D), var = fmaxf(ssq * (1.f / D) - mean * mean, 0.f), rstd = rsqrtf(var + 1e-5f);
      tab[tid] = rstd; tab[128 + tid] = mean * rstd;
    } else if (tid < 416) {
      const int c = tid - 128;
      tab[256 + c] = ld4f_c((const float*)(ws + WS_CS) + (c < 144 ? FH + ft * 144 + c : ft * 144 + c - 144));
    }
#define P45_PATH(NTL) do { unsigned z1v[4 * NTL]; \
      k4_compute<NTL>(lds, lane, w, pi, ft, ws, z1v); \
      p4_copyout(ws, lds, w, pi, ft); \
      TS(5, 0); TS(5, 6); \
      glds16(src5(w), lds + w * 1024); if (w < 4) glds16(src5(12 + w), lds + (12 + w) * 1024); \
      __syncthreads(); \
      glds16a<16>(src5(16 + w), lds + (16 + w) * 1024); if (w < 6) glds16a<16>(src5(28 + w), lds + (28 + w) * 1024); \
      k5_compute<NTL>(lds, lane_id(), w, pi, ft, bsp, ws, z1v); \
      p5_copyout(ws, lds, w, pi, ft); } while (0)
    if (w < 4) P45_PATH(5); else P45_PATH(4);
  }
}

__global__ __launch_bounds__(768, 3) void mega_kernel(const u16* __restrict__ x, const u16* __restrict__ wo, const u16* __restrict__ w_in, const u16* __restrict__ w_sp,
                                                      const u16* __restrict__ b_sp, const u16* __restrict__ w_out, u16* __restrict__ out, char* __restrict__ ws) {
  extern __shared__ __attribute__((aligned(16))) char lds[];
  const int b = blockIdx.x;
  p2_attention(wo, w_in, w_out, w_sp, ws, lds);
  if (b < 192) g_body<768, true, 3>(ws, lds, (const u16*)(ws + WS_O), ws + WS_WO, x, (u16*)(ws + WS_H), (float*)(ws + WS_ST2), b);
  else p3_colsum(w_in, ws, lds, b);
  __syncthreads();
  p45(w_sp, b_sp, ws, lds);
  if (b < 192) g_body<FH, false, 0>(ws, lds, (const u16*)(ws + WS_U), ws + WS_WOUT, (const u16*)(ws + WS_H), out, nullptr, b);
}

extern "C" void kernel_launch(void* const* d_in, const int* in_sizes, int n_in, void* d_out, int out_size, void* d_ws, size_t ws_size, hipStream_t stream) {
  const u16* x = (const u16*)d_in[0]; const u16* wqkv = (const u16*)d_in[1]; const u16* wo = (const u16*)d_in[2];
  const u16* w_in = (const u16*)d_in[4]; const u16* w_sp = (const u16*)d_in[5]; const u16* b_sp = (const u16*)d_in[6]; const u16* w_out = (const u16*)d_in[7];
  u16* out = (u16*)d_out; char* ws = (char*)d_ws;
  static bool attr = false;
  if (!attr) {
    (void)hipFuncSetAttribute((const void*)k1_kernel, hipFuncAttributeMaxDynamicSharedMemorySize, K1_LDS);
    (void)hipFuncSetAttribute((const void*)mega_kernel, hipFuncAttributeMaxDynamicSharedMemorySize, 163840);
    attr = true;
  }
  k1_kernel<<<256, K1_THREADS, K1_LDS, stream>>>(x, wqkv, ws);
  mega_kernel<<<256, 768, 163840, stream>>>(x, wo, w_in, w_sp, b_sp, w_out, out, ws);
}
```

```cpp
#include <hip/hip_runtime.h>
#include <hip/hip_bf16.h>
#include <stdint.h>
typedef unsigned short u16;
using bf16x8 = __attribute__((ext_vector_type(8))) short;
using s16x4 = __attribute__((ext_vector_type(4))) short;
using f32x4 = __attribute__((ext_vector_type(4))) float;
using f32x16 = __attribute__((ext_vector_type(16))) float;
using u32x4 = __attribute__((ext_vector_type(4))) unsigned;
using u32x2 = __attribute__((ext_vector_type(2))) unsigned;
typedef __bf16 bf16x2_t __attribute__((ext_vector_type(2)));
typedef float f32x2_t __attribute__((ext_vector_type(2)));
constexpr int S = 2048, D = 576, H = 32, DH = 18, F = 4608, FH = 2304;
#define DI __device__ __forceinline__
#define LAS __attribute__((address_space(3)))
#define GAS __attribute__((address_space(1)))
DI int lane_id() { int l = __builtin_amdgcn_mbcnt_hi(~0u, __builtin_amdgcn_mbcnt_lo(~0u, 0u)); asm volatile("" : "+v"(l)); return l; }
DI float bf2f(u16 x) { return __uint_as_float((unsigned)x << 16); }
DI float bflo(unsigned x) { return __uint_as_float(x << 16); }
DI float bfhi(unsigned x) { return __uint_as_float(x & 0xffff0000u); }
DI unsigned pk2(float lo, float hi) { f32x2_t v = {lo, hi}; return __builtin_bit_cast(unsigned, __builtin_convertvector(v, bf16x2_t)); }
#define MFMA32(a, b, c) __builtin_amdgcn_mfma_f32_32x32x16_bf16((a), (b), (c), 0, 0, 0)
#define MFMA16(a, b, c) __builtin_amdgcn_mfma_f32_16x16x32_bf16((a), (b), (c), 0, 0, 0)
#define SB() __builtin_amdgcn_sched_barrier(0)
#define SGB(mask, n) __builtin_amdgcn_sched_group_barrier(mask, n, 0)
#define PIN(x) asm volatile("" : "+v"(x))
#define BAR() __builtin_amdgcn_s_barrier()
#define WAITV(n) asm volatile("s_waitcnt vmcnt(%0)" :: "i"(n) : "memory")
#define WAITL0() asm volatile("s_waitcnt lgkmcnt(0)" ::: "memory")
DI void glds16(const void* g, void* l) { __builtin_amdgcn_global_load_lds((const GAS unsigned*)g, (LAS unsigned*)l, 16, 0, 0); }
template <int AUX> DI void glds16a(const void* g, void* l) { __builtin_amdgcn_global_load_lds((const GAS unsigned*)g, (LAS unsigned*)l, 16, 0, AUX); }
DI void st16_wt(char* ws, size_t off, u32x4 v) {
  const __amdgpu_buffer_rsrc_t r = __builtin_amdgcn_make_buffer_rsrc((void*)ws, 0, 128 << 20, 0x00020000);
  __builtin_amdgcn_raw_buffer_store_b128(v, r, (unsigned)off, 0, 16);
}
typedef LAS const char* lds_cptr;
#define LDS_RD128(p) (*(const LAS bf16x8*)(p))
#define LDS_RD32(p) (*(const LAS unsigned*)(p))
typedef short v4i16_t __attribute__((ext_vector_type(4)));
DI s16x4 lds_tr(lds_cptr p) { return __builtin_bit_cast(s16x4, __builtin_amdgcn_ds_read_tr16_b64_v4i16((LAS v4i16_t*)p)); }
DI bf16x8 lds_tr2(lds_cptr p0, lds_cptr p1) { const s16x4 a = lds_tr(p0), b = lds_tr(p1); return __builtin_shufflevector(a, b, 0, 1, 2, 3, 4, 5, 6, 7); }

constexpr int KV_TILE = 2400, KV_K1 = 1024, KV_V = 1152, KV_VS = 624, KV_Z = KV_V + 608, KV_HEAD = 64 * KV_TILE;
constexpr int Q_TILE = 1152, Q_HEAD = 64 * Q_TILE;
constexpr size_t WS_KV = 0;
constexpr size_t WS_Q = 5u << 20;
constexpr size_t WS_O = 8u << 20;
constexpr size_t WS_H = 11u << 20;
constexpr size_t WS_ST2 = 14u << 20;
constexpr size_t WS_CS = 15u << 20;
constexpr size_t WS_XS = 16u << 20;
constexpr size_t WS_FLAG = WS_XS + (512u << 10);
constexpr size_t WS_Z2T = 17u << 20;
constexpr size_t WS_U = 37u << 20;
constexpr size_t WS_WIN = 47u << 20;
constexpr size_t WS_WO = 53u << 20;
constexpr size_t WS_WOUT = 54u << 20;
constexpr size_t WS_DBG = 64u << 20;

constexpr int F_O = 0, F_W = 8, F_H = 9, F_CS = 25, F_Z = 26, F_U = 42;
typedef GAS unsigned gu32;
DI gu32* flagp(char* ws, int i) { return (gu32*)(ws + WS_FLAG + (size_t)i * 256); }
DI void flag_add(char* ws, int i) { __hip_atomic_fetch_add(flagp(ws, i), 1u, __ATOMIC_RELAXED, __HIP_MEMORY_SCOPE_AGENT); }
DI void flag_wait(char* ws, int i, unsigned target) {
  gu32* p = flagp(ws, i);
  for (int spin = 0; spin < (1 << 20); ++spin) {
    if ((unsigned)__builtin_amdgcn_readfirstlane(__hip_atomic_load(p, __ATOMIC_RELAXED, __HIP_MEMORY_SCOPE_AGENT)) >= target) break;
    __builtin_amdgcn_s_sleep(2);
  }
}
DI void st4_wt(void* p, unsigned v) { __hip_atomic_store((gu32*)p, v, __ATOMIC_RELAXED, __HIP_MEMORY_SCOPE_AGENT); }
DI void st8_wt(void* p, u32x2 v) { __hip_atomic_store((GAS unsigned long long*)p, ((unsigned long long)v[1] << 32) | v[0], __ATOMIC_RELAXED, __HIP_MEMORY_SCOPE_AGENT); }
DI u32x2 ld8_c(const void* p) { const unsigned long long v = __hip_atomic_load((GAS unsigned long long*)p, __ATOMIC_RELAXED, __HIP_MEMORY_SCOPE_AGENT); return u32x2{(unsigned)v, (unsigned)(v >> 32)}; }
DI float ld4f_c(const void* p) { return __uint_as_float(__hip_atomic_load((gu32*)p, __ATOMIC_RELAXED, __HIP_MEMORY_SCOPE_AGENT)); }
template <int NP, int NW, int NPX = 0, int AX = 0, int AW = 0, class SRC>
DI void first_tile(char* lds, int w, SRC src) {
#pragma unroll
  for (int j = 0; j < (NP + NW - 1) / NW; ++j) { const int p = w + NW * j; if (p < NP) { if (p < NPX) glds16a<AX>(src(p), lds + p * 1024); else glds16a<AW>(src(p), lds + p * 1024); } }
}
template <int NT, int NS, int STG, int NP, int NPX, int NLD, int AX = 0, int AW = 0, class SRC, class OFF, class POST>
DI void loader_loop(char* lds, int lw, SRC src, OFF off, POST post) {
  constexpr int PPW = (NP + NLD - 1) / NLD, PMIN = NP / NLD;
  static_assert((NS - 1) * PPW + 4 <= 63, "vmcnt");
  static_assert(NPX % NLD == 0, "X pieces");
  const char* base[PPW];
#pragma unroll
  for (int j = 0; j < PPW; ++j) { const int p = lw + NLD * j; base[j] = src(p < NP ? p : NP - 1); }
  auto issue = [&](int kt, int stage) {
    long ox, ow; off(kt, ox, ow);
#pragma unroll
    for (int j = 0; j < PPW; ++j) if (j < PMIN || lw + NLD * j < NP) {
      if (j < NPX / NLD) glds16a<AX>(base[j] + ox, lds + stage * STG + (lw + NLD * j) * 1024); else glds16a<AW>(base[j] + ow, lds + stage * STG + (lw + NLD * j) * 1024); }
  };
  issue(1, 1);
  WAITV(PMIN);
  BAR();
#pragma unroll
  for (int t = 2; t < NS - 1; ++t) issue(t, t);
  post(0);
  int stg = NS - 1, pst = 1;
#pragma unroll 1
  for (int kt = 0; kt < NT - (NS - 1); ++kt) {
    WAITV((NS - 3) * PMIN);
    BAR();
    issue(kt + NS - 1, stg); stg = stg + 1 == NS ? 0 : stg + 1;
    post(pst); pst = pst + 1 == NS ? 0 : pst + 1;
  }
#pragma unroll
  for (int r = NS - 2; r >= 0; --r) {
    WAITV((r - 1 > 0 ? r - 1 : 0) * PMIN); BAR();
    if (r > 0) { post(pst); pst = pst + 1 == NS ? 0 : pst + 1; }
  }
}
struct NoPost { DI void operator()(int) const {} };
DI unsigned tn_off(int row, int lane, int ld_bytes) { return (unsigned)row * ld_bytes + ((((lane & 7) ^ ((row >> 1) & 7))) << 4); }
template <int BN> DI unsigned kn_off(int p, int lane, int ld_bytes) {
  constexpr int CPR = BN / 8;
  const int c = 64 * p + lane, kp = c / CPR, cc = c - kp * CPR, odd = (kp >> 3) & 1, k = kp ^ (odd << 2);
  int cs = cc;
  if (BN == 96) { int blk = (cc >> 1) - odd; if (blk < 0) blk += 6; cs = blk * 2 + (cc & 1); }
  return (unsigned)k * ld_bytes + (unsigned)cs * 16;
}

DI float gelu_t(float x) {
  const float c1 = -2.f * 0.7978845608028654f * 1.4426950408889634f, c2 = c1 * 0.044715f;
  const float e = __builtin_amdgcn_exp2f(x * __builtin_fmaf(x * x, c2, c1));
  return x * __builtin_amdgcn_rcpf(1.f + e);
}

constexpr int C8_STG = 272 * 128, C8_W = 16384, C8_NS = 4, C8_LDS = C8_NS * C8_STG;
struct C8Addr { lds_cptr x0, x1, w0, w1; };
template <bool BKN> DI C8Addr c8_addr(char* lds, int lane, int wr, int nb0) {
  lds_cptr L = (lds_cptr)lds; const int fr = lane & 15, fq = lane >> 4, sw = (fr >> 1) & 7; C8Addr a;
  a.x0 = L + (32 * wr + fr) * 128 + ((fq ^ sw) << 4); a.x1 = L + (32 * wr + fr) * 128 + (((4 + fq) ^ sw) << 4);
  if (BKN) { const int q = fr >> 2, p4 = lane & 3, b = C8_W + (8 * fq + q) * 288 + nb0 * 32 + p4 * 8;
             a.w0 = L + b + (fq & 1) * 1152; a.w1 = L + b + ((fq & 1) ^ 1) * 1152; }
  else { a.w0 = L + C8_W + (16 * nb0 + fr) * 128 + ((fq ^ sw) << 4); a.w1 = L + C8_W + (16 * nb0 + fr) * 128 + (((4 + fq) ^ sw) << 4); }
  return a;
}
template <int NTL> struct C8Frag { bf16x8 x[2], w[NTL]; };
template <int NTL, bool BKN> DI void c8_read(C8Frag<NTL>& f, const C8Addr& a, int st, int ks) {
  f.x[0] = LDS_RD128((ks ? a.x1 : a.x0) + st); f.x[1] = LDS_RD128((ks ? a.x1 : a.x0) + st + 2048);
#pragma unroll
  for (int n = 0; n < NTL; ++n) {
    if (BKN) f.w[n] = lds_tr2(a.w0 + st + ks * 9216 + n * 32, a.w1 + st + ks * 9216 + n * 32);
    else f.w[n] = LDS_RD128((ks ? a.w1 : a.w0) + st + n * 2048);
  }
}
template <int NTL, bool WX> DI void c8_mma(f32x4 (&acc)[NTL][2], const C8Frag<NTL>& f) {
#pragma unroll
  for (int n = 0; n < NTL; ++n) {
#pragma unroll
    for (int m = 0; m < 2; ++m) acc[n][m] = WX ? MFMA16(f.w[n], f.x[m], acc[n][m]) : MFMA16(f.x[m], f.w[n], acc[n][m]);
  }
}
template <int NTL, bool BKN> DI void c8_sched() {
  constexpr int NR = 2 + (BKN ? 2 * NTL : NTL), NM = 2 * NTL;
  if (NR <= NM) {
#pragma unroll
    for (int i = 0; i < NR; ++i) { SGB(0x8, 1); SGB(0x100, 1); }
    SGB(0x8, NM - NR);
  } else {
#pragma unroll
    for (int i = 0; i < NM; ++i) { SGB(0x8, 1); SGB(0x100, 1); if (i < NR - NM) SGB(0x100, 1); }
  }
}

constexpr int K1_THREADS = 768;
constexpr int EP_PITCH = 304;
template <int WC, bool WX>
DI void k1_compute(char* lds, int lane, int w, f32x4 (&acc)[WC == 0 ? 3 : 2][4], float* tabs, char* ws) {
  constexpr int NTL = WC == 0 ? 3 : 2, NB0 = WC == 0 ? 0 : 2 * WC + 1, NST = WC == 0 ? 0 : (WC == 1 ? 2 : 1), M0 = WC == 1 ? 0 : WC - 1, M1 = 3;
  const int wr = w & 1, fr = lane & 15, fq = lane >> 4, sw = (fr >> 1) & 7;
  lds_cptr L = (lds_cptr)lds;
  lds_cptr x0 = L + (64 * wr + fr) * 128 + ((fq ^ sw) << 4), x1 = L + (64 * wr + fr) * 128 + (((4 + fq) ^ sw) << 4);
  const int wb = C8_W + (8 * fq + (fr >> 2)) * 288 + NB0 * 32 + (lane & 3) * 8;
  lds_cptr w0 = L + wb + (fq & 1) * 1152, w1 = L + wb + ((fq & 1) ^ 1) * 1152;
  const u32x4 onesv = {0x3f803f80u, 0x3f803f80u, 0x3f803f80u, 0x3f803f80u}; const bf16x8 ones = __builtin_bit_cast(bf16x8, onesv);
  bf16x8 xa[4], wa[NTL], xb[4], wb_[NTL];
  f32x4 cs[NTL] = {}, rsum[2] = {}, rsq[2] = {};
#define K1RD(X, Wf, st, ks) do { _Pragma("unroll") for (int n = 0; n < NTL; ++n) Wf[n] = lds_tr2(w0 + (st) + (ks) * 9216 + n * 32, w1 + (st) + (ks) * 9216 + n * 32); \
    _Pragma("unroll") for (int m = 0; m < 4; ++m) X[m] = LDS_RD128(((ks) ? x1 : x0) + (st) + m * 2048); } while (0)
#define K1MM(X, Wf) do { _Pragma("unroll") for (int n = 0; n < NTL; ++n) { _Pragma("unroll") for (int m = 0; m < 4; ++m) acc[n][m] = WX ? MFMA16(Wf[n], X[m], acc[n][m]) : MFMA16(X[m], Wf[n], acc[n][m]); \
    cs[n] = MFMA16(ones, Wf[n], cs[n]); } \
    if (NST >= 1) { rsum[0] = MFMA16(X[M0], ones, rsum[0]); rsq[0] = MFMA16(X[M0], X[M0], rsq[0]); } \
    if (NST >= 2) { rsum[1] = MFMA16(X[M1], ones, rsum[1]); rsq[1] = MFMA16(X[M1], X[M1], rsq[1]); } } while (0)
#define K1SCHED() do { _Pragma("unroll") for (int i_ = 0; i_ < 2 * NTL + 4; ++i_) { SGB(0x8, 1); SGB(0x100, 1); } SGB(0x8, 5 * NTL + 2 * NST - (2 * NTL + 4)); } while (0)
  WAITV(0); BAR();

  K1RD(xa, wa, 0, 0);
  int st = 0;
#pragma unroll 1
  for (int kt = 0; kt < 9; ++kt) {
    const int stn = st + C8_STG == C8_LDS ? 0 : st + C8_STG;
    K1RD(xb, wb_, st, 1); K1MM(xa, wa); K1SCHED();
    SB(); BAR(); SB();
    K1RD(xa, wa, stn, 0); K1MM(xb, wb_); K1SCHED();
    SB();
    st = stn;
  }

  const int l2 = lane_id(), r2 = l2 & 15, q2 = l2 >> 4;
  if (l2 < 16 && wr == 0) {
#pragma unroll
    for (int n = 0; n < NTL; ++n) tabs[256 + 16 * (NB0 + n) + l2] = cs[n][0];
  }
#pragma unroll
  for (int t = 0; t < NST; ++t) {
    const int mrow = 64 * wr + 16 * (t == 0 ? M0 : M1);
    if (r2 == 0) *(f32x4*)(tabs + mrow + 4 * q2) = rsum[t];
    if (q2 == (r2 >> 2)) tabs[128 + mrow + r2] = (r2 & 2) ? ((r2 & 1) ? rsq[t][3] : rsq[t][2]) : ((r2 & 1) ? rsq[t][1] : rsq[t][0]);
  }
}
template <int NTL>
DI void k1_epilogue(char* lds, int lane, int w, int type, int rt, int hg, const float* tabs, f32x4 (&acc)[NTL][4], char* kvimg) {
  asm volatile("" : "+v"(lane));
  const int wr = w & 1, wc = w >> 1, nb0 = wc == 0 ? 0 : 2 * wc + 1, fr = lane & 15, fq = lane >> 4;
  const float* rs = tabs; const float* mr = tabs + 128; const float* cs = tabs + 256;
  if (type == 2) {
#pragma unroll
    for (int n = 0; n < NTL; ++n) {
      const int col = 16 * (nb0 + n) + fr, hh = col / 18, d = col - hh * 18; const float c = cs[col];
      char* hb = kvimg + (size_t)(hg * 8 + hh) * KV_HEAD + (size_t)(rt * 4 + 2 * wr) * KV_TILE + KV_V + (fq & 1) * 304 + (fq >> 1) * 8 + d * 16;
#pragma unroll
      for (int m = 0; m < 4; ++m) {
        const int row = 64 * wr + 16 * m + 4 * fq;
        const f32x4 r4 = *(const f32x4*)(rs + row), m4 = *(const f32x4*)(mr + row);
        u32x2 o; o[0] = pk2(acc[n][m][0] * r4[0] - m4[0] * c, acc[n][m][1] * r4[1] - m4[1] * c); o[1] = pk2(acc[n][m][2] * r4[2] - m4[2] * c, acc[n][m][3] * r4[3] - m4[3] * c);
        *(u32x2*)(hb + (m >> 1) * KV_TILE + (m & 1) * KV_VS) = o;
      }
    }
  } else {
    const float qs = type == 0 ? 0.23570226039551584f * 1.4426950408889634f : 1.f;
#pragma unroll
    for (int m = 0; m < 4; ++m) {
      const int row = 64 * wr + 16 * m + fr; const float r = rs[row] * qs, mm = mr[row] * qs;
#pragma unroll
      for (int n = 0; n < NTL; ++n) {
        const int col = 16 * (nb0 + n) + 4 * fq; const f32x4 c4 = *(const f32x4*)(cs + col);
        u32x2 o; o[0] = pk2(acc[n][m][0] * r - mm * c4[0], acc[n][m][1] * r - mm * c4[1]); o[1] = pk2(acc[n][m][2] * r - mm * c4[2], acc[n][m][3] * r - mm * c4[3]);
        *(u32x2*)(lds + row * EP_PITCH + col * 2) = o;
      }
    }
  }
}
__global__ __launch_bounds__(K1_THREADS, 3) void k1_kernel(const u16* __restrict__ x, const u16* __restrict__ wqkv, char* __restrict__ ws) {
  extern __shared__ __attribute__((aligned(16))) char lds[];
  const int tid = threadIdx.x, lane = tid & 63; const int w = __builtin_amdgcn_readfirstlane(tid >> 6);
  const int b = blockIdx.x;

  if (b >= 192) {
    if (tid < 512) ((u32x4*)(ws + WS_XS))[(b - 192) * 512 + tid] = u32x4{0, 0, 0, 0};
    else if (tid < 528) ((u32x4*)(ws + WS_FLAG))[(b - 192) * 16 + tid - 512] = u32x4{0, 0, 0, 0};
    return;
  }
  const int xc = b & 7, idx = b >> 3;
  const int rt = xc * 2 + (idx & 1), ct = idx >> 1;
  const int type = ct >> 2, hg = ct & 3;
  float* tabs = (float*)(lds + C8_LDS);
  char* kvimg = ws + WS_KV; char* qimg = ws + WS_Q;
  const char* Xb = (const char*)(x + (size_t)rt * 128 * D); const char* Wb = (const char*)(wqkv + ct * 144);
  auto src = [&](int p) { return p < 16 ? Xb + tn_off(p * 8 + (lane >> 3), lane, D * 2) : Wb + kn_off<144>(p - 16, lane, 1728 * 2); };
  first_tile<34, 12>(lds, w, src);
  if (w >= 8) {
    __builtin_amdgcn_s_setprio(2);
    loader_loop<9, C8_NS, C8_STG, 34, 16, 4>(lds, w - 8, src, [&](int kt, long& ox, long& ow) { ox = (long)kt * 128; ow = (long)kt * 64 * 1728 * 2; }, NoPost());
    __syncthreads();
    const int row = (w - 8) * 64 + lane_id();
    if (row < 128) { const float mean = tabs[row] * (1.f / D), var = fmaxf(tabs[128 + row] * (1.f / D) - mean * mean, 0.f), rstd = rsqrtf(var + 1e-5f); tabs[row] = rstd; tabs[128 + row] = mean * rstd; }
    __syncthreads();
  } else {
#define K1_PATH(WC, WX) do { f32x4 acc[WC == 0 ? 3 : 2][4] = {}; \
      k1_compute<WC, WX>(lds, lane, w, acc, tabs, ws); \
      __syncthreads(); __syncthreads(); \
      k1_epilogue<WC == 0 ? 3 : 2>(lds, lane_id(), w, WX ? (type & 1) : 2, rt, hg, tabs, acc, kvimg); } while (0)
#define K1_SEL(WX) do { if (w < 2) K1_PATH(0, WX); else if (w < 4) K1_PATH(1, WX); else if (w < 6) K1_PATH(2, WX); else K1_PATH(3, WX); } while (0)
    if (type == 2) K1_SEL(false); else K1_SEL(true);
  }
  const int tid2 = w * 64 + lane_id();
  if (type == 2) {
    if (tid2 < 192) {
      const int tl = tid2 / 48, r = tid2 - tl * 48, hh = r / 6, c = r - hh * 6;
      char* tb = kvimg + (size_t)(hg * 8 + hh) * KV_HEAD + (size_t)(rt * 4 + tl) * KV_TILE;
      if (c < 4) *(u32x4*)(tb + KV_V + (c >> 1) * KV_VS + (c & 1) * 304 + 18 * 16) = u32x4{0x3f803f80u, 0x3f803f80u, 0x3f803f80u, 0x3f803f80u};
      else *(u32x4*)(tb + KV_Z + (c - 4) * KV_VS) = u32x4{0, 0, 0, 0};
    }

    return;
  }
  __syncthreads();
  for (int it = tid2; it < 1024; it += K1_THREADS) {
    const int tok = it & 127, hh = it >> 7;
    const unsigned* sp = (const unsigned*)(lds + tok * EP_PITCH + hh * 36);
    u32x4 c0 = {sp[0], sp[1], sp[2], sp[3]}, c1 = {sp[4], sp[5], sp[6], sp[7]}; const unsigned c2 = sp[8];
    char* tb = type == 0 ? qimg + (size_t)(hg * 8 + hh) * Q_HEAD + (size_t)(rt * 4 + (tok >> 5)) * Q_TILE
                         : kvimg + (size_t)(hg * 8 + hh) * KV_HEAD + (size_t)(rt * 4 + (tok >> 5)) * KV_TILE;
    const int key = tok & 31;
    *(u32x4*)(tb + key * 16) = c0; *(u32x4*)(tb + 512 + key * 16) = c1; *(unsigned*)(tb + 1024 + key * 4) = c2;
  }

}
static_assert(128 * EP_PITCH <= C8_LDS, "");
constexpr int K1_LDS = C8_LDS + 400 * 4;

constexpr int ATT_LDS = KV_HEAD + 8 * Q_TILE;
#define SWITCH 50
DI void p2_attention(const u16* __restrict__ wo, const u16* __restrict__ w_in, const u16* __restrict__ w_out, const u16* __restrict__ w_sp, char* __restrict__ ws, char* lds) {

  const char* kvimg = ws + WS_KV; const char* qimg = ws + WS_Q; u16* o = (u16*)(ws + WS_O);
  const int tid = threadIdx.x, lane = tid & 63; const int w = __builtin_amdgcn_readfirstlane(tid >> 6);
  const int b = blockIdx.x, xcd = b & 7, idx = b >> 3, h = xcd * 4 + (idx >> 3), qb = idx & 7;
  const char* kvg = kvimg + (size_t)h * KV_HEAD + lane * 16;
  if (w < 8) {
    const char* qg = qimg + (size_t)h * Q_HEAD + (qb * 8 + w) * Q_TILE + lane * 16;
    char* qtile = lds + KV_HEAD + w * Q_TILE;
    glds16(qg, qtile);
    if (lane < 8) glds16(qg + 1024, qtile + 1024);
  }
  glds16(kvg + w * 1024, lds + w * 1024); glds16(kvg + (w + 12) * 1024, lds + (w + 12) * 1024);
  WAITV(0);
  BAR();
  if (w >= 8) {
    const int lw = w - 8;
    __builtin_amdgcn_s_setprio(3);
#pragma unroll 1
    for (int p = 24 + lw; p < 150; p += 4) glds16(kvg + p * 1024, lds + p * 1024);
    WAITV(0);
    BAR();
    {
      const u32x4* wp = (const u32x4*)w_sp + (size_t)(b * 4 + lw) * 512 + lane;
      u32x4 acc = wp[0];
#pragma unroll
      for (int i = 1; i < 8; ++i) acc ^= wp[i * 64];
      if (acc[0] == 0x12345678u && acc[1] == 0x9abcdef0u && acc[2] == 0x0fedcba9u && acc[3] == 0x87654321u) ((unsigned*)(ws + WS_DBG))[0] = 1;
    }
    const int gw = b * 4 + lw, c = lane & 7, nb = lane >> 3;
    if (nb < 6) {
#pragma unroll 1
      for (int blk = gw; blk < 864 + 144 + 432; blk += 1024) {
        const u16* sp; int ld, kt, n0; size_t db;
        if (blk < 864) { kt = blk / 96; const int nbk = blk - kt * 96, z2 = nbk >= 48, nn = z2 ? nbk - 48 : nbk, t = nn / 3; n0 = nbk * 48; sp = w_in; ld = F;
          db = WS_WIN + ((size_t)((z2 ? 0 : 16) + t) * 9 + kt) * 18432 + (nn - t * 3) * 48 * 128; }
        else if (blk < 1008) { const int q = blk - 864; kt = q / 12; const int nbk = q - kt * 12; n0 = nbk * 48; sp = wo; ld = D; db = WS_WO + ((size_t)(nbk >> 1) * 12 + kt) * 12288 + (nbk & 1) * 48 * 128; }
        else { const int q = blk - 1008; kt = q / 12; const int nbk = q - kt * 12; n0 = nbk * 48; sp = w_out; ld = D; db = WS_WOUT + ((size_t)(nbk >> 1) * 36 + kt) * 12288 + (nbk & 1) * 48 * 128; }
        u32x4 r[8];
        if (blk >= 864 && blk < 1008) {
          const int kc = kt * 8 + c, hh = kc / 3, part = kc - hh * 3;
          const u16* sq = sp + (size_t)(hh * DH) * ld + n0 + nb * 8;
#pragma unroll
          for (int j = 0; j < 8; ++j) {
            const int d = part == 2 ? 16 + j : (j & 3) + 8 * (j >> 2) + 4 * part;
            r[j] = (part == 2 && j >= 2) ? u32x4{0, 0, 0, 0} : *(const u32x4*)(sq + (size_t)(d < DH ? d : 0) * ld);
          }
        } else {
          sp += (size_t)(kt * 64 + c * 8) * ld + n0 + nb * 8;
#pragma unroll
          for (int j = 0; j < 8; ++j) r[j] = *(const u32x4*)(sp + (size_t)j * ld);
        }
        const unsigned lo = (unsigned)db + nb * 1024;
#pragma unroll
        for (int e = 0; e < 4; ++e) {
          const unsigned sl = (unsigned)((c ^ ((4 * nb + e) & 7)) << 4);
#pragma unroll
          for (int hi = 0; hi < 2; ++hi) {
            const unsigned sel = hi ? 0x07060302u : 0x05040100u;
            u32x4 o;
#pragma unroll
            for (int q = 0; q < 4; ++q) o[q] = __builtin_amdgcn_perm(r[2 * q + 1][e], r[2 * q][e], sel);
            st16_wt(ws, lo + (2 * e + hi) * 128 + sl, o);
          }
        }
      }
    }
    WAITV(0);
    if (lane == 0) flag_add(ws, F_W);
    __builtin_amdgcn_s_setprio(0);
  } else {

  if (w >= 4) __builtin_amdgcn_s_setprio(1);
  const int r32 = lane & 31, hh = lane >> 5;
  lds_cptr L = (lds_cptr)lds;
  const bf16x8 qf0 = LDS_RD128(L + KV_HEAD + w * Q_TILE + lane * 16);
  u32x4 q1v = {0, 0, 0, 0}; { unsigned t = LDS_RD32(L + KV_HEAD + w * Q_TILE + 1024 + r32 * 4); q1v[0] = hh ? 0u : t; }
  const bf16x8 qf1 = __builtin_bit_cast(bf16x8, q1v);
  lds_cptr kp0 = L + lane * 16, kp1 = L + (hh ? KV_Z : KV_K1 + r32 * 4), vp = L + (r32 <= 18 ? KV_V + hh * 304 + r32 * 16 : KV_Z);
  f32x16 negm;
  {
    bf16x8 k0 = LDS_RD128(kp0); u32x4 k1 = {LDS_RD32(kp1), 0, 0, 0};
    f32x16 s = {}; s = MFMA32(k0, qf0, s); s = MFMA32(__builtin_bit_cast(bf16x8, k1), qf1, s);
    float m = s[0];
#pragma unroll
    for (int i = 1; i < 16; ++i) m = fmaxf(m, s[i]);
    auto rr = __builtin_amdgcn_permlane32_swap(__float_as_uint(m), __float_as_uint(m), false, false);
    m = fmaxf(__uint_as_float(rr[0]), __uint_as_float(rr[1]));
#pragma unroll
    for (int i = 0; i < 16; ++i) negm[i] = -m;
  }
  f32x16 O = {};
#define EX2(Sx, i) pk2(__builtin_amdgcn_exp2f(Sx[i]), __builtin_amdgcn_exp2f(Sx[(i) + 1]))
  bf16x8 k0, v0, v1; u32x4 k1 = {0, 0, 0, 0};
  f32x16 SA, SBc; u32x4 pA0, pA1, pB0, pB1;
#define STEP(Sc, Sn, Pc0, Pc1, Pp0, Pp1, koff, voff) do { \
    SB(); Sn = MFMA32(k0, qf0, negm); SB(); \
    Pc0[0] = EX2(Sc, 0); Pc0[1] = EX2(Sc, 2); PIN(Pc0); SB(); \
    Sn = MFMA32(__builtin_bit_cast(bf16x8, k1), qf1, Sn); SB(); \
    k0 = LDS_RD128(kp0 + (koff)); k1[0] = LDS_RD32(kp1 + (koff)); SB(); \
    Pc0[2] = EX2(Sc, 4); Pc0[3] = EX2(Sc, 6); PIN(Pc0); SB(); \
    O = MFMA32(v0, __builtin_bit_cast(bf16x8, Pp0), O); SB(); \
    Pc1[0] = EX2(Sc, 8); Pc1[1] = EX2(Sc, 10); PIN(Pc1); SB(); \
    O = MFMA32(v1, __builtin_bit_cast(bf16x8, Pp1), O); SB(); \
    v0 = LDS_RD128(vp + (voff)); v1 = LDS_RD128(vp + (voff) + KV_VS); SB(); \
    Pc1[2] = EX2(Sc, 12); Pc1[3] = EX2(Sc, 14); PIN(Pc1); SB(); \
  } while (0)
  k0 = LDS_RD128(kp0); k1[0] = LDS_RD32(kp1);
  SA = MFMA32(k0, qf0, negm); SA = MFMA32(__builtin_bit_cast(bf16x8, k1), qf1, SA);
  k0 = LDS_RD128(kp0 + KV_TILE); k1[0] = LDS_RD32(kp1 + KV_TILE);
  v0 = LDS_RD128(vp); v1 = LDS_RD128(vp + KV_VS);
  pB0 = u32x4{0, 0, 0, 0}; pB1 = u32x4{0, 0, 0, 0};
  int off = 0;
  for (int t = 0; t < 8; t += 2) {
    STEP(SA, SBc, pA0, pA1, pB0, pB1, off + 2 * KV_TILE, off);
    STEP(SBc, SA, pB0, pB1, pA0, pA1, off + 3 * KV_TILE, off + KV_TILE);
    off += 2 * KV_TILE;
  }
  BAR();
  for (int t = 8; t < 62; t += 2) {
    if (t == SWITCH) __builtin_amdgcn_s_setprio(0);
    STEP(SA, SBc, pA0, pA1, pB0, pB1, off + 2 * KV_TILE, off);
    STEP(SBc, SA, pB0, pB1, pA0, pA1, off + 3 * KV_TILE, off + KV_TILE);
    off += 2 * KV_TILE;
  }
  STEP(SA, SBc, pA0, pA1, pB0, pB1, off + KV_TILE, off);
  STEP(SBc, SA, pB0, pB1, pA0, pA1, off + KV_TILE, off + KV_TILE);
  O = MFMA32(v0, __builtin_bit_cast(bf16x8, pB0), O); O = MFMA32(v1, __builtin_bit_cast(bf16x8, pB1), O);

  {
    float mx = 0.f;
#pragma unroll
    for (int i = 0; i < 11; ++i) mx = fmaxf(mx, fabsf(O[i]));
    bool bad = !(mx < 1.2676506e30f);
#pragma unroll
    for (int i = 0; i < 11; ++i) bad |= (O[i] != O[i]);
    if (__any(bad)) {
      float m = -3.0e38f; O = f32x16{};
#pragma unroll 1
      for (int t = 0; t < 64; ++t) {
        const int of = t * KV_TILE;
        const bf16x8 a0 = LDS_RD128(kp0 + of); const u32x4 a1 = {LDS_RD32(kp1 + of), 0, 0, 0};
        f32x16 s = {}; s = MFMA32(a0, qf0, s); s = MFMA32(__builtin_bit_cast(bf16x8, a1), qf1, s);
        float tm = s[0];
#pragma unroll
        for (int i = 1; i < 16; ++i) tm = fmaxf(tm, s[i]);
        auto rr = __builtin_amdgcn_permlane32_swap(__float_as_uint(tm), __float_as_uint(tm), false, false);
        tm = fmaxf(__uint_as_float(rr[0]), __uint_as_float(rr[1]));
        const float mn = fmaxf(m, tm), al = __builtin_amdgcn_exp2f(m - mn); m = mn;
#pragma unroll
        for (int i = 0; i < 16; ++i) { O[i] *= al; s[i] -= mn; }
        u32x4 p0, p1;
#pragma unroll
        for (int i = 0; i < 4; ++i) { p0[i] = EX2(s, 2 * i); p1[i] = EX2(s, 8 + 2 * i); }
        O = MFMA32(LDS_RD128(vp + of), __builtin_bit_cast(bf16x8, p0), O); O = MFMA32(LDS_RD128(vp + of + KV_VS), __builtin_bit_cast(bf16x8, p1), O);
      }
    }
  }
  {
    float l = O[10];
    { auto rr = __builtin_amdgcn_permlane32_swap(__float_as_uint(l), __float_as_uint(l), false, false); l = __uint_as_float(rr[0]); }
    const float inv = 1.f / l;
    const size_t ob = WS_O + (size_t)(qb * 256 + w * 32 + r32) * 1536 + h * 48;
    const u32x4 c0 = {pk2(O[0] * inv, O[1] * inv), pk2(O[2] * inv, O[3] * inv), pk2(O[4] * inv, O[5] * inv), pk2(O[6] * inv, O[7] * inv)};
    st16_wt(ws, ob + hh * 16, c0);
    if (hh == 0) st16_wt(ws, ob + 32, u32x4{pk2(O[8] * inv, O[9] * inv), 0, 0, 0});
  }

  }
  WAITV(0); __syncthreads();
  if (tid == 0) flag_add(ws, F_O + qb);

}

constexpr int G_STG = 160 * 128, G_W = 8192, G_NS = 8, G_LDS = G_NS * G_STG;
template <int K, bool STATS, int KID>
DI void g_body(char* ws, char* lds, const u16* __restrict__ A, const char* __restrict__ W, const u16* __restrict__ res, u16* __restrict__ out, float* __restrict__ st2, int b) {
  constexpr int NT = K / 64;
  const int lane = lane_id(); const int w = __builtin_amdgcn_readfirstlane(threadIdx.x >> 6);
  const int xc = b & 7, idx = b >> 3, rt = xc * 4 + (idx & 3), ct = idx >> 2;
  const int m0 = rt * 64, n0 = ct * 96;

  const char* Xb = (const char*)(A + (size_t)m0 * K); const char* Wb = W + (size_t)ct * NT * 12288;
  const int lw = w - 4;
  const char* xs = Xb + tn_off(lw * 8 + (lane >> 3), lane, K * 2); const char* wsrc = Wb + lw * 1024 + lane * 16;
  auto issW = [&](int kt, int stage) { glds16a<16>(wsrc + (long)kt * 12288, lds + stage * G_STG + (8 + lw) * 1024); if (lw < 4) glds16a<16>(wsrc + (long)kt * 12288 + 8192, lds + stage * G_STG + (16 + lw) * 1024); };
  auto issX = [&](int kt, int stage) { glds16a<16>(xs + (long)kt * 128, lds + stage * G_STG + lw * 1024); };
  if (STATS) { if (w == 11) flag_wait(ws, F_W, 1024); __syncthreads(); }
  if (w >= 4) {
#pragma unroll
    for (int t = 0; t < 7; ++t) issW(t, t);
  }
  if (w == 11) { if (STATS) flag_wait(ws, F_O + (rt >> 2), 32); else flag_wait(ws, F_U + (rt >> 1), 16); }
  __syncthreads();
  if (w >= 4) {
    __builtin_amdgcn_s_setprio(2);
#pragma unroll
    for (int t = 0; t < 7; ++t) issX(t, t);
    WAITV(6); BAR();
#pragma unroll
    for (int kt = 0; kt < 6; ++kt) {
      constexpr int dummy = 0; (void)dummy;
      const int imax = kt + 6 < NT - 1 ? kt + 6 : NT - 1;
      switch ((5 - kt) + 2 * (imax - 6)) { case 5: WAITV(5); break; case 6: WAITV(6); break; case 7: WAITV(7); break; case 8: WAITV(8); break; case 9: WAITV(9); break; default: WAITV(10); break; }
      BAR();
      if (kt + 7 < NT) { issX(kt + 7, (kt + 7) & 7); issW(kt + 7, (kt + 7) & 7); }
    }
#pragma unroll 1
    for (int kt = 6; kt + 7 < NT; ++kt) { WAITV(10); BAR(); issX(kt + 7, (kt + 7) & 7); issW(kt + 7, (kt + 7) & 7); }
    {
      constexpr int K0 = NT - 7 > 6 ? NT - 7 : 6;
#pragma unroll
      for (int kt = K0; kt < NT; ++kt) {
        const int later = 2 * (NT - 2 - kt);
        if (later >= 10) WAITV(10); else if (later >= 8) WAITV(8); else if (later >= 6) WAITV(6); else if (later >= 4) WAITV(4); else if (later >= 2) WAITV(2); else WAITV(0);
        BAR();
      }
    }
    __builtin_amdgcn_s_setprio(0);
  } else {
  const int wr = w & 1, wc = w >> 1, fr = lane & 15, fq = lane >> 4, sw = (fr >> 1) & 7;
  lds_cptr L = (lds_cptr)lds;
  lds_cptr x0 = L + (32 * wr + fr) * 128 + ((fq ^ sw) << 4), x1 = L + (32 * wr + fr) * 128 + (((4 + fq) ^ sw) << 4);
  lds_cptr w0 = L + G_W + (48 * wc + fr) * 128 + ((fq ^ sw) << 4), w1 = L + G_W + (48 * wc + fr) * 128 + (((4 + fq) ^ sw) << 4);
  f32x4 acc[3][2] = {};
  bf16x8 xa[2], wa[3], xb[2], wb_[3];
#define GRD(X, Wf, st, ks) do { X[0] = LDS_RD128(((ks) ? x1 : x0) + (st)); X[1] = LDS_RD128(((ks) ? x1 : x0) + (st) + 2048); \
    _Pragma("unroll") for (int n = 0; n < 3; ++n) Wf[n] = LDS_RD128(((ks) ? w1 : w0) + (st) + n * 2048); } while (0)
#define GSCHED() do { _Pragma("unroll") for (int i_ = 0; i_ < 5; ++i_) { SGB(0x8, 1); SGB(0x100, 1); } SGB(0x8, 1); } while (0)
#define GMM(X, Wf) do { _Pragma("unroll") for (int n = 0; n < 3; ++n) { acc[n][0] = MFMA16(Wf[n], X[0], acc[n][0]); acc[n][1] = MFMA16(Wf[n], X[1], acc[n][1]); } } while (0)
  BAR();

  GRD(xa, wa, 0, 0);
  u32x2 rv[2][3];
#pragma unroll
  for (int m = 0; m < 2; ++m)
#pragma unroll
    for (int n = 0; n < 3; ++n) rv[m][n] = ld8_c(res + (size_t)(m0 + 32 * wr + 16 * m + fr) * D + n0 + 48 * wc + 16 * n + 4 * fq);
  int st = 0;
#pragma unroll 1
  for (int kt = 0; kt < NT; ++kt) {
    const int stn = st + G_STG == G_LDS ? 0 : st + G_STG;
    GRD(xb, wb_, st, 1);
    GMM(xa, wa);
    GSCHED();
    SB(); BAR(); SB();
    GRD(xa, wa, stn, 0);
    GMM(xb, wb_);
    GSCHED();
    SB();
    st = stn;
  }

#pragma unroll
  for (int m = 0; m < 2; ++m) {
    const int row = m0 + 32 * wr + 16 * m + fr;
    float sum = 0.f, ssq = 0.f;
#pragma unroll
    for (int n = 0; n < 3; ++n) {
      const int col = n0 + 48 * wc + 16 * n + 4 * fq;
      const u32x2 r = rv[m][n];
      u32x2 o; o[0] = pk2(bflo(r[0]) + acc[n][m][0], bfhi(r[0]) + acc[n][m][1]); o[1] = pk2(bflo(r[1]) + acc[n][m][2], bfhi(r[1]) + acc[n][m][3]);
      if (STATS) st8_wt(out + (size_t)row * D + col, o); else *(u32x2*)(out + (size_t)row * D + col) = o;
      if (STATS) { const float a = bflo(o[0]), bq = bfhi(o[0]), c = bflo(o[1]), d = bfhi(o[1]); sum += (a + bq) + (c + d); ssq += (a * a + bq * bq) + (c * c + d * d); }
    }
    if (STATS) {
      sum += __shfl_xor(sum, 16); sum += __shfl_xor(sum, 32); ssq += __shfl_xor(ssq, 16); ssq += __shfl_xor(ssq, 32);
      if (fq == 0) st8_wt(st2 + ((size_t)row * 12 + ct * 2 + wc) * 2, u32x2{__float_as_uint(sum), __float_as_uint(ssq)});
    }
  }

  }
  if (STATS) { WAITV(0); __syncthreads(); if (threadIdx.x == 0) flag_add(ws, F_H + (rt >> 1)); }

}
DI void p3_colsum(const u16* __restrict__ w_in, char* __restrict__ ws, char* lds, int b) {
    const int tid = threadIdx.x, c = tid % 9, rg = tid / 9;
    float* red = (float*)lds;
    if (rg < 48) {
      const char* p = (const char*)(w_in + (b - 192) * 72 + c * 8) + (size_t)rg * (F * 2);
      u32x4 v[12];
#pragma unroll
      for (int i = 0; i < 12; ++i) v[i] = *(const u32x4*)(p + (size_t)i * 48 * F * 2);
      float a[8] = {};
#pragma unroll
      for (int i = 0; i < 12; ++i)
#pragma unroll
        for (int e = 0; e < 4; ++e) { a[2 * e] += bflo(v[i][e]); a[2 * e + 1] += bfhi(v[i][e]); }
#pragma unroll
      for (int e = 0; e < 8; ++e) red[rg * 72 + c * 8 + e] = a[e];
    }
    __syncthreads();
    if (tid < 72) { float t = 0.f; for (int g = 0; g < 48; ++g) t += red[g * 72 + tid]; st4_wt((float*)(ws + WS_CS) + (b - 192) * 72 + tid, __float_as_uint(t)); }
    WAITV(0); __syncthreads();
    if (tid == 0) flag_add(ws, F_CS);
}
constexpr unsigned XTAG = 0x9E3779B9u;
DI float dpp_sum16(float v) {
  v += __builtin_bit_cast(float, __builtin_amdgcn_mov_dpp(__builtin_bit_cast(int, v), 0xB1, 0xf, 0xf, true));
  v += __builtin_bit_cast(float, __builtin_amdgcn_mov_dpp(__builtin_bit_cast(int, v), 0x4E, 0xf, 0xf, true));
  v += __builtin_bit_cast(float, __builtin_amdgcn_mov_dpp(__builtin_bit_cast(int, v), 0x141, 0xf, 0xf, true));
  v += __builtin_bit_cast(float, __builtin_amdgcn_mov_dpp(__builtin_bit_cast(int, v), 0x140, 0xf, 0xf, true));
  return v;
}
constexpr int K4_STG = (128 + 288) * 128, K4_W2 = 16384, K4_W1 = 16384 + 18432, K4_NS = 3;
constexpr int K4_TAB = K4_NS * K4_STG;
constexpr int K4_PART = 40960, K4_ST2 = 45056;
constexpr int K4_LDS = K4_TAB + 544 * 4;
template <int NTL>
DI void k4_compute(char* lds, int lane, int w, int pi, int ft, char* ws, unsigned (&z1v)[4 * NTL]) {
  const int wr = w & 3, nb0 = NTL == 5 ? 0 : 5, wc = NTL == 5 ? 0 : 1, fr = lane & 15, fq = lane >> 4, sw = (fr >> 1) & 7;
  lds_cptr L = (lds_cptr)lds;
  lds_cptr x0 = L + (32 * wr + fr) * 128 + ((fq ^ sw) << 4), x1 = L + (32 * wr + fr) * 128 + (((4 + fq) ^ sw) << 4);
  lds_cptr w0 = L + K4_W2 + (16 * nb0 + fr) * 128 + ((fq ^ sw) << 4), w1 = L + K4_W2 + (16 * nb0 + fr) * 128 + (((4 + fq) ^ sw) << 4);
  f32x4 acc2[NTL][2] = {}, acc1[NTL][2] = {};
  bf16x8 xa[2], xb[2], wa[NTL], wb[NTL];
#define RDX(X, P, st) do { X[0] = LDS_RD128(P + (st)); X[1] = LDS_RD128(P + (st) + 2048); } while (0)
#define RDW(Wf, P, st) do { _Pragma("unroll") for (int n = 0; n < NTL; ++n) Wf[n] = LDS_RD128(P + (st) + n * 2048); } while (0)
#define MM2(X, Wf) do { _Pragma("unroll") for (int n = 0; n < NTL; ++n) { acc2[n][0] = MFMA16(X[0], Wf[n], acc2[n][0]); acc2[n][1] = MFMA16(X[1], Wf[n], acc2[n][1]); } } while (0)
#define MM1(X, Wf) do { _Pragma("unroll") for (int n = 0; n < NTL; ++n) { acc1[n][0] = MFMA16(Wf[n], X[0], acc1[n][0]); acc1[n][1] = MFMA16(Wf[n], X[1], acc1[n][1]); } } while (0)
#define SCH(NR) do { _Pragma("unroll") for (int i_ = 0; i_ < (NR); ++i_) { SGB(0x8, 1); SGB(0x100, 1); } SGB(0x8, 2 * NTL - (NR)); SB(); } while (0)
  WAITV(0); WAITL0();
  if (NTL == 4) BAR();
  int st = 0;
#pragma unroll 1
  for (int kt = 0; kt < 9; ++kt) {
    BAR(); SB();

    RDX(xa, x0, st); RDW(wa, w0, st); SB();
    RDW(wb, w0, st + 18432); MM2(xa, wa); SCH(NTL);
    RDX(xb, x1, st); RDW(wa, w1, st); MM1(xa, wb); SCH(NTL + 2);
    BAR(); SB();
    RDW(wb, w1, st + 18432); MM2(xb, wa); SCH(NTL);
    MM1(xb, wb); SB();
    st = st + K4_STG == K4_TAB ? 0 : st + K4_STG;
  }
  if (NTL == 5) BAR();

  int lz = lane_id();
  const float* tab = (const float*)(lds + K4_TAB); const float* rs = tab; const float* mr = tab + 128; const float* cs = tab + 256;
  float* part = (float*)(lds + K4_PART); const float* st2 = (const float*)(lds + K4_ST2);
  u32x2 z2p[NTL][2];
  {
    const int fr = lz & 15, fq = lz >> 4;
#pragma unroll
    for (int m = 0; m < 2; ++m) {
      const f32x4 r4 = *(const f32x4*)(rs + 32 * wr + 16 * m + 4 * fq), m4 = *(const f32x4*)(mr + 32 * wr + 16 * m + 4 * fq);
      f32x4 su = {0, 0, 0, 0}, sq = {0, 0, 0, 0};
#pragma unroll
      for (int n = 0; n < NTL; ++n) {
        const float c = cs[16 * (nb0 + n) + fr];
        f32x4 g;
#pragma unroll
        for (int r = 0; r < 4; ++r) { g[r] = gelu_t(acc2[n][m][r] * r4[r] - m4[r] * c); su[r] += g[r]; sq[r] = __builtin_fmaf(g[r], g[r], sq[r]); }
        z2p[n][m][0] = pk2(g[0], g[1]); z2p[n][m][1] = pk2(g[2], g[3]); PIN(z2p[n][m]);
      }
#pragma unroll
      for (int r = 0; r < 4; ++r) { su[r] = dpp_sum16(su[r]); sq[r] = dpp_sum16(sq[r]); }
      if (fr == 0) {
#pragma unroll
        for (int r = 0; r < 4; ++r) *(f32x2_t*)(part + (wc * 128 + 32 * wr + 16 * m + 4 * fq + r) * 2) = f32x2_t{su[r], sq[r]};
      }
    }
  }
  WAITL0(); BAR();
  if (wc == 0 && lz < 32) {
    const int row = 32 * wr + lz; const f32x2_t p0 = *(const f32x2_t*)(part + row * 2), p1 = *(const f32x2_t*)(part + (128 + row) * 2);
    const u32x4 g = {__float_as_uint(p0[0] + p1[0]), XTAG, __float_as_uint(p0[1] + p1[1]), XTAG};
    st16_wt(ws, WS_XS + (size_t)(((pi * 16 + ft) * 128 + row) * 16), g);
  }

  {
    const int fr = lz & 15, fq = lz >> 4;
    const float r0 = rs[32 * wr + fr], mm0 = mr[32 * wr + fr], r1 = rs[32 * wr + 16 + fr], mm1 = mr[32 * wr + 16 + fr];
#pragma unroll
    for (int n = 0; n < NTL; ++n) {
      const f32x4 c4 = *(const f32x4*)(cs + 144 + 16 * (nb0 + n) + 4 * fq);
      float g0[4], g1[4];
#pragma unroll
      for (int r = 0; r < 4; ++r) { g0[r] = gelu_t(acc1[n][0][r] * r0 - mm0 * c4[r]); g1[r] = gelu_t(acc1[n][1][r] * r1 - mm1 * c4[r]); }
      z1v[4 * n] = pk2(g0[0], g0[1]); z1v[4 * n + 1] = pk2(g0[2], g0[3]); z1v[4 * n + 2] = pk2(g1[0], g1[1]); z1v[4 * n + 3] = pk2(g1[2], g1[3]);
      PIN(z1v[4 * n]); PIN(z1v[4 * n + 1]); PIN(z1v[4 * n + 2]); PIN(z1v[4 * n + 3]);
    }
  }

  __syncthreads();
  asm volatile("" : "+v"(lz));
#pragma unroll
  for (int m = 0; m < 2; ++m) {
    const int fr = lz & 15, fq = lz >> 4;
    const int sl = 32 * wr + 16 * m + 4 * fq;
    const f32x4 sa = *(const f32x4*)(st2 + sl * 2), sb = *(const f32x4*)(st2 + sl * 2 + 4);
    const float mu[4] = {sa[0], sa[2], sb[0], sb[2]}, rr[4] = {sa[1], sa[3], sb[1], sb[3]};
#pragma unroll
    for (int n = 0; n < NTL; ++n) {
      const int f = 16 * (nb0 + n) + fr; const u32x2 p = z2p[n][m];
      u32x2 o; o[0] = pk2(bflo(p[0]) * rr[0] - mu[0], bfhi(p[0]) * rr[1] - mu[1]); o[1] = pk2(bflo(p[1]) * rr[2] - mu[2], bfhi(p[1]) * rr[3] - mu[3]);
      *(u32x2*)(lds + (sl >> 6) * 18432 + f * 128 + ((((sl & 63) >> 3) ^ ((f >> 1) & 7)) << 4) + (sl & 4) * 2) = o;
    }
  }
}
template <int NTL>
DI void k5_compute(char* lds, int lane, int w, int tt, int ft, const u16* __restrict__ bsp, char* ws, const unsigned (&z1v)[4 * NTL]) {
  const int wr = w & 3, nb0 = NTL == 5 ? 0 : 5, fr = lane & 15, fq = lane >> 4;
  const C8Addr a = c8_addr<false>(lds, lane, wr, nb0);
  f32x4 acc[NTL][2] = {};
  C8Frag<NTL> f0, f1;
  WAITV(0); BAR();

  c8_read<NTL, false>(f0, a, 0, 0);
  float bb[2];
#pragma unroll
  for (int m = 0; m < 2; ++m) bb[m] = bf2f(bsp[tt * 128 + 32 * wr + 16 * m + fr]);
  int st = 0;
#define C5_ITER(SIDE0, SIDE1) do { const int stn = st + C8_STG == C8_LDS ? 0 : st + C8_STG; \
    c8_read<NTL, false>(f1, a, st, 1); c8_mma<NTL, true>(acc, f0); c8_sched<NTL, false>(); SB(); SIDE0; SB(); BAR(); SB(); \
    c8_read<NTL, false>(f0, a, stn, 0); c8_mma<NTL, true>(acc, f1); c8_sched<NTL, false>(); SB(); SIDE1; SB(); st = stn; } while (0)
#pragma unroll 1
  for (int kt = 0; kt < 32; ++kt) C5_ITER((void)0, (void)0);

  __syncthreads();
#pragma unroll
  for (int m = 0; m < 2; ++m) {
    const int t = 32 * wr + 16 * m + fr;
#pragma unroll
    for (int n = 0; n < NTL; ++n) {
      const int f = 16 * (nb0 + n) + 4 * fq; const unsigned z0 = z1v[4 * n + 2 * m], z1 = z1v[4 * n + 2 * m + 1];
      u32x2 o; o[0] = pk2(bflo(z0) * (acc[n][m][0] + bb[m]), bfhi(z0) * (acc[n][m][1] + bb[m]));
      o[1] = pk2(bflo(z1) * (acc[n][m][2] + bb[m]), bfhi(z1) * (acc[n][m][3] + bb[m]));
      *(u32x2*)(lds + t * EP_PITCH + f * 2) = o;
    }
  }
}

DI void p4_copyout(char* ws, char* lds, int w, int pi, int ft) {
  __syncthreads();
  const size_t dst = WS_Z2T + ((size_t)ft * 32 + pi * 2) * 18432;
  for (int i = w * 64 + lane_id(); i < 2304; i += 768) st16_wt(ws, dst + i * 16, *(const u32x4*)(lds + i * 16));
  WAITV(0); __syncthreads();
  if (threadIdx.x == 0) flag_add(ws, F_Z + ft);

}
DI void p5_copyout(char* ws, char* lds, int w, int tt, int ft) {
  __syncthreads();
  const size_t ub = WS_U + ((size_t)tt * 128 * FH + ft * 144) * 2;
  for (int i = w * 64 + lane_id(); i < 128 * 18; i += 768) { const int t = i / 18, c = i - t * 18; st16_wt(ws, ub + (size_t)t * FH * 2 + c * 16, *(const u32x4*)(lds + t * EP_PITCH + c * 16)); }
  WAITV(0); __syncthreads();
  if (threadIdx.x == 0) flag_add(ws, F_U + tt);

}
DI void p45(const u16* __restrict__ wsp, const u16* __restrict__ bsp, char* __restrict__ ws, char* lds) {
  const int lane = lane_id(); const int w = __builtin_amdgcn_readfirstlane(threadIdx.x >> 6); const int tid = w * 64 + lane;
  const int b = blockIdx.x, xc = b & 7, idx = b >> 3;
  const int pi = (xc & 1) * 8 + (idx & 7), ft = (xc >> 1) * 4 + (idx >> 3);
  float* tab = (float*)(lds + K4_TAB);

  const char* Xb = ws + WS_H + (size_t)pi * 128 * D * 2; const char* W2b = ws + WS_WIN + (size_t)ft * 9 * 18432; const char* W1b = W2b + (size_t)16 * 9 * 18432;
  auto src = [&](int p) { return p < 16 ? Xb + tn_off(p * 8 + (lane >> 3), lane, D * 2) : (p < 34 ? W2b + (p - 16) * 1024 : W1b + (p - 34) * 1024) + lane * 16; };
  if (b >= 192) { if (w == 11) flag_wait(ws, F_W, 1024); __syncthreads(); }
#pragma unroll
  for (int j = 0; j < 3; ++j) glds16a<16>(src(16 + w + 12 * j), lds + (16 + w + 12 * j) * 1024);
  if (w == 11) { flag_wait(ws, F_H + pi, 12); flag_wait(ws, F_CS, 64); }
  __syncthreads();
  glds16a<16>(src(w), lds + w * 1024);
  if (w < 4) glds16a<16>(src(12 + w), lds + (12 + w) * 1024);
  const char* Xb5 = (const char*)(wsp + (size_t)pi * 128 * S); const char* Wb5 = ws + WS_Z2T + (size_t)ft * 32 * 18432;
  auto src5 = [&](int p) { return p < 16 ? Xb5 + tn_off(p * 8 + (lane >> 3), lane, S * 2) : Wb5 + (p - 16) * 1024 + lane * 16; };
  if (w >= 8) {
    const int lw = w - 8;
    __builtin_amdgcn_s_setprio(2);
    {
      const char* base[13];
#pragma unroll
      for (int j = 0; j < 13; ++j) base[j] = src(lw + 4 * j);
      auto issue = [&](int kt, int stage) {
#pragma unroll
        for (int j = 0; j < 13; ++j) glds16a<16>(base[j] + (j < 4 ? (long)kt * 128 : (long)kt * 18432), lds + stage * K4_STG + (lw + 4 * j) * 1024);
      };
      issue(1, 1);
      int stg = 2;
#pragma unroll 1
      for (int kt = 0; kt < 7; ++kt) { WAITV(13); BAR(); BAR(); issue(kt + 2, stg); stg = stg == 2 ? 0 : stg + 1; }
      WAITV(13); BAR(); BAR();
      WAITV(0); BAR(); BAR();
      BAR();
      BAR();
      const int row = 32 * lw + (lane >> 1);
      const __amdgpu_buffer_rsrc_t rs_ = __builtin_amdgcn_make_buffer_rsrc((void*)(ws + WS_XS), 0, 2048 * 16 * 16, 0x00020000);
      const unsigned vo = (unsigned)(((pi * 16 + (lane & 1) * 8) * 128 + row) * 16);
      float sum = 0.f, ssq = 0.f;
      for (int spin = 0; spin < (1 << 20); ++spin) {
        bool ok = true; sum = 0.f; ssq = 0.f;
        u32x4 g[8];
#pragma unroll
        for (int t = 0; t < 8; ++t) g[t] = __builtin_amdgcn_raw_buffer_load_b128(rs_, vo + t * 2048, 0, 16);
#pragma unroll
        for (int t = 0; t < 8; ++t) { ok &= g[t][1] == XTAG && g[t][3] == XTAG; sum += __uint_as_float(g[t][0]); ssq += __uint_as_float(g[t][2]); }
        if (__all(ok)) break;
        __builtin_amdgcn_s_sleep(8);
      }
      sum += __shfl_xor(sum, 1); ssq += __shfl_xor(ssq, 1);
      const float mean = sum * (1.f / FH), var = fmaxf(ssq * (1.f / FH) - mean * mean, 0.f), rstd = rsqrtf(var + 1e-5f);
      if ((lane & 1) == 0) *(f32x2_t*)(lds + K4_ST2 + row * 8) = f32x2_t{mean * rstd, rstd};
      __syncthreads();
    }
    p4_copyout(ws, lds, w, pi, ft);

    glds16(src5(w), lds + w * 1024); if (w < 4) glds16(src5(12 + w), lds + (12 + w) * 1024);
    if (w == 11) flag_wait(ws, F_Z + ft, 16);
    __syncthreads();
    glds16a<16>(src5(16 + w), lds + (16 + w) * 1024); if (w < 6) glds16a<16>(src5(28 + w), lds + (28 + w) * 1024);
    loader_loop<32, C8_NS, C8_STG, 34, 16, 4, 0, 16>(lds, lw, src5, [&](int kt, long& ox, long& ow) { ox = (long)kt * 128; ow = (long)kt * 18432; }, NoPost());
    __builtin_amdgcn_s_setprio(0);
    __syncthreads();
    p5_copyout(ws, lds, w, pi, ft);
  } else {
    if (tid < 128) {
      const char* p = ws + WS_ST2 + (size_t)(pi * 128 + tid) * 96;
      float sum = 0.f, ssq = 0.f;
#pragma unroll
      for (int t = 0; t < 12; ++t) { const u32x2 v = ld8_c(p + t * 8); sum += __uint_as_float(v[0]); ssq += __uint_as_float(v[1]); }
      const float mean = sum * (1.f / D), var = fmaxf(ssq * (1.f / D) - mean * mean, 0.f), rstd = rsqrtf(var + 1e-5f);
      tab[tid] = rstd; tab[128 + tid] = mean * rstd;
    } else if (tid < 416) {
      const int c = tid - 128;
      tab[256 + c] = ld4f_c((const float*)(ws + WS_CS) + (c < 144 ? FH + ft * 144 + c : ft * 144 + c - 144));
    }
#define P45_PATH(NTL) do { unsigned z1v[4 * NTL]; \
      k4_compute<NTL>(lds, lane, w, pi, ft, ws, z1v); \
      p4_copyout(ws, lds, w, pi, ft); \
 \
      glds16(src5(w), lds + w * 1024); if (w < 4) glds16(src5(12 + w), lds + (12 + w) * 1024); \
      __syncthreads(); \
      glds16a<16>(src5(16 + w), lds + (16 + w) * 1024); if (w < 6) glds16a<16>(src5(28 + w), lds + (28 + w) * 1024); \
      k5_compute<NTL>(lds, lane_id(), w, pi, ft, bsp, ws, z1v); \
      p5_copyout(ws, lds, w, pi, ft); } while (0)
    if (w < 4) P45_PATH(5); else P45_PATH(4);
  }
}

__global__ __launch_bounds__(768, 3) void mega_kernel(const u16* __restrict__ x, const u16* __restrict__ wo, const u16* __restrict__ w_in, const u16* __restrict__ w_sp,
                                                      const u16* __restrict__ b_sp, const u16* __restrict__ w_out, u16* __restrict__ out, char* __restrict__ ws) {
  extern __shared__ __attribute__((aligned(16))) char lds[];
  const int b = blockIdx.x;
  p2_attention(wo, w_in, w_out, w_sp, ws, lds);
  if (b < 192) g_body<768, true, 3>(ws, lds, (const u16*)(ws + WS_O), ws + WS_WO, x, (u16*)(ws + WS_H), (float*)(ws + WS_ST2), b);
  else p3_colsum(w_in, ws, lds, b);
  __syncthreads();
  p45(w_sp, b_sp, ws, lds);
  if (b < 192) g_body<FH, false, 0>(ws, lds, (const u16*)(ws + WS_U), ws + WS_WOUT, (const u16*)(ws + WS_H), out, nullptr, b);
}

extern "C" void kernel_launch(void* const* d_in, const int* in_sizes, int n_in, void* d_out, int out_size, void* d_ws, size_t ws_size, hipStream_t stream) {
  const u16* x = (const u16*)d_in[0]; const u16* wqkv = (const u16*)d_in[1]; const u16* wo = (const u16*)d_in[2];
  const u16* w_in = (const u16*)d_in[4]; const u16* w_sp = (const u16*)d_in[5]; const u16* b_sp = (const u16*)d_in[6]; const u16* w_out = (const u16*)d_in[7];
  u16* out = (u16*)d_out; char* ws = (char*)d_ws;
  static bool attr = false;
  if (!attr) {
    (void)hipFuncSetAttribute((const void*)k1_kernel, hipFuncAttributeMaxDynamicSharedMemorySize, K1_LDS);
    (void)hipFuncSetAttribute((const void*)mega_kernel, hipFuncAttributeMaxDynamicSharedMemorySize, 163840);
    attr = true;
  }
  k1_kernel<<<256, K1_THREADS, K1_LDS, stream>>>(x, wqkv, ws);
  mega_kernel<<<256, 768, 163840, stream>>>(x, wo, w_in, w_sp, b_sp, w_out, out, ws);
}
```
